```python
import torch

def _softmax(x, axis=-1):
    return torch.softmax(x, dim=axis)
def _gelu(x):
    return torch.nn.functional.gelu(x, approximate="tanh")
def _silu(x):
    return torch.nn.functional.silu(x)
def _sigmoid(x):
    return torch.sigmoid(x)
def _layernorm(x, axis=-1, eps=1e-5):
    m = x.mean(dim=axis, keepdim=True)
    v = x.var(dim=axis, keepdim=True, unbiased=False)
    return (x - m) / torch.sqrt(v + eps)
def _scatter_add(tgt, idx, src, axis=0):
    return tgt.clone().index_add_(axis, idx, src)
def _segment_sum(src, seg, n):
    out = torch.zeros((n,) + src.shape[1:], dtype=src.dtype)
    return out.index_add_(0, seg, src)
def _pad(x, pw):
    flat = tuple(v for pair in reversed(pw) for v in pair)
    return torch.nn.functional.pad(x, flat)

kernel_name = 'sequential_preLN__mha_block__glu_gated_reglu__prompt'

def setup_inputs():

    _w = (lambda a: (lambda t: t.to(torch.bfloat16) if t.is_floating_point() else t)(torch.as_tensor(a)))
    def _A_setup():
        B, S, D, H = 1, 2048, 640, 32
        return {
            "x": _w((lambda *s: torch.randn(*s, dtype=torch.bfloat16) * 0.05)(B, S, D)),
            "wqkv": _w((lambda *s: torch.randn(*s, dtype=torch.bfloat16) * 0.05)(D, 3 * D)),
            "wo": _w((lambda *s: torch.randn(*s, dtype=torch.bfloat16) * 0.05)(D, D)),
        }
    _A_in = _A_setup()

    def _B_setup():
        B, S, D, F = 1, 2048, 640, 1728
        return {
            "x": _w((lambda *s: torch.randn(*s, dtype=torch.bfloat16) * 0.05)(B, S, D)),
            "W_gate": _w((lambda *s: torch.randn(*s, dtype=torch.bfloat16) * 0.4)(D, F) * (2.0 / D) ** 0.5),
            "W_up": _w((lambda *s: torch.randn(*s, dtype=torch.bfloat16) * 0.4)(D, F) * (2.0 / D) ** 0.5),
            "W_down": _w((lambda *s: torch.randn(*s, dtype=torch.bfloat16) * 0.4)(F, D) * (1.0 / F) ** 0.5),
        }
    _B_in = _B_setup()

    return {
        "A_x": _A_in["x"],
        "A_wqkv": _A_in["wqkv"],
        "A_wo": _A_in["wo"],
        "B_x": _B_in["x"],
        "B_W_gate": _B_in["W_gate"],
        "B_W_up": _B_in["W_up"],
        "B_W_down": _B_in["W_down"],
    }

def reference(A_x, A_wqkv, A_wo, B_x, B_W_gate, B_W_up, B_W_down):
    def _A_ref(x, wqkv, wo):
        B, S, D = x.shape
        H = 32
        Dh = D // H
        qkv = (lambda x, *d: x.permute(*d))((x @ wqkv).reshape(B, S, 3, H, Dh), 2, 0, 3, 1, 4)
        q, k, v = qkv[0], qkv[1], qkv[2]
        scores = torch.einsum("bhqd,bhkd->bhqk", q, k) * (Dh ** -0.5)
        p = _softmax(scores, axis=-1)
        o = torch.einsum("bhqk,bhkd->bhqd", p, v)
        return (lambda x, *d: x.permute(*d))(o, 0, 2, 1, 3).reshape(B, S, D) @ wo

    def _B_ref(x, W_gate, W_up, W_down):
        gate = torch.einsum("bsd,df->bsf", x, W_gate)
        up = torch.einsum("bsd,df->bsf", x, W_up)
        gate = gate * (lambda x, dt: x.to(dt))(gate > 0, gate.dtype)
        h = gate * up
        return torch.einsum("bsf,fd->bsd", h, W_down)

    x = A_x
    h = x + _A_ref(_layernorm(x, axis=-1), A_wqkv, A_wo)
    h2 = h + _B_ref(_layernorm(h, axis=-1), B_W_gate, B_W_up, B_W_down)
    return h2
```

```cpp
#include <hip/hip_runtime.h>
#include <hip/hip_bf16.h>

typedef __attribute__((ext_vector_type(8))) __bf16 bf16x8;
typedef __attribute__((ext_vector_type(16))) __bf16 bf16x16;
typedef __attribute__((ext_vector_type(2))) __bf16 bf16x2;
typedef __attribute__((ext_vector_type(4))) short s16x4;
typedef __attribute__((ext_vector_type(8))) short s16x8;
typedef __attribute__((ext_vector_type(16))) float f32x16;
typedef __attribute__((ext_vector_type(8))) float f32x8;
typedef __attribute__((ext_vector_type(4))) float f32x4;
typedef __attribute__((ext_vector_type(2))) float f32x2;
typedef __attribute__((ext_vector_type(4))) unsigned u32x4;
typedef __attribute__((ext_vector_type(2))) unsigned u32x2;
typedef unsigned long long u64;
#define LAS __attribute__((address_space(3)))
#define DEV __device__ __forceinline__
#define SCHED() __builtin_amdgcn_sched_barrier(0)

constexpr int S_ = 2048, D_ = 640, F_ = 1728;
constexpr int QT = 1280;
constexpr int KVT = 2624;
constexpr size_t WS_Q = 0;
constexpr size_t WS_KV = WS_Q + (size_t)32 * 64 * QT;
constexpr size_t WS_O = WS_KV + (size_t)32 * 64 * KVT;
constexpr size_t WS_H = WS_O + (size_t)S_ * D_ * 2;
constexpr size_t WS_ST = WS_H + (size_t)S_ * D_ * 2;
constexpr size_t WS_A = WS_ST + (size_t)S_ * 64;
constexpr size_t WS_WO = WS_A + (size_t)S_ * F_ * 2;
constexpr size_t WS_WD = WS_WO + (size_t)D_ * D_ * 2;
constexpr size_t WS_WGU = WS_WD + (size_t)F_ * D_ * 2;
constexpr size_t WS_END = WS_WGU + (size_t)2 * F_ * D_ * 2;

DEV int swap23(int r) { return (r & 0x13) | ((r & 4) << 1) | ((r & 8) >> 1); }
DEV unsigned pack2(float a, float b) { f32x2 v = {a, b}; bf16x2 r = __builtin_convertvector(v, bf16x2); return __builtin_bit_cast(unsigned, r); }
DEV float bflo(unsigned u) { return __builtin_bit_cast(float, u << 16); }
DEV float bfhi(unsigned u) { return __builtin_bit_cast(float, u & 0xffff0000u); }
DEV float rbf(float a) { return bflo(pack2(a, a)); }
DEV bf16x8 lds_b128(unsigned a) { return *(const LAS bf16x8*)a; }
DEV s16x4 lds_tr(unsigned a) { return __builtin_amdgcn_ds_read_tr16_b64_v4i16((LAS s16x4*)a); }
DEV bf16x8 cat(s16x4 a, s16x4 b) { s16x8 r = __builtin_shufflevector(a, b, 0, 1, 2, 3, 4, 5, 6, 7); return __builtin_bit_cast(bf16x8, r); }
DEV void ln_scalars(float sum, float sq, float& mean, float& rinv) {
  float m = sum * (1.0f / D_);
  float v = fmaxf(sq * (1.0f / D_) - m * m, 0.0f);
  float sd = rbf(__builtin_sqrtf(rbf(rbf(v) + 1e-5f)));
  mean = rbf(m);
  rinv = 1.0f / sd;
}
DEV void dma1(unsigned voff, const void* sbase, unsigned dst) {
  asm volatile("s_mov_b32 m0, %2\n\ts_nop 0\n\tglobal_load_lds_dwordx4 %0, %1" ::"v"(voff), "s"(sbase), "s"(dst) : "memory");
}
DEV void dma1m(unsigned voff, const void* sbase, unsigned dst, u64 mask) {
  asm volatile("s_mov_b32 m0, %2\n\ts_mov_b64 exec, %3\n\tglobal_load_lds_dwordx4 %0, %1\n\ts_mov_b64 exec, -1" ::"v"(voff), "s"(sbase), "s"(dst), "s"(mask) : "memory");
}
#define WAITV(n) asm volatile("s_waitcnt vmcnt(" #n ")" ::: "memory")
#define WAITL0() asm volatile("s_waitcnt lgkmcnt(0)" ::: "memory")
#define BAR() do { asm volatile("" ::: "memory"); __builtin_amdgcn_s_barrier(); asm volatile("" ::: "memory"); } while (0)

constexpr int K1_LDS = 4 * 32768 + 2048;
template <bool TR>
DEV void k1_body(const char* xs, const char* ws, char* qb, char* kvb, int rb, int cb) {
  extern __shared__ __attribute__((aligned(1024))) char lds_raw[];
  const unsigned lds0 = (unsigned)(size_t)(LAS char*)lds_raw;
  const int tid = threadIdx.x, lane = tid & 63, wv = __builtin_amdgcn_readfirstlane(tid >> 6);
  const int wm = wv >> 1, wn = wv & 1, h = lane >> 5, rho = lane & 31, sw = swap23(rho);
  unsigned vx[4], vw[4];
  {
    int r = 8 * wv + (lane >> 3);
    unsigned o = r * 1280 + (((lane & 7) ^ ((r >> 1) & 7)) << 4);
    int kr = 4 * wv + (lane >> 4);
    unsigned ow = kr * 3840 + (((lane & 15) ^ ((lane >> 4) << 2)) << 4);
#pragma unroll
    for (int n = 0; n < 4; ++n) { vx[n] = o + n * 32 * 1280; vw[n] = ow + n * 16 * 3840; }
  }
  const unsigned dst0 = lds0 + wv * 1024;
#pragma unroll
  for (int s = 0; s < 3; ++s) {
#pragma unroll
    for (int n = 0; n < 4; ++n) { dma1(vx[n], xs + s * 128, dst0 + s * 32768 + n * 4096); dma1(vw[n], ws + s * 64 * 3840, dst0 + s * 32768 + 16384 + n * 4096); }
  }
  unsigned ax[2][4], aw[2];
#pragma unroll
  for (int t = 0; t < 2; ++t) {
    int row = wm * 64 + ((t ^ wn) << 5) + sw;
#pragma unroll
    for (int ks = 0; ks < 4; ++ks) ax[t][ks] = lds0 + row * 128 + (((2 * ks + h) ^ ((row >> 1) & 7)) << 4);
    int q = (lane & 15) >> 2, p = lane & 3, blk = (lane >> 4) & 1;
    int ch = wn * 8 + ((t ^ wm) << 2) + 2 * blk + (p & 1);
    aw[t] = lds0 + 16384 + (8 * h + q) * 256 + ((ch ^ (q << 2)) << 4) + 8 * (p >> 1);
  }
  bf16x8 sel;
  { __bf16 one = (__bf16)((((lane >> 4) ^ (lane >> 2)) & 1) ? 0.0f : 1.0f);
#pragma unroll
    for (int j = 0; j < 8; ++j) sel[j] = one; }
  f32x16 acc[2][2] = {};
  f32x4 sx = {}, sww = {};
  float q0 = 0.f, q1 = 0.f;
  WAITV(16); BAR();
  unsigned slot = 0;
  bf16x8 fx[2], fw[2];
#pragma unroll
  for (int t = 0; t < 2; ++t) { fx[t] = lds_b128(ax[t][0]); fw[t] = cat(lds_tr(aw[t]), lds_tr(aw[t] + 1024)); }
#pragma unroll 1
  for (int s = 0; s < 10; ++s) {
    const int st = s + 3 < 10 ? s + 3 : 9;
    const u64 mask = s + 3 < 10 ? ~0ull : 1ull;
    const char* xg = xs + st * 128;
    const char* wg = ws + st * (64 * 3840);
    const unsigned dst = dst0 + ((s + 3) & 3) * 32768;
    const unsigned nslot = ((s + 1) & 3) * 32768;
#pragma unroll
    for (int ks = 0; ks < 4; ++ks) {
      bf16x8 nx[2], nw[2];
      if (ks == 3) { WAITV(16); WAITL0(); BAR(); }
      SCHED();
#pragma unroll
      for (int t = 0; t < 2; ++t) {
        if (ks < 3) { nx[t] = lds_b128(ax[t][ks + 1] + slot); nw[t] = cat(lds_tr(aw[t] + slot + (ks + 1) * 4096), lds_tr(aw[t] + slot + (ks + 1) * 4096 + 1024)); }
        else { nx[t] = lds_b128(ax[t][0] + nslot); nw[t] = cat(lds_tr(aw[t] + nslot), lds_tr(aw[t] + nslot + 1024)); }
      }
      u32x4 xu = __builtin_bit_cast(u32x4, fx[0]);
#pragma unroll
      for (int i = 0; i < 4; ++i) {
        const int tm = i >> 1, tn = i & 1;
        if (TR) acc[tm][tn] = __builtin_amdgcn_mfma_f32_32x32x16_bf16(fw[tn], fx[tm], acc[tm][tn], 0, 0, 0);
        else acc[tm][tn] = __builtin_amdgcn_mfma_f32_32x32x16_bf16(fx[tm], fw[tn], acc[tm][tn], 0, 0, 0);
        float lo = bflo(xu[i]), hi = bfhi(xu[i]);
        q0 = fmaf(lo, lo, q0); q1 = fmaf(hi, hi, q1);
        asm volatile("" : "+v"(q0), "+v"(q1));
        if (ks < 3 && i < 3) {
          const int j = ks * 3 + i;
          if (j < 8) { if (j & 1) dma1m(vw[j >> 1], wg, dst + 16384 + (j >> 1) * 4096, mask); else dma1m(vx[j >> 1], xg, dst + (j >> 1) * 4096, mask); }
        }
        SCHED();
      }
      sx = __builtin_amdgcn_mfma_f32_16x16x32_bf16(sel, fx[0], sx, 0, 0, 0);
      sww = __builtin_amdgcn_mfma_f32_16x16x32_bf16(sel, fw[0], sww, 0, 0, 0);
#pragma unroll
      for (int t = 0; t < 2; ++t) { fx[t] = nx[t]; fw[t] = nw[t]; }
    }
    slot = nslot;
  }
  LAS float* stf = (LAS float*)(lds0 + 4 * 32768);
  {
    float sq = q0 + q1;
    sq += __shfl_xor(sq, 32);
    float mean, rinv;
    ln_scalars(sx[0], sq, mean, rinv);
    if (h == 0) {
      int row = wm * 64 + (wn << 5) + sw;
      stf[row] = rinv; stf[128 + row] = mean * rinv;
      stf[256 + wn * 64 + (wm << 5) + sw] = sww[0];
    }
  }
  WAITL0(); BAR();
  if (TR) {
    const bool isq = cb < 5;
    const float sc = isq ? 0.32259580f : 1.0f;
    char* base = isq ? qb : kvb;
    const int tstride = isq ? QT : KVT;
    const int cc0 = (isq ? cb : cb - 5) * 16 + wn * 8 + h;
#pragma unroll
    for (int tm = 0; tm < 2; ++tm) {
      const int row = wm * 64 + ((tm ^ wn) << 5) + sw;
      const float rinv = stf[row] * sc, mr = stf[128 + row] * sc;
      const int tile = rb * 4 + wm * 2 + (tm ^ wn);
#pragma unroll
      for (int tn = 0; tn < 2; ++tn)
#pragma unroll
        for (int s = 0; s < 2; ++s) {
          const int cl = ((tn ^ wm) << 2) + 2 * s;
          const LAS f32x4* cs = (const LAS f32x4*)(stf + 256 + wn * 64 + (cl + h) * 8);
          const f32x4 c0 = cs[0], c1 = cs[1];
          u32x4 o;
#pragma unroll
          for (int j = 0; j < 2; ++j) {
            o[j] = pack2(acc[tm][tn][8 * s + 2 * j] * rinv - mr * c0[2 * j], acc[tm][tn][8 * s + 2 * j + 1] * rinv - mr * c0[2 * j + 1]);
            o[2 + j] = pack2(acc[tm][tn][8 * s + 4 + 2 * j] * rinv - mr * c1[2 * j], acc[tm][tn][8 * s + 5 + 2 * j] * rinv - mr * c1[2 * j + 1]);
          }
          const int cc = cc0 + cl, p = (cc * 205) >> 10, r = cc - 5 * p;
          if (r == 2) {
            char* a = base + (size_t)((2 * p * 64 + tile) * tstride + 1024 + sw * 8);
            *(u32x2*)a = u32x2{o[0], o[1]};
            *(u32x2*)(a + 64 * tstride) = u32x2{o[2], o[3]};
          } else {
            const int odd = r > 2, rr = r - 3 * odd;
            *(u32x4*)(base + (size_t)(((2 * p + odd) * 64 + tile) * tstride + rr * 512 + sw * 16)) = o;
          }
        }
    }
  } else {
#pragma unroll
    for (int tn = 0; tn < 2; ++tn) {
      const int cl = wn * 64 + ((tn ^ wm) << 5) + sw;
      const float cs = stf[256 + cl];
      const int f = (cb - 10) * 128 + cl, head = (f * 3277) >> 16, d = f - head * 20;
      const int dp = (head & 1) ? (d >= 4 ? d - 4 : d + 16) : d;
#pragma unroll
      for (int tm = 0; tm < 2; ++tm) {
        const int tile = rb * 4 + wm * 2 + (tm ^ wn);
        char* a = kvb + (size_t)((head * 64 + tile) * KVT + QT + (h * 21 + dp) * 16);
#pragma unroll
        for (int s = 0; s < 2; ++s) {
          const int row = wm * 64 + ((tm ^ wn) << 5) + 16 * s + 8 * h;
          const LAS f32x4* rp = (const LAS f32x4*)(stf + row);
          const f32x4 r0 = rp[0], r1 = rp[1], m0 = rp[32], m1 = rp[33];
          u32x4 o;
#pragma unroll
          for (int j = 0; j < 2; ++j) {
            o[j] = pack2(acc[tm][tn][8 * s + 2 * j] * r0[2 * j] - m0[2 * j] * cs, acc[tm][tn][8 * s + 2 * j + 1] * r0[2 * j + 1] - m0[2 * j + 1] * cs);
            o[2 + j] = pack2(acc[tm][tn][8 * s + 4 + 2 * j] * r1[2 * j] - m1[2 * j] * cs, acc[tm][tn][8 * s + 5 + 2 * j] * r1[2 * j + 1] - m1[2 * j + 1] * cs);
          }
          *(u32x4*)(a + s * 42 * 16) = o;
          if (dp == 0) *(u32x4*)(a + s * 42 * 16 + 320) = u32x4{0x3f803f80u, 0x3f803f80u, 0x3f803f80u, 0x3f803f80u};
        }
      }
    }
  }
  WAITV(0);
}

__global__ __launch_bounds__(256, 1) void k1_qkv(const char* __restrict__ x, const char* __restrict__ w, char* __restrict__ ws) {
  const int b = blockIdx.x, t = (b & 7) * 30 + (b >> 3), half = t >= 120, u = t - 120 * half;
  const int cb = u >> 3, rb = half * 8 + (u & 7);
  const char* xs = x + (size_t)rb * 128 * 1280;
  const char* wsrc = w + cb * 256;
  if (cb < 10) k1_body<true>(xs, wsrc, ws + WS_Q, ws + WS_KV, rb, cb);
  else k1_body<false>(xs, wsrc, ws + WS_Q, ws + WS_KV, rb, cb);
}

constexpr int PH = 16 * KVT;
constexpr int K2_LDS = 3 * PH + 3 * 5120 + 5 * 4096;

DEV f32x16 qk(bf16x8 k0, s16x4 kt, bf16x8 q0, s16x4 qt) {
  f32x16 z = {};
  f32x16 s = __builtin_amdgcn_mfma_f32_32x32x16_bf16(k0, q0, z, 0, 0, 0);
  return __builtin_amdgcn_mfma_f32_32x32x8bf16_1k(kt, qt, s, 0, 0, 0);
}
DEV bf16x16 phalf(f32x16 s, int i) {
  f32x8 r = i ? __builtin_shufflevector(s, s, 8, 9, 10, 11, 12, 13, 14, 15) : __builtin_shufflevector(s, s, 0, 1, 2, 3, 4, 5, 6, 7);
  return __builtin_bit_cast(bf16x16, r);
}

__global__ __launch_bounds__(512, 2) void k2_attn(const char* __restrict__ wo, const char* __restrict__ wg, const char* __restrict__ wu,
                                                  const char* __restrict__ wd, char* __restrict__ ws) {
  extern __shared__ __attribute__((aligned(1024))) char lds_raw[];
  const unsigned lds0 = (unsigned)(size_t)(LAS char*)lds_raw;
  const int tid = threadIdx.x, lane = tid & 63, wv = __builtin_amdgcn_readfirstlane(tid >> 6);
  const int h = lane >> 5, rho = lane & 31, sw = swap23(rho);
  const int b = blockIdx.x, head = (b & 7) * 4 + ((b >> 3) & 3), qtile = (b >> 5) * 8 + wv;
  const char* kv = ws + WS_KV + (size_t)head * 64 * KVT;
  const char* qp = ws + WS_Q + (size_t)(head * 64 + qtile) * QT;
  const unsigned v16 = lane * 16;
  bf16x8 q0 = *(const bf16x8*)(qp + h * 512 + rho * 16);
  s16x4 qt = *(const s16x4*)(qp + 1024 + rho * 8);
#pragma unroll
  for (int p = 0; p < 3; ++p)
#pragma unroll
    for (int n = 0; n < 6; ++n) { int i = wv + 8 * n; i = i > 40 ? 40 : i; dma1(v16, kv + p * PH + i * 1024, lds0 + p * PH + i * 1024); }
  if (h) qt = s16x4{0, 0, 0, 0};
  asm volatile("" : "+v"(q0), "+v"(qt));
  const int idx = 0xDDDD;
  const unsigned ak = lds0 + h * 512 + rho * 16, at = lds0 + 1024 + rho * 8, av = lds0 + QT + (h * 21 + (sw > 20 ? 20 : sw)) * 16;
  const bool ta = wv < 3;
  const int unit = ta ? b * 3 + wv : b * 5 + wv - 3;
  const bool tact = ta ? unit < 592 : unit < 1080;
  const unsigned tl = lds0 + 3 * PH + (ta ? wv * 5120 : 3 * 5120 + (wv - 3) * 4096);

  WAITV(12); BAR();
  f32x16 o = {};
  f32x16 sc = qk(lds_b128(ak), *(const LAS s16x4*)at, q0, qt);
  bf16x8 v0 = lds_b128(av), v1 = lds_b128(av + 42 * 16);
  unsigned slot = 0;
#pragma unroll 1
  for (int p = 0; p < 4; ++p) {
    const unsigned nslot = p >= 2 ? 0 : slot + PH;
#pragma unroll
    for (int t = 0; t < 16; ++t) {
      if (t == 15) {
        if (p == 0) {
          WAITV(0); WAITL0(); BAR();
#pragma unroll
          for (int n = 0; n < 6; ++n) { int i = wv + 8 * n; if (i < 41) dma1(v16, kv + 3 * PH + i * 1024, lds0 + i * 1024); }
          if (tact) {
            if (ta) {
              const int isd = unit >= 160, u = unit - 160 * isd;
              const char* src = (isd ? wd : wo) + (size_t)(u >> 3) * 32 * 1280 + (u & 7) * 160;
#pragma unroll
              for (int n = 0; n < 5; ++n) { int c = lane + 64 * n, r = (c * 205) >> 11; dma1(r * 1280 + (c - 10 * r) * 16, src, tl + n * 1024); }
            } else {
              const int ks = (unit * 2428) >> 16, fg = unit - 27 * ks;
              const unsigned vo = (lane >> 3) * (F_ * 2) + (lane & 7) * 16;
              const size_t so = (size_t)ks * 16 * (F_ * 2) + fg * 128;
#pragma unroll
              for (int n = 0; n < 4; ++n) dma1(vo, ((n >> 1) ? wu : wg) + so + (n & 1) * 8 * (F_ * 2), tl + n * 1024);
            }
          }
        } else if (p == 2) {
          WAITV(0); WAITL0(); BAR();
          if (tact) {
            const int q = (lane & 15) >> 2, pp = lane & 3;
            if (ta) {
              const int isd = unit >= 160, u = unit - 160 * isd;
              char* dst = ws + (isd ? WS_WD + (size_t)((u & 7) * 54 + (u >> 3)) * 5120 : WS_WO + (size_t)((u & 7) * 20 + (u >> 3)) * 5120) + v16;
              const unsigned a = tl + (8 * (lane >> 4) + q) * 160;
#pragma unroll
              for (int T = 0; T < 5; ++T) {
                const unsigned c = a + (T < 4 ? (T >> 1) * 64 + (T & 1) * 8 + pp * 16 : 128 + pp * 8);
                *(bf16x8*)(dst + T * 1024) = cat(lds_tr(c), lds_tr(c + 640));
              }
            } else {
              char* dst = ws + WS_WGU + (size_t)unit * 4096 + v16;
              const unsigned a = tl + ((lane >> 4) & 1) * 2048 + (8 * h + q) * 128 + (pp & 1) * 16 + (pp >> 1) * 8;
#pragma unroll
              for (int T = 0; T < 4; ++T) *(bf16x8*)(dst + T * 1024) = cat(lds_tr(a + T * 32), lds_tr(a + T * 32 + 512));
            }
          }
        }
      }
      SCHED();
      const unsigned nb = t < 15 ? slot + (t + 1) * KVT : nslot;
      bf16x8 k0 = lds_b128(ak + nb);
      s16x4 kt = *(const LAS s16x4*)(at + nb);
      bf16x8 n0 = lds_b128(av + nb), n1 = lds_b128(av + nb + 42 * 16);
      SCHED();
#pragma unroll
      for (int i = 0; i < 8; ++i) sc[i] = __builtin_amdgcn_exp2f(sc[i]);
      SCHED();
      f32x16 z = {};
      f32x16 sn = __builtin_amdgcn_mfma_f32_32x32x16_bf16(k0, q0, z, 0, 0, 0);
      SCHED();
#pragma unroll
      for (int i = 8; i < 11; ++i) sc[i] = __builtin_amdgcn_exp2f(sc[i]);
      SCHED();
      sn = __builtin_amdgcn_mfma_f32_32x32x8bf16_1k(kt, qt, sn, 0, 0, 0);
      SCHED();
#pragma unroll
      for (int i = 11; i < 14; ++i) sc[i] = __builtin_amdgcn_exp2f(sc[i]);
      SCHED();
      o = __builtin_amdgcn_smfmac_f32_32x32x32_bf16(v0, phalf(sc, 0), o, idx, 0, 0);
      SCHED();
#pragma unroll
      for (int i = 14; i < 16; ++i) sc[i] = __builtin_amdgcn_exp2f(sc[i]);
      SCHED();
      o = __builtin_amdgcn_smfmac_f32_32x32x32_bf16(v1, phalf(sc, 1), o, idx, 0, 0);
      sc = sn; v0 = n0; v1 = n1;
    }
    slot = nslot;
  }
  {
    float mx = 0.f;
#pragma unroll
    for (int i = 0; i < 12; ++i) mx = fmaxf(mx, fabsf(o[i]));
    float chk = 0.f;
#pragma unroll
    for (int i = 0; i < 12; ++i) chk += o[i] * 0.0f;
    const float l = o[12];
    const bool ok = (l >= 5.421010862e-20f) && (l <= 1.8446744e19f) && (chk == 0.f) && (mx <= 3.0e38f);
    if (__builtin_amdgcn_ballot_w64(!ok) != 0) {
      float m = -3.0e38f;
      o = f32x16{};
      const char* g = kv;
#pragma unroll 1
      for (int t = 0; t < 64; ++t, g += KVT) {
        const bf16x8 k0 = *(const bf16x8*)(g + (ak - lds0));
        const s16x4 kt = *(const s16x4*)(g + (at - lds0));
        const bf16x8 w0 = *(const bf16x8*)(g + (av - lds0)), w1 = *(const bf16x8*)(g + (av - lds0) + 42 * 16);
        f32x16 s = qk(k0, kt, q0, qt);
        float mn = m;
#pragma unroll
        for (int i = 0; i < 16; ++i) mn = fmaxf(mn, s[i]);
        mn = fmaxf(mn, __shfl_xor(mn, 32));
        const float alpha = __builtin_amdgcn_exp2f(m - mn);
        m = mn;
#pragma unroll
        for (int i = 0; i < 16; ++i) { s[i] = __builtin_amdgcn_exp2f(s[i] - mn); o[i] *= alpha; }
        o = __builtin_amdgcn_smfmac_f32_32x32x32_bf16(w0, phalf(s, 0), o, idx, 0, 0);
        o = __builtin_amdgcn_smfmac_f32_32x32x32_bf16(w1, phalf(s, 1), o, idx, 0, 0);
      }
    }
  }
  const float il = 1.0f / o[12];
  const int tok = qtile * 32 + rho, odd = head & 1;
  char* ob = ws + WS_O + (size_t)(tok >> 4) * 20480 + (tok & 15) * 16;
  const int cc = (head >> 1) * 5 + 3 * odd + h, c2 = (head >> 1) * 5 + 2;
  *(u32x4*)(ob + cc * 256) = u32x4{pack2(o[0] * il, o[1] * il), pack2(o[2] * il, o[3] * il), pack2(o[4] * il, o[5] * il), pack2(o[6] * il, o[7] * il)};
  if (h == 0) *(u32x2*)(ob + c2 * 256 + odd * 8) = u32x2{pack2(o[8] * il, o[9] * il), pack2(o[10] * il, o[11] * il)};
}

constexpr int KS_LDS = 4 * 3 * 5 * 1024;
template <int NK, int DEPTH>
DEV void ksplit_gemm(const char* act, const char* wt, int wv, int lane, f32x4 (&res)[5]) {
  extern __shared__ __attribute__((aligned(1024))) char lds_raw[];
  const unsigned lds0 = (unsigned)(size_t)(LAS char*)lds_raw;
  constexpr int NI = (NK + 3) / 4;
  const char* ap[4];
#pragma unroll
  for (int m = 0; m < 4; ++m) ap[m] = act + (size_t)((m ^ wv) * NK + wv) * 1024;
  const char* wp = wt + wv * 5120;
  const unsigned v16 = lane * 16;
  const bool last = (NI - 1) * 4 + wv < NK;
  bf16x8 fa[NI][4], fw[NI][5];
  f32x4 acc[4][5] = {};
#pragma unroll
  for (int i = 0; i < NI + DEPTH; ++i) {
    if (i < NI) {
      const int ii = (i == NI - 1 && NK % 4) ? (last ? i : i - 1) : i;
#pragma unroll
      for (int m = 0; m < 4; ++m) {
        fa[i][m] = *(const bf16x8*)(ap[m] + (size_t)ii * 4096 + v16);
        fw[i][m] = *(const bf16x8*)(wp + (size_t)ii * 20480 + m * 1024 + v16);
      }
      fw[i][4] = *(const bf16x8*)(wp + (size_t)ii * 20480 + 4096 + v16);
    }
    SCHED();
    const int c = i - DEPTH;
    if (c >= 0 && (c < NI - 1 || NK % 4 == 0 || last)) {
#pragma unroll
      for (int m = 0; m < 4; ++m)
#pragma unroll
        for (int T = 0; T < 5; ++T) acc[m][T] = __builtin_amdgcn_mfma_f32_16x16x32_bf16(fw[c][T], fa[c][m], acc[m][T], 0, 0, 0);
    }
    SCHED();
  }
#pragma unroll
  for (int m = 1; m < 4; ++m)
#pragma unroll
    for (int T = 0; T < 5; ++T) *(LAS f32x4*)(lds0 + (((m ^ wv) * 3 + m - 1) * 5 + T) * 1024 + v16) = acc[m][T];
  WAITL0(); BAR();
#pragma unroll
  for (int T = 0; T < 5; ++T) {
    f32x4 r = acc[0][T];
#pragma unroll
    for (int m = 0; m < 3; ++m) r += *(const LAS f32x4*)(lds0 + ((wv * 3 + m) * 5 + T) * 1024 + v16);
    res[T] = r;
  }
}
DEV size_t h_off(int tok, int chunk) { return (size_t)(tok >> 5) * 40960 + chunk * 512 + (tok & 31) * 16; }
DEV unsigned addres(unsigned r, float a, float b) { return pack2(bflo(r) + rbf(a), bfhi(r) + rbf(b)); }

__global__ __launch_bounds__(256, 1) void k3_wo(const char* __restrict__ x, char* __restrict__ ws) {
  const int tid = threadIdx.x, lane = tid & 63, wv = __builtin_amdgcn_readfirstlane(tid >> 6);
  const int b = blockIdx.x, xc = b & 7, j = b >> 3, rb = (xc >> 1) * 8 + (j & 7), cb = (xc & 1) * 4 + (j >> 3);
  const int tok = rb * 64 + wv * 16 + (lane & 15), g = lane >> 4;
  const char* xr = x + (size_t)tok * 1280 + cb * 160;
  const u32x4 r0 = *(const u32x4*)(xr + g * 16), r1 = *(const u32x4*)(xr + 64 + g * 16);
  const u32x2 r2 = *(const u32x2*)(xr + 128 + g * 8);
  f32x4 res[5];
  ksplit_gemm<20, 5>(ws + WS_O + (size_t)rb * 4 * 20480, ws + WS_WO + (size_t)cb * 20 * 5120, wv, lane, res);
  unsigned o[10];
#pragma unroll
  for (int i = 0; i < 4; ++i) {
    o[i] = addres(r0[i], res[i >> 1][2 * (i & 1)], res[i >> 1][2 * (i & 1) + 1]);
    o[4 + i] = addres(r1[i], res[2 + (i >> 1)][2 * (i & 1)], res[2 + (i >> 1)][2 * (i & 1) + 1]);
  }
  o[8] = addres(r2[0], res[4][0], res[4][1]); o[9] = addres(r2[1], res[4][2], res[4][3]);
  char* hb = ws + WS_H;
  *(u32x4*)(hb + h_off(tok, cb * 10 + g)) = u32x4{o[0], o[1], o[2], o[3]};
  *(u32x4*)(hb + h_off(tok, cb * 10 + 4 + g)) = u32x4{o[4], o[5], o[6], o[7]};
  *(u32x2*)(hb + h_off(tok, cb * 10 + 8 + (g >> 1)) + (g & 1) * 8) = u32x2{o[8], o[9]};
  float sm = 0.f, sq = 0.f;
#pragma unroll
  for (int i = 0; i < 10; ++i) { float a = bflo(o[i]), c = bfhi(o[i]); sm += a + c; sq = fmaf(a, a, fmaf(c, c, sq)); }
  sm += __shfl_xor(sm, 16); sq += __shfl_xor(sq, 16);
  sm += __shfl_xor(sm, 32); sq += __shfl_xor(sq, 32);
  if (g == 0) *(f32x2*)(ws + WS_ST + (size_t)tok * 64 + cb * 8) = f32x2{sm, sq};
}

__global__ __launch_bounds__(256, 1) void k5_down(char* __restrict__ ws, char* __restrict__ out) {
  const int tid = threadIdx.x, lane = tid & 63, wv = __builtin_amdgcn_readfirstlane(tid >> 6);
  const int b = blockIdx.x, xc = b & 7, j = b >> 3, rb = (xc >> 1) * 8 + (j & 7), cb = (xc & 1) * 4 + (j >> 3);
  const int tok = rb * 64 + wv * 16 + (lane & 15), g = lane >> 4;
  const char* hb = ws + WS_H;
  const u32x4 r0 = *(const u32x4*)(hb + h_off(tok, cb * 10 + g)), r1 = *(const u32x4*)(hb + h_off(tok, cb * 10 + 4 + g));
  const u32x2 r2 = *(const u32x2*)(hb + h_off(tok, cb * 10 + 8 + (g >> 1)) + (g & 1) * 8);
  f32x4 res[5];
  ksplit_gemm<54, 5>(ws + WS_A + (size_t)rb * 4 * 54 * 1024, ws + WS_WD + (size_t)cb * 54 * 5120, wv, lane, res);
  unsigned o[10];
#pragma unroll
  for (int i = 0; i < 4; ++i) {
    o[i] = addres(r0[i], res[i >> 1][2 * (i & 1)], res[i >> 1][2 * (i & 1) + 1]);
    o[4 + i] = addres(r1[i], res[2 + (i >> 1)][2 * (i & 1)], res[2 + (i >> 1)][2 * (i & 1) + 1]);
  }
  o[8] = addres(r2[0], res[4][0], res[4][1]); o[9] = addres(r2[1], res[4][2], res[4][3]);
  char* orow = out + (size_t)tok * 1280 + cb * 160;
  *(u32x4*)(orow + g * 16) = u32x4{o[0], o[1], o[2], o[3]};
  *(u32x4*)(orow + 64 + g * 16) = u32x4{o[4], o[5], o[6], o[7]};
  *(u32x2*)(orow + 128 + g * 8) = u32x2{o[8], o[9]};
}

constexpr int K4_ST = 28672 + 16384;
constexpr int K4_LDS = 3 * K4_ST;
DEV void dma4m(unsigned voff, const void* sbase, unsigned dst, u64 mask) {
  asm volatile("s_mov_b32 m0, %2\n\ts_mov_b64 exec, %3\n\tglobal_load_lds_dwordx4 %0, %1\n\tglobal_load_lds_dwordx4 %0, %1 offset:1024\n\t"
               "global_load_lds_dwordx4 %0, %1 offset:2048\n\tglobal_load_lds_dwordx4 %0, %1 offset:3072\n\ts_mov_b64 exec, -1" ::"v"(voff), "s"(sbase), "s"(dst), "s"(mask) : "memory");
}
DEV bf16x8 ln_frag(bf16x8 f, float rinv, float nmr) {
  u32x4 u = __builtin_bit_cast(u32x4, f), r;
#pragma unroll
  for (int i = 0; i < 4; ++i) r[i] = pack2(fmaf(bflo(u[i]), rinv, nmr), fmaf(bfhi(u[i]), rinv, nmr));
  return __builtin_bit_cast(bf16x8, r);
}
__global__ __launch_bounds__(256, 1) void k4_gu(char* __restrict__ ws) {
  extern __shared__ __attribute__((aligned(1024))) char lds_raw[];
  const unsigned lds0 = (unsigned)(size_t)(LAS char*)lds_raw;
  const int tid = threadIdx.x, lane = tid & 63, wv = __builtin_amdgcn_readfirstlane(tid >> 6);
  const int h = lane >> 5, rho = lane & 31;
  const int b = blockIdx.x, xc = b & 7, j = b >> 3, tb = (xc & 1) * 8 + (j & 7), fb = (xc >> 1) * 4 + (j >> 3);
  const int ft0 = (27 * fb) >> 2, nft = ((27 * (fb + 1)) >> 2) - ft0;
  const int tok = tb * 128 + wv * 32 + rho;
  const unsigned v16 = lane * 16;
  const f32x4* sp = (const f32x4*)(ws + WS_ST + (size_t)tok * 64 + h * 32);
  const f32x4 s0 = sp[0], s1 = sp[1];
  const char* wsrc = ws + WS_WGU + (size_t)(wv * 108 + ft0) * 1024;
  const char* hsrc = ws + WS_H + (size_t)(tb * 4 + wv) * 40960;
  const unsigned dw = lds0 + wv * 7168, dh = lds0 + 28672 + wv * 4096;
#pragma unroll
  for (int s = 0; s < 2; ++s) {
#pragma unroll
    for (int n = 0; n < 4; ++n) dma1(v16, hsrc + s * 4096 + n * 1024, dh + s * K4_ST + n * 1024);
#pragma unroll
    for (int n = 0; n < 7; ++n) dma1(v16, wsrc + s * (432 * 1024) + n * 1024, dw + s * K4_ST + n * 1024);
  }
  float mean, rinv;
  {
    float sm = (s0[0] + s0[2]) + (s1[0] + s1[2]), sq = (s0[1] + s0[3]) + (s1[1] + s1[3]);
    sm += __shfl_xor(sm, 32); sq += __shfl_xor(sq, 32);
    ln_scalars(sm, sq, mean, rinv);
  }
  const float nmr = -mean * rinv;
  const unsigned aw = lds0 + v16, ah = dh + v16;
  f32x16 acc[7] = {};
  WAITV(11); BAR();
  bf16x8 fw[7], fh;
#pragma unroll
  for (int T = 0; T < 7; ++T) fw[T] = lds_b128(aw + T * 1024);
  fh = ln_frag(lds_b128(ah), rinv, nmr);
  unsigned slot = 0;
#pragma unroll 1
  for (int s = 0; s < 10; ++s) {
    const int st = s + 2 < 10 ? s + 2 : 9;
    const u64 mask = s + 2 < 10 ? ~0ull : 1ull;
    const unsigned nslot = slot == 2 * K4_ST ? 0 : slot + K4_ST;
    const unsigned pslot = slot == 0 ? 2 * K4_ST : slot - K4_ST;
    const char* wgp = wsrc + (size_t)st * (432 * 1024);
    const char* hgp = hsrc + st * 4096;
#pragma unroll
    for (int ks = 0; ks < 4; ++ks) {
      if (ks == 3) { WAITV(11); WAITL0(); BAR(); }
      SCHED();
      const unsigned wa = ks < 3 ? aw + slot + (ks + 1) * 7168 : aw + nslot;
      const bf16x8 hraw = lds_b128(ks < 3 ? ah + slot + (ks + 1) * 1024 : ah + nslot);
      const u32x4 hu = __builtin_bit_cast(u32x4, hraw);
      u32x4 hn;
      bf16x8 nw[7];
#pragma unroll
      for (int T = 0; T < 7; ++T) {
        acc[T] = __builtin_amdgcn_mfma_f32_32x32x16_bf16(fw[T], fh, acc[T], 0, 0, 0);
        nw[T] = lds_b128(wa + T * 1024);
        if (T >= 3) hn[T - 3] = pack2(fmaf(bflo(hu[T - 3]), rinv, nmr), fmaf(bfhi(hu[T - 3]), rinv, nmr));
        if (ks < 3 && !(T & 1)) {
          const int jj = ks * 4 + (T >> 1);
          if (jj < 4) dma1m(v16, hgp + jj * 1024, dh + pslot + jj * 1024, mask);
          else if (jj < 11) dma1m(v16, wgp + (jj - 4) * 1024, dw + pslot + (jj - 4) * 1024, mask);
        }
        SCHED();
      }
#pragma unroll
      for (int T = 0; T < 7; ++T) fw[T] = nw[T];
      fh = __builtin_bit_cast(bf16x8, hn);
    }
    slot = nslot;
  }
  char* ab = ws + WS_A + (size_t)(tok >> 4) * (54 * 1024) + (tok & 15) * 16 + (2 * ft0 + h) * 256;
#pragma unroll
  for (int T = 0; T < 7; ++T) {
    u32x4 o;
#pragma unroll
    for (int i = 0; i < 4; ++i) o[i] = pack2(fmaxf(acc[T][2 * i], 0.f) * acc[T][8 + 2 * i], fmaxf(acc[T][2 * i + 1], 0.f) * acc[T][9 + 2 * i]);
    if (T < 6 || nft == 7) *(u32x4*)(ab + T * 512) = o;
  }
  WAITV(0);
}

extern "C" void kernel_launch(void* const* d_in, const int* in_sizes, int n_in, void* d_out, int out_size, void* d_ws, size_t ws_size,
                              hipStream_t stream) {
  const char* x = (const char*)d_in[0];
  const char* wqkv = (const char*)d_in[1];
  const char* wo = (const char*)d_in[2];
  const char* wg = (const char*)d_in[4];
  const char* wu = (const char*)d_in[5];
  const char* wd = (const char*)d_in[6];
  char* ws = (char*)d_ws;
  (void)hipFuncSetAttribute((const void*)k1_qkv, hipFuncAttributeMaxDynamicSharedMemorySize, K1_LDS);
  (void)hipFuncSetAttribute((const void*)k2_attn, hipFuncAttributeMaxDynamicSharedMemorySize, K2_LDS);
  (void)hipFuncSetAttribute((const void*)k4_gu, hipFuncAttributeMaxDynamicSharedMemorySize, K4_LDS);
  k1_qkv<<<240, 256, K1_LDS, stream>>>(x, wqkv, ws);
  k2_attn<<<256, 512, K2_LDS, stream>>>(wo, wg, wu, wd, ws);
  k3_wo<<<256, 256, KS_LDS, stream>>>(x, ws);
  k4_gu<<<256, 256, K4_LDS, stream>>>(ws);
  k5_down<<<256, 256, KS_LDS, stream>>>(ws, (char*)d_out);
}
```

```cpp
#include <hip/hip_runtime.h>
#include <hip/hip_bf16.h>

typedef __attribute__((ext_vector_type(8))) __bf16 bf16x8;
typedef __attribute__((ext_vector_type(16))) __bf16 bf16x16;
typedef __attribute__((ext_vector_type(2))) __bf16 bf16x2;
typedef __attribute__((ext_vector_type(4))) short s16x4;
typedef __attribute__((ext_vector_type(8))) short s16x8;
typedef __attribute__((ext_vector_type(16))) float f32x16;
typedef __attribute__((ext_vector_type(8))) float f32x8;
typedef __attribute__((ext_vector_type(4))) float f32x4;
typedef __attribute__((ext_vector_type(2))) float f32x2;
typedef __attribute__((ext_vector_type(4))) unsigned u32x4;
typedef __attribute__((ext_vector_type(2))) unsigned u32x2;
typedef unsigned long long u64;
#define LAS __attribute__((address_space(3)))
#define DEV __device__ __forceinline__
#define SCHED() __builtin_amdgcn_sched_barrier(0)

constexpr int S_ = 2048, D_ = 640, F_ = 1728;
constexpr int QT = 1280;
constexpr int KVT = 2624;
constexpr size_t WS_Q = 0;
constexpr size_t WS_KV = WS_Q + (size_t)32 * 64 * QT;
constexpr size_t WS_O = WS_KV + (size_t)32 * 64 * KVT;
constexpr size_t WS_H = WS_O + (size_t)S_ * D_ * 2;
constexpr size_t WS_ST = WS_H + (size_t)S_ * D_ * 2;
constexpr size_t WS_A = WS_ST + (size_t)S_ * 64;
constexpr size_t WS_WO = WS_A + (size_t)S_ * F_ * 2;
constexpr size_t WS_WD = WS_WO + (size_t)D_ * D_ * 2;
constexpr size_t WS_WGU = WS_WD + (size_t)F_ * D_ * 2;
constexpr size_t WS_END = WS_WGU + (size_t)2 * F_ * D_ * 2;

DEV int swap23(int r) { return (r & 0x13) | ((r & 4) << 1) | ((r & 8) >> 1); }
DEV unsigned pack2(float a, float b) { f32x2 v = {a, b}; bf16x2 r = __builtin_convertvector(v, bf16x2); return __builtin_bit_cast(unsigned, r); }
DEV float bflo(unsigned u) { return __builtin_bit_cast(float, u << 16); }
DEV float bfhi(unsigned u) { return __builtin_bit_cast(float, u & 0xffff0000u); }
DEV float rbf(float a) { return bflo(pack2(a, a)); }
DEV bf16x8 lds_b128(unsigned a) { return *(const LAS bf16x8*)a; }
DEV s16x4 lds_tr(unsigned a) { return __builtin_amdgcn_ds_read_tr16_b64_v4i16((LAS s16x4*)a); }
DEV bf16x8 cat(s16x4 a, s16x4 b) { s16x8 r = __builtin_shufflevector(a, b, 0, 1, 2, 3, 4, 5, 6, 7); return __builtin_bit_cast(bf16x8, r); }
#define AUX_ST 16
typedef __amdgpu_buffer_rsrc_t rsrc_t;
DEV rsrc_t mkrsrc(const void* p) { return __builtin_amdgcn_make_buffer_rsrc((void*)p, 0, 0x7fffffff, 0x00020000); }
DEV void st16(rsrc_t r, unsigned v, unsigned s, u32x4 d) { __builtin_amdgcn_raw_buffer_store_b128(d, r, v, s, AUX_ST); }
DEV void st8(rsrc_t r, unsigned v, unsigned s, u32x2 d) { __builtin_amdgcn_raw_buffer_store_b64(d, r, v, s, AUX_ST); }
DEV void ln_scalars(float sum, float sq, float& mean, float& rinv) {
  float m = sum * (1.0f / D_);
  float v = fmaxf(sq * (1.0f / D_) - m * m, 0.0f);
  float sd = rbf(__builtin_sqrtf(rbf(rbf(v) + 1e-5f)));
  mean = rbf(m);
  rinv = 1.0f / sd;
}
DEV void dma1(unsigned voff, const void* sbase, unsigned dst) {
  asm volatile("s_mov_b32 m0, %2\n\ts_nop 0\n\tglobal_load_lds_dwordx4 %0, %1" ::"v"(voff), "s"(sbase), "s"(dst) : "memory");
}
DEV void dma1m(unsigned voff, const void* sbase, unsigned dst, u64 mask) {
  asm volatile("s_mov_b32 m0, %2\n\ts_mov_b64 exec, %3\n\tglobal_load_lds_dwordx4 %0, %1\n\ts_mov_b64 exec, -1" ::"v"(voff), "s"(sbase), "s"(dst), "s"(mask) : "memory");
}
#define ST_DECL
#define STAMP(i)
#define ST_OUT(wsp, k)
#define WAITV(n) asm volatile("s_waitcnt vmcnt(" #n ")" ::: "memory")
#define WAITL0() asm volatile("s_waitcnt lgkmcnt(0)" ::: "memory")
#define BAR() do { asm volatile("" ::: "memory"); __builtin_amdgcn_s_barrier(); asm volatile("" ::: "memory"); } while (0)

constexpr int K1_LDS = 4 * 32768 + 2048;
template <bool TR>
DEV void k1_body(const char* xs, const char* ws, char* wsp, int rb, int cb) {
  extern __shared__ __attribute__((aligned(1024))) char lds_raw[];
  const unsigned lds0 = (unsigned)(size_t)(LAS char*)lds_raw;
  const int tid = threadIdx.x, lane = tid & 63, wv = __builtin_amdgcn_readfirstlane(tid >> 6);
  const int wm = wv >> 1, wn = wv & 1, h = lane >> 5, rho = lane & 31, sw = swap23(rho);
  ST_DECL STAMP(0);
  unsigned vx[4], vw[4];
  {
    int r = 8 * wv + (lane >> 3);
    unsigned o = r * 1280 + (((lane & 7) ^ ((r >> 1) & 7)) << 4);
    int kr = 4 * wv + (lane >> 4);
    unsigned ow = kr * 3840 + (((lane & 15) ^ ((lane >> 4) << 2)) << 4);
#pragma unroll
    for (int n = 0; n < 4; ++n) { vx[n] = o + n * 32 * 1280; vw[n] = ow + n * 16 * 3840; }
  }
  const unsigned dst0 = lds0 + wv * 1024;
#pragma unroll
  for (int s = 0; s < 3; ++s) {
#pragma unroll
    for (int n = 0; n < 4; ++n) { dma1(vx[n], xs + s * 128, dst0 + s * 32768 + n * 4096); dma1(vw[n], ws + s * 64 * 3840, dst0 + s * 32768 + 16384 + n * 4096); }
  }
  unsigned ax[2][4], aw[2];
#pragma unroll
  for (int t = 0; t < 2; ++t) {
    int row = wm * 64 + ((t ^ wn) << 5) + sw;
#pragma unroll
    for (int ks = 0; ks < 4; ++ks) ax[t][ks] = lds0 + row * 128 + (((2 * ks + h) ^ ((row >> 1) & 7)) << 4);
    int q = (lane & 15) >> 2, p = lane & 3, blk = (lane >> 4) & 1;
    int ch = wn * 8 + ((t ^ wm) << 2) + 2 * blk + (p & 1);
    aw[t] = lds0 + 16384 + (8 * h + q) * 256 + ((ch ^ (q << 2)) << 4) + 8 * (p >> 1);
  }
  bf16x8 sel;
  { __bf16 one = (__bf16)((((lane >> 4) ^ (lane >> 2)) & 1) ? 0.0f : 1.0f);
#pragma unroll
    for (int j = 0; j < 8; ++j) sel[j] = one; }
  f32x16 acc[2][2] = {};
  f32x4 sx = {}, sww = {};
  float q0 = 0.f, q1 = 0.f;
  STAMP(1);
  WAITV(16); BAR();
  STAMP(2);
  unsigned slot = 0;
  bf16x8 fx[2], fw[2];
#pragma unroll
  for (int t = 0; t < 2; ++t) { fx[t] = lds_b128(ax[t][0]); fw[t] = cat(lds_tr(aw[t]), lds_tr(aw[t] + 1024)); }
#pragma unroll 1
  for (int s = 0; s < 10; ++s) {
    const int st = s + 3 < 10 ? s + 3 : 9;
    const u64 mask = s + 3 < 10 ? ~0ull : 1ull;
    const char* xg = xs + st * 128;
    const char* wg = ws + st * (64 * 3840);
    const unsigned dst = dst0 + ((s + 3) & 3) * 32768;
    const unsigned nslot = ((s + 1) & 3) * 32768;
#pragma unroll
    for (int ks = 0; ks < 4; ++ks) {
      bf16x8 nx[2], nw[2];
      if (ks == 3) { WAITV(16); WAITL0(); BAR(); }
      SCHED();
#pragma unroll
      for (int t = 0; t < 2; ++t) {
        if (ks < 3) { nx[t] = lds_b128(ax[t][ks + 1] + slot); nw[t] = cat(lds_tr(aw[t] + slot + (ks + 1) * 4096), lds_tr(aw[t] + slot + (ks + 1) * 4096 + 1024)); }
        else { nx[t] = lds_b128(ax[t][0] + nslot); nw[t] = cat(lds_tr(aw[t] + nslot), lds_tr(aw[t] + nslot + 1024)); }
      }
      u32x4 xu = __builtin_bit_cast(u32x4, fx[0]);
#pragma unroll
      for (int i = 0; i < 4; ++i) {
        const int tm = i >> 1, tn = i & 1;
        if (TR) acc[tm][tn] = __builtin_amdgcn_mfma_f32_32x32x16_bf16(fw[tn], fx[tm], acc[tm][tn], 0, 0, 0);
        else acc[tm][tn] = __builtin_amdgcn_mfma_f32_32x32x16_bf16(fx[tm], fw[tn], acc[tm][tn], 0, 0, 0);
        float lo = bflo(xu[i]), hi = bfhi(xu[i]);
        q0 = fmaf(lo, lo, q0); q1 = fmaf(hi, hi, q1);
        asm volatile("" : "+v"(q0), "+v"(q1));
        if (ks < 3 && i < 3) {
          const int j = ks * 3 + i;
          if (j < 8) { if (j & 1) dma1m(vw[j >> 1], wg, dst + 16384 + (j >> 1) * 4096, mask); else dma1m(vx[j >> 1], xg, dst + (j >> 1) * 4096, mask); }
        }
        SCHED();
      }
      sx = __builtin_amdgcn_mfma_f32_16x16x32_bf16(sel, fx[0], sx, 0, 0, 0);
      sww = __builtin_amdgcn_mfma_f32_16x16x32_bf16(sel, fw[0], sww, 0, 0, 0);
#pragma unroll
      for (int t = 0; t < 2; ++t) { fx[t] = nx[t]; fw[t] = nw[t]; }
    }
    slot = nslot;
  }
  STAMP(3);
  LAS float* stf = (LAS float*)(lds0 + 4 * 32768);
  {
    float sq = q0 + q1;
    sq += __shfl_xor(sq, 32);
    float mean, rinv;
    ln_scalars(sx[0], sq, mean, rinv);
    if (h == 0) {
      int row = wm * 64 + (wn << 5) + sw;
      stf[row] = rinv; stf[128 + row] = mean * rinv;
      stf[256 + wn * 64 + (wm << 5) + sw] = sww[0];
    }
  }
  WAITL0(); BAR();
  STAMP(4);
  const rsrc_t wr = mkrsrc(wsp);
  if (TR) {
    const bool isq = cb < 5;
    const float sc = isq ? 0.32259580f : 1.0f;
    const unsigned base = isq ? WS_Q : WS_KV;
    const int tstride = isq ? QT : KVT;
    const int cc0 = (isq ? cb : cb - 5) * 16 + wn * 8 + h;
#pragma unroll
    for (int tm = 0; tm < 2; ++tm) {
      const int row = wm * 64 + ((tm ^ wn) << 5) + sw;
      const float rinv = stf[row] * sc, mr = stf[128 + row] * sc;
      const int tile = rb * 4 + wm * 2 + (tm ^ wn);
#pragma unroll
      for (int tn = 0; tn < 2; ++tn)
#pragma unroll
        for (int s = 0; s < 2; ++s) {
          const int cl = ((tn ^ wm) << 2) + 2 * s;
          const LAS f32x4* cs = (const LAS f32x4*)(stf + 256 + wn * 64 + (cl + h) * 8);
          const f32x4 c0 = cs[0], c1 = cs[1];
          u32x4 o;
#pragma unroll
          for (int j = 0; j < 2; ++j) {
            o[j] = pack2(acc[tm][tn][8 * s + 2 * j] * rinv - mr * c0[2 * j], acc[tm][tn][8 * s + 2 * j + 1] * rinv - mr * c0[2 * j + 1]);
            o[2 + j] = pack2(acc[tm][tn][8 * s + 4 + 2 * j] * rinv - mr * c1[2 * j], acc[tm][tn][8 * s + 5 + 2 * j] * rinv - mr * c1[2 * j + 1]);
          }
          const int cc = cc0 + cl, p = (cc * 205) >> 10, r = cc - 5 * p;
          if (r == 2) {
            const unsigned a = (2 * p * 64 + tile) * tstride + 1024 + sw * 8;
            st8(wr, a, base, u32x2{o[0], o[1]});
            st8(wr, a, base + 64 * tstride, u32x2{o[2], o[3]});
          } else {
            const int odd = r > 2, rr = r - 3 * odd;
            st16(wr, ((2 * p + odd) * 64 + tile) * tstride + rr * 512 + sw * 16, base, o);
          }
        }
    }
  } else {
#pragma unroll
    for (int tn = 0; tn < 2; ++tn) {
      const int cl = wn * 64 + ((tn ^ wm) << 5) + sw;
      const float cs = stf[256 + cl];
      const int f = (cb - 10) * 128 + cl, head = (f * 3277) >> 16, d = f - head * 20;
      const int dp = (head & 1) ? (d >= 4 ? d - 4 : d + 16) : d;
#pragma unroll
      for (int tm = 0; tm < 2; ++tm) {
        const int tile = rb * 4 + wm * 2 + (tm ^ wn);
        const unsigned a = (head * 64 + tile) * KVT + QT + (h * 21 + dp) * 16;
#pragma unroll
        for (int s = 0; s < 2; ++s) {
          const int row = wm * 64 + ((tm ^ wn) << 5) + 16 * s + 8 * h;
          const LAS f32x4* rp = (const LAS f32x4*)(stf + row);
          const f32x4 r0 = rp[0], r1 = rp[1], m0 = rp[32], m1 = rp[33];
          u32x4 o;
#pragma unroll
          for (int j = 0; j < 2; ++j) {
            o[j] = pack2(acc[tm][tn][8 * s + 2 * j] * r0[2 * j] - m0[2 * j] * cs, acc[tm][tn][8 * s + 2 * j + 1] * r0[2 * j + 1] - m0[2 * j + 1] * cs);
            o[2 + j] = pack2(acc[tm][tn][8 * s + 4 + 2 * j] * r1[2 * j] - m1[2 * j] * cs, acc[tm][tn][8 * s + 5 + 2 * j] * r1[2 * j + 1] - m1[2 * j + 1] * cs);
          }
          st16(wr, a + s * 42 * 16, WS_KV, o);
          const unsigned one = h ? 0u : 0x3f803f80u;
          if (dp == 0) st16(wr, a + s * 42 * 16 + 320, WS_KV, u32x4{one, one, one, one});
        }
      }
    }
  }
  WAITV(0);
  ST_OUT(wsp, 0);
}

__global__ __launch_bounds__(256, 1) void k1_qkv(const char* __restrict__ x, const char* __restrict__ w, char* __restrict__ ws) {
  const int b = blockIdx.x, t = (b & 7) * 30 + (b >> 3), half = t >= 120, u = t - 120 * half;
  const int cb = u >> 3, rb = half * 8 + (u & 7);
  const char* xs = x + (size_t)rb * 128 * 1280;
  const char* wsrc = w + cb * 256;
  if (cb < 10) k1_body<true>(xs, wsrc, ws, rb, cb);
  else k1_body<false>(xs, wsrc, ws, rb, cb);
}

constexpr int PH = 16 * KVT;
constexpr int K2_LDS = 3 * PH + 3 * 5120 + 5 * 4096;

DEV f32x16 qk(bf16x8 k0, s16x4 kt, bf16x8 q0, s16x4 qt) {
  f32x16 z = {};
  f32x16 s = __builtin_amdgcn_mfma_f32_32x32x16_bf16(k0, q0, z, 0, 0, 0);
  return __builtin_amdgcn_mfma_f32_32x32x8bf16_1k(kt, qt, s, 0, 0, 0);
}
DEV bf16x16 phalf(f32x16 s, int i) {
  f32x8 r = i ? __builtin_shufflevector(s, s, 8, 9, 10, 11, 12, 13, 14, 15) : __builtin_shufflevector(s, s, 0, 1, 2, 3, 4, 5, 6, 7);
  return __builtin_bit_cast(bf16x16, r);
}

#define R_SA 32
#define R_SB 48
#define R_O 64
#define R_K0 80
#define R_VA 84
#define R_VB 88
#define R_V1 92
#define R_Q0 96
#define R_KT 100
#define R_QT 102
#define R_PH 104
#define STR_(x) #x
#define STR(x) STR_(x)
#define VR(b, lo, hi) "v[" STR(b) "+" #lo ":" STR(b) "+" #hi "]"
#define EX(X, i) "v_exp_f32 v[" STR(X) "+" #i "], v[" STR(X) "+" #i "]\n"
#define EH(X, i) "v_exp_f32 v[" STR(R_PH) "+" #i "], v[" STR(X) "+8+" #i "]\n"
#define TILE(X, Y, VC, VN, t, AK, AT, tk, AV, tv) \
  "s_waitcnt lgkmcnt(0)\n" \
  "v_mfma_f32_32x32x16_bf16 " VR(Y, 0, 15) ", " VR(R_K0, 0, 3) ", " VR(R_Q0, 0, 3) ", 0\n" \
  "ds_read_b128 " VR(R_K0, 0, 3) ", %[" #AK "] offset:" #tk "*2624\n" \
  EX(X, 0) EX(X, 1) EX(X, 2) EX(X, 3) \
  "v_mfma_f32_32x32x8_bf16 " VR(Y, 0, 15) ", " VR(R_KT, 0, 1) ", " VR(R_QT, 0, 1) ", " VR(Y, 0, 15) "\n" \
  "ds_read_b64 " VR(R_KT, 0, 1) ", %[" #AT "] offset:" #tk "*2624\n" \
  EX(X, 4) EX(X, 5) EX(X, 6) EX(X, 7) \
  "v_smfmac_f32_32x32x32_bf16 " VR(R_O, 0, 15) ", " VR(R_V1, 0, 3) ", " VR(R_PH, 0, 7) ", %[idx]\n" \
  "ds_read_b128 " VR(R_V1, 0, 3) ", %[av] offset:" #t "*2624+672\n" \
  "ds_read_b128 " VR(VN, 0, 3) ", %[" #AV "] offset:" #tv "*2624\n" \
  EH(X, 0) EH(X, 1) EH(X, 2) EH(X, 3) \
  "v_smfmac_f32_32x32x32_bf16 " VR(R_O, 0, 15) ", " VR(VC, 0, 3) ", " VR(X, 0, 7) ", %[idx]\n" \
  EH(X, 4) EH(X, 5) EH(X, 6) EH(X, 7)
#define TILE2(t0, t1, t2, t3) TILE(R_SA, R_SB, R_VA, R_VB, t0, ak, at, t2, av, t1) TILE(R_SB, R_SA, R_VB, R_VA, t1, ak, at, t3, av, t2)
#define FIX(b, n) "{v[" STR(b) ":" STR(n) "]}"
template <int N> DEV void dman(unsigned voff, const void* sbase, unsigned dst) {
  if (N == 1) asm volatile("s_mov_b32 m0, %2\n\ts_nop 0\n\tglobal_load_lds_dwordx4 %0, %1" ::"v"(voff), "s"(sbase), "s"(dst) : "memory");
  if (N == 2) asm volatile("s_mov_b32 m0, %2\n\ts_nop 0\n\tglobal_load_lds_dwordx4 %0, %1\n\tglobal_load_lds_dwordx4 %0, %1 offset:1024" ::"v"(voff), "s"(sbase), "s"(dst) : "memory");
  if (N == 3) asm volatile("s_mov_b32 m0, %2\n\ts_nop 0\n\tglobal_load_lds_dwordx4 %0, %1\n\tglobal_load_lds_dwordx4 %0, %1 offset:1024\n\t"
                           "global_load_lds_dwordx4 %0, %1 offset:2048" ::"v"(voff), "s"(sbase), "s"(dst) : "memory");
  if (N == 4) asm volatile("s_mov_b32 m0, %2\n\ts_nop 0\n\tglobal_load_lds_dwordx4 %0, %1\n\tglobal_load_lds_dwordx4 %0, %1 offset:1024\n\t"
                           "global_load_lds_dwordx4 %0, %1 offset:2048\n\tglobal_load_lds_dwordx4 %0, %1 offset:3072" ::"v"(voff), "s"(sbase), "s"(dst) : "memory");
}

DEV void k2_loader(const char* wo, const char* wg, const char* wu, const char* wd, char* ws, const char* kv, unsigned lds0, int lane, int b) {
  const unsigned v16 = lane * 16, tl = lds0 + 3 * PH;
  __builtin_amdgcn_s_setprio(3);
  BAR();
#pragma unroll 1
  for (int i = 0; i < 19; ++i) { const int c = (i < 18 ? 48 + 4 * i : 119) * 1024; dman<4>(v16, kv + c, lds0 + c); }
  WAITV(0); BAR();
#pragma unroll 1
  for (int i = 0; i < 11; ++i) { const int c = (i < 10 ? 4 * i : 37) * 1024; dman<4>(v16, kv + 3 * PH + c, lds0 + c); }
  const int na = b < 80 ? 3 : 2, ua = b * 2 + (b < 80 ? b : 80);
  const int nb = b < 56 ? 5 : 4, ub = b * 4 + (b < 56 ? b : 56);
  unsigned vo[5];
#pragma unroll
  for (int n = 0; n < 5; ++n) { int c = lane + 64 * n, r = (c * 205) >> 11; vo[n] = r * 1280 + (c - 10 * r) * 16; }
#pragma unroll 1
  for (int i = 0; i < na; ++i) {
    const int unit = ua + i, isd = unit >= 160, u = unit - 160 * isd;
    const char* src = (isd ? wd : wo) + (size_t)(u >> 3) * 32 * 1280 + (u & 7) * 160;
#pragma unroll
    for (int n = 0; n < 5; ++n) dma1(vo[n], src, tl + i * 5120 + n * 1024);
  }
  const unsigned vg = (lane >> 3) * (F_ * 2) + (lane & 7) * 16;
#pragma unroll 1
  for (int i = 0; i < nb; ++i) {
    const int unit = ub + i, ks = (unit * 2428) >> 16, fg = unit - 27 * ks;
    const size_t so = (size_t)ks * 16 * (F_ * 2) + fg * 128;
#pragma unroll
    for (int n = 0; n < 4; ++n) dma1(vg, ((n >> 1) ? wu : wg) + so + (n & 1) * 8 * (F_ * 2), tl + 15360 + i * 4096 + n * 1024);
  }
  WAITV(26); BAR();
  WAITV(0);
  const rsrc_t wr = mkrsrc(ws);
  const int q = (lane & 15) >> 2, pp = lane & 3;
#pragma unroll 1
  for (int i = 0; i < na; ++i) {
    const int unit = ua + i, isd = unit >= 160, u = unit - 160 * isd;
    const unsigned dst = isd ? WS_WD + ((u & 7) * 54 + (u >> 3)) * 5120 : WS_WO + ((u & 7) * 20 + (u >> 3)) * 5120;
    const unsigned a = tl + i * 5120 + (8 * (lane >> 4) + q) * 160;
#pragma unroll
    for (int T = 0; T < 5; ++T) {
      const unsigned c = a + (T < 4 ? (T >> 1) * 64 + (T & 1) * 8 + pp * 16 : 128 + pp * 8);
      st16(wr, v16 + (T & 3) * 1024, dst + (T >> 2) * 4096, __builtin_bit_cast(u32x4, cat(lds_tr(c), lds_tr(c + 640))));
    }
  }
#pragma unroll 1
  for (int i = 0; i < nb; ++i) {
    const unsigned a = tl + 15360 + i * 4096 + ((lane >> 4) & 1) * 2048 + (8 * (lane >> 5) + q) * 128 + (pp & 1) * 16 + (pp >> 1) * 8;
#pragma unroll
    for (int T = 0; T < 4; ++T) st16(wr, v16 + T * 1024, WS_WGU + (ub + i) * 4096, __builtin_bit_cast(u32x4, cat(lds_tr(a + T * 32), lds_tr(a + T * 32 + 512))));
  }
}

__global__ __launch_bounds__(576) void k2_attn(const char* __restrict__ wo, const char* __restrict__ wg, const char* __restrict__ wu,
                                               const char* __restrict__ wd, char* __restrict__ ws) {
  extern __shared__ __attribute__((aligned(1024))) char lds_raw[];
  const unsigned lds0 = (unsigned)(size_t)(LAS char*)lds_raw;
  const int tid = threadIdx.x, lane = tid & 63, wv = __builtin_amdgcn_readfirstlane(tid >> 6);
  const int h = lane >> 5, rho = lane & 31, sw = swap23(rho);
  ST_DECL STAMP(0);
  const int b = blockIdx.x, head = (b & 7) * 4 + ((b >> 3) & 3), qtile = (b >> 5) * 8 + wv;
  const char* kv = ws + WS_KV + (size_t)head * 64 * KVT;
  if (wv == 8) { k2_loader(wo, wg, wu, wd, ws, kv, lds0, lane, b); return; }
  const char* qp = ws + WS_Q + (size_t)(head * 64 + qtile) * QT;
  const unsigned v16 = lane * 16;
  bf16x8 q0;
  s16x4 qt;
  asm volatile("global_load_dwordx4 %0, %2, %4\n\tv_mov_b32 v[" STR(R_QT) "], 0\n\tv_mov_b32 v[" STR(R_QT) "+1], 0\n\ts_mov_b32 exec_hi, 0\n\t"
               "global_load_dwordx2 %1, %3, %4 offset:1024\n\ts_mov_b32 exec_hi, -1"
               : "=&" FIX(R_Q0, 99)(q0), "=&" FIX(R_QT, 103)(qt) : "v"(v16), "v"(lane * 8), "s"(qp) : "memory");
  dman<4>(v16, kv + wv * 4096, lds0 + wv * 4096);
  dman<2>(v16, kv + 32768 + wv * 2048, lds0 + 32768 + wv * 2048);
  int idx = 0xDDDD;
  unsigned ak = lds0 + h * 512 + rho * 16, at = lds0 + (h ? QT + 41 * 16 : 1024 + rho * 8);
  unsigned av = lds0 + QT + (sw < 20 ? h * 21 + sw : (sw & 7) == 4 ? 20 : 41) * 16;
  const rsrc_t wr = mkrsrc(ws);
  f32x16 o = {}, sb;
  f32x8 ph = {};
  bf16x8 vb, v1 = {};
  asm volatile("" : "+" FIX(R_O, 79)(o), "+" FIX(R_PH, 111)(ph), "+" FIX(R_V1, 95)(v1), "+v"(ak), "+v"(at), "+v"(av), "+v"(idx));
  asm volatile("s_waitcnt vmcnt(0)" : "+" FIX(R_Q0, 99)(q0), "+" FIX(R_QT, 103)(qt) :: "memory");
  BAR();
  STAMP(1);
  f32x16 sa = qk(lds_b128(ak), *(const LAS s16x4*)at, q0, qt);
  bf16x8 k0 = lds_b128(ak + KVT), va = lds_b128(av);
  s16x4 kt = *(const LAS s16x4*)(at + KVT);
  asm volatile("s_nop 7\n\ts_nop 3" : "+" FIX(R_SA, 47)(sa));
  unsigned slot = 0;
#pragma unroll 1
  for (int p = 0; p < 4; ++p) {
    const unsigned nslot = p >= 2 ? 0 : slot + PH;
    if (p == 1 || p == 2) BAR();
    asm volatile("s_setprio 3\n" TILE2(0, 1, 2, 3) TILE2(2, 3, 4, 5) TILE2(4, 5, 6, 7) TILE2(6, 7, 8, 9) TILE2(8, 9, 10, 11) "s_setprio 2\n" TILE2(10, 11, 12, 13) TILE2(12, 13, 14, 15)
                 "s_setprio 1\n" TILE(R_SA, R_SB, R_VA, R_VB, 14, nk, nt, 0, av, 15) "s_setprio 0\n" TILE(R_SB, R_SA, R_VB, R_VA, 15, nk, nt, 1, nv, 0)
                 "s_waitcnt lgkmcnt(0)"
                 : "+" FIX(R_SA, 47)(sa), "=&" FIX(R_SB, 63)(sb), "+" FIX(R_O, 79)(o), "+" FIX(R_K0, 83)(k0), "+" FIX(R_VA, 87)(va), "=&" FIX(R_VB, 91)(vb),
                   "+" FIX(R_V1, 95)(v1), "+" FIX(R_KT, 101)(kt), "+" FIX(R_PH, 111)(ph)
                 : FIX(R_Q0, 99)(q0), FIX(R_QT, 103)(qt), [idx] "v"(idx), [ak] "v"(ak + slot), [at] "v"(at + slot), [av] "v"(av + slot),
                   [nk] "v"(ak + nslot), [nt] "v"(at + nslot), [nv] "v"(av + nslot)
                 : "memory");
    slot = nslot;
  }
  asm volatile("s_nop 1\n\tv_smfmac_f32_32x32x32_bf16 " VR(R_O, 0, 15) ", " VR(R_V1, 0, 3) ", " VR(R_PH, 0, 7) ", %[idx]\n\ts_nop 7\n\ts_nop 3"
               : "+" FIX(R_O, 79)(o) : FIX(R_PH, 111)(ph), FIX(R_V1, 95)(v1), [idx] "v"(idx));
  STAMP(3);
  {
    float mx = 0.f;
#pragma unroll
    for (int i = 0; i < 12; ++i) mx = fmaxf(mx, fabsf(o[i]));
    float chk = 0.f;
#pragma unroll
    for (int i = 0; i < 12; ++i) chk += o[i] * 0.0f;
    const float l = o[12];
    const bool ok = (l >= 5.421010862e-20f) && (l <= 1.8446744e19f) && (chk == 0.f) && (mx <= 3.0e38f);
    if (__builtin_amdgcn_ballot_w64(!ok) != 0) {
      float m = -3.0e38f;
      o = f32x16{};
      const char* g = kv;
#pragma unroll 1
      for (int t = 0; t < 64; ++t, g += KVT) {
        const bf16x8 k0 = *(const bf16x8*)(g + (ak - lds0));
        const s16x4 kt = *(const s16x4*)(g + (at - lds0));
        const bf16x8 w0 = *(const bf16x8*)(g + (av - lds0)), w1 = *(const bf16x8*)(g + (av - lds0) + 42 * 16);
        f32x16 s = qk(k0, kt, q0, qt);
        float mn = m;
#pragma unroll
        for (int i = 0; i < 16; ++i) mn = fmaxf(mn, s[i]);
        mn = fmaxf(mn, __shfl_xor(mn, 32));
        const float alpha = __builtin_amdgcn_exp2f(m - mn);
        m = mn;
#pragma unroll
        for (int i = 0; i < 16; ++i) { s[i] = __builtin_amdgcn_exp2f(s[i] - mn); o[i] *= alpha; }
        o = __builtin_amdgcn_smfmac_f32_32x32x32_bf16(w0, phalf(s, 0), o, idx, 0, 0);
        o = __builtin_amdgcn_smfmac_f32_32x32x32_bf16(w1, phalf(s, 1), o, idx, 0, 0);
      }
    }
  }
  const float il = 1.0f / o[12];
  const int tok = qtile * 32 + rho, odd = head & 1;
  const unsigned ob = (tok >> 4) * 20480 + (tok & 15) * 16;
  const int cc = (head >> 1) * 5 + 3 * odd + h, c2 = (head >> 1) * 5 + 2;
  st16(wr, ob + cc * 256, WS_O, u32x4{pack2(o[0] * il, o[1] * il), pack2(o[2] * il, o[3] * il), pack2(o[4] * il, o[5] * il), pack2(o[6] * il, o[7] * il)});
  if (h == 0) st8(wr, ob, WS_O + c2 * 256 + odd * 8, u32x2{pack2(o[8] * il, o[9] * il), pack2(o[10] * il, o[11] * il)});
  ST_OUT(ws, 1);
}

constexpr int KS_LDS = 4 * 3 * 5 * 1024;
template <int NK, int DEPTH>
DEV void ksplit_gemm(const char* act, const char* wt, int wv, int lane, f32x4 (&res)[5]
#define ST_ARG
) {
  extern __shared__ __attribute__((aligned(1024))) char lds_raw[];
  const unsigned lds0 = (unsigned)(size_t)(LAS char*)lds_raw;
  constexpr int NI = (NK + 3) / 4;
  const char* ap[4];
#pragma unroll
  for (int m = 0; m < 4; ++m) ap[m] = act + (size_t)((m ^ wv) * NK + wv) * 1024;
  const char* wp = wt + wv * 5120;
  const unsigned v16 = lane * 16;
  const bool last = (NI - 1) * 4 + wv < NK;
  bf16x8 fa[NI][4], fw[NI][5];
  f32x4 acc[4][5] = {};
#pragma unroll
  for (int i = 0; i < NI + DEPTH; ++i) {
    if (i < NI) {
      const int ii = (i == NI - 1 && NK % 4) ? (last ? i : i - 1) : i;
#pragma unroll
      for (int m = 0; m < 4; ++m) {
        fa[i][m] = *(const bf16x8*)(ap[m] + (size_t)ii * 4096 + v16);
        fw[i][m] = *(const bf16x8*)(wp + (size_t)ii * 20480 + m * 1024 + v16);
      }
      fw[i][4] = *(const bf16x8*)(wp + (size_t)ii * 20480 + 4096 + v16);
    }
    SCHED();
    const int c = i - DEPTH;
    if (c == -1) STAMP(1);
    if (c == 1) STAMP(2);
    if (c >= 0 && (c < NI - 1 || NK % 4 == 0 || last)) {
#pragma unroll
      for (int m = 0; m < 4; ++m)
#pragma unroll
        for (int T = 0; T < 5; ++T) acc[m][T] = __builtin_amdgcn_mfma_f32_16x16x32_bf16(fw[c][T], fa[c][m], acc[m][T], 0, 0, 0);
    }
    SCHED();
  }
  STAMP(3);
#pragma unroll
  for (int m = 1; m < 4; ++m)
#pragma unroll
    for (int T = 0; T < 5; ++T) *(LAS f32x4*)(lds0 + (((m ^ wv) * 3 + m - 1) * 5 + T) * 1024 + v16) = acc[m][T];
  WAITL0(); BAR();
  STAMP(4);
#pragma unroll
  for (int T = 0; T < 5; ++T) {
    f32x4 r = acc[0][T];
#pragma unroll
    for (int m = 0; m < 3; ++m) r += *(const LAS f32x4*)(lds0 + ((wv * 3 + m) * 5 + T) * 1024 + v16);
    res[T] = r;
  }
}
DEV unsigned h_off(int tok, int chunk) { return (tok >> 5) * 40960 + chunk * 512 + (tok & 31) * 16; }
DEV unsigned addres(unsigned r, float a, float b) { return pack2(bflo(r) + rbf(a), bfhi(r) + rbf(b)); }

__global__ __launch_bounds__(256, 1) void k3_wo(const char* __restrict__ x, char* __restrict__ ws) {
  const int tid = threadIdx.x, lane = tid & 63, wv = __builtin_amdgcn_readfirstlane(tid >> 6);
  ST_DECL STAMP(0);
  const int b = blockIdx.x, xc = b & 7, j = b >> 3, rb = (xc >> 1) * 8 + (j & 7), cb = (xc & 1) * 4 + (j >> 3);
  const int tok = rb * 64 + wv * 16 + (lane & 15), g = lane >> 4;
  const char* xr = x + (size_t)tok * 1280 + cb * 160;
  const u32x4 r0 = *(const u32x4*)(xr + g * 16), r1 = *(const u32x4*)(xr + 64 + g * 16);
  const u32x2 r2 = *(const u32x2*)(xr + 128 + g * 8);
  f32x4 res[5];
  ksplit_gemm<20, 5>(ws + WS_O + (size_t)rb * 4 * 20480, ws + WS_WO + (size_t)cb * 20 * 5120, wv, lane, res ST_ARG);
  unsigned o[10];
#pragma unroll
  for (int i = 0; i < 4; ++i) {
    o[i] = addres(r0[i], res[i >> 1][2 * (i & 1)], res[i >> 1][2 * (i & 1) + 1]);
    o[4 + i] = addres(r1[i], res[2 + (i >> 1)][2 * (i & 1)], res[2 + (i >> 1)][2 * (i & 1) + 1]);
  }
  o[8] = addres(r2[0], res[4][0], res[4][1]); o[9] = addres(r2[1], res[4][2], res[4][3]);
  const rsrc_t wr = mkrsrc(ws);
  st16(wr, h_off(tok, cb * 10 + g), WS_H, u32x4{o[0], o[1], o[2], o[3]});
  st16(wr, h_off(tok, cb * 10 + 4 + g), WS_H, u32x4{o[4], o[5], o[6], o[7]});
  st8(wr, h_off(tok, cb * 10 + 8 + (g >> 1)) + (g & 1) * 8, WS_H, u32x2{o[8], o[9]});
  float sm = 0.f, sq = 0.f;
#pragma unroll
  for (int i = 0; i < 10; ++i) { float a = bflo(o[i]), c = bfhi(o[i]); sm += a + c; sq = fmaf(a, a, fmaf(c, c, sq)); }
  sm += __shfl_xor(sm, 16); sq += __shfl_xor(sq, 16);
  sm += __shfl_xor(sm, 32); sq += __shfl_xor(sq, 32);
  if (g == 0) st8(wr, tok * 64 + cb * 8, WS_ST, __builtin_bit_cast(u32x2, f32x2{sm, sq}));
  ST_OUT(ws, 2);
}

__global__ __launch_bounds__(256, 1) void k5_down(char* __restrict__ ws, char* __restrict__ out) {
  const int tid = threadIdx.x, lane = tid & 63, wv = __builtin_amdgcn_readfirstlane(tid >> 6);
  ST_DECL STAMP(0);
  const int b = blockIdx.x, xc = b & 7, j = b >> 3, rb = (xc >> 1) * 8 + (j & 7), cb = (xc & 1) * 4 + (j >> 3);
  const int tok = rb * 64 + wv * 16 + (lane & 15), g = lane >> 4;
  const char* hb = ws + WS_H;
  const u32x4 r0 = *(const u32x4*)(hb + h_off(tok, cb * 10 + g)), r1 = *(const u32x4*)(hb + h_off(tok, cb * 10 + 4 + g));
  const u32x2 r2 = *(const u32x2*)(hb + h_off(tok, cb * 10 + 8 + (g >> 1)) + (g & 1) * 8);
  f32x4 res[5];
  ksplit_gemm<54, 5>(ws + WS_A + (size_t)rb * 4 * 54 * 1024, ws + WS_WD + (size_t)cb * 54 * 5120, wv, lane, res ST_ARG);
  unsigned o[10];
#pragma unroll
  for (int i = 0; i < 4; ++i) {
    o[i] = addres(r0[i], res[i >> 1][2 * (i & 1)], res[i >> 1][2 * (i & 1) + 1]);
    o[4 + i] = addres(r1[i], res[2 + (i >> 1)][2 * (i & 1)], res[2 + (i >> 1)][2 * (i & 1) + 1]);
  }
  o[8] = addres(r2[0], res[4][0], res[4][1]); o[9] = addres(r2[1], res[4][2], res[4][3]);
  const rsrc_t wr = mkrsrc(out);
  st16(wr, tok * 1280 + g * 16, cb * 160, u32x4{o[0], o[1], o[2], o[3]});
  st16(wr, tok * 1280 + 64 + g * 16, cb * 160, u32x4{o[4], o[5], o[6], o[7]});
  st8(wr, tok * 1280 + 128 + g * 8, cb * 160, u32x2{o[8], o[9]});
  ST_OUT(ws, 4);
}

constexpr int K4_ST = 28672 + 16384;
constexpr int K4_LDS = 3 * K4_ST;
DEV void dma4m(unsigned voff, const void* sbase, unsigned dst, u64 mask) {
  asm volatile("s_mov_b32 m0, %2\n\ts_mov_b64 exec, %3\n\tglobal_load_lds_dwordx4 %0, %1\n\tglobal_load_lds_dwordx4 %0, %1 offset:1024\n\t"
               "global_load_lds_dwordx4 %0, %1 offset:2048\n\tglobal_load_lds_dwordx4 %0, %1 offset:3072\n\ts_mov_b64 exec, -1" ::"v"(voff), "s"(sbase), "s"(dst), "s"(mask) : "memory");
}
DEV bf16x8 ln_frag(bf16x8 f, float rinv, float nmr) {
  u32x4 u = __builtin_bit_cast(u32x4, f), r;
#pragma unroll
  for (int i = 0; i < 4; ++i) r[i] = pack2(fmaf(bflo(u[i]), rinv, nmr), fmaf(bfhi(u[i]), rinv, nmr));
  return __builtin_bit_cast(bf16x8, r);
}
__global__ __launch_bounds__(256, 1) void k4_gu(char* __restrict__ ws) {
  extern __shared__ __attribute__((aligned(1024))) char lds_raw[];
  const unsigned lds0 = (unsigned)(size_t)(LAS char*)lds_raw;
  const int tid = threadIdx.x, lane = tid & 63, wv = __builtin_amdgcn_readfirstlane(tid >> 6);
  const int h = lane >> 5, rho = lane & 31;
  ST_DECL STAMP(0);
  const int b = blockIdx.x, xc = b & 7, j = b >> 3, tb = (xc & 1) * 8 + (j & 7), fb = (xc >> 1) * 4 + (j >> 3);
  const int ft0 = (27 * fb) >> 2, nft = ((27 * (fb + 1)) >> 2) - ft0;
  const int tok = tb * 128 + wv * 32 + rho;
  const unsigned v16 = lane * 16;
  const f32x4* sp = (const f32x4*)(ws + WS_ST + (size_t)tok * 64 + h * 32);
  const f32x4 s0 = sp[0], s1 = sp[1];
  const char* wsrc = ws + WS_WGU + (size_t)(wv * 108 + ft0) * 1024;
  const char* hsrc = ws + WS_H + (size_t)(tb * 4 + wv) * 40960;
  const unsigned dw = lds0 + wv * 7168, dh = lds0 + 28672 + wv * 4096;
#pragma unroll
  for (int s = 0; s < 2; ++s) {
#pragma unroll
    for (int n = 0; n < 4; ++n) dma1(v16, hsrc + s * 4096 + n * 1024, dh + s * K4_ST + n * 1024);
#pragma unroll
    for (int n = 0; n < 7; ++n) dma1(v16, wsrc + s * (432 * 1024) + n * 1024, dw + s * K4_ST + n * 1024);
  }
  float mean, rinv;
  {
    float sm = (s0[0] + s0[2]) + (s1[0] + s1[2]), sq = (s0[1] + s0[3]) + (s1[1] + s1[3]);
    sm += __shfl_xor(sm, 32); sq += __shfl_xor(sq, 32);
    ln_scalars(sm, sq, mean, rinv);
  }
  const float nmr = -mean * rinv;
  const unsigned aw = lds0 + v16, ah = dh + v16;
  f32x16 acc[7] = {};
  STAMP(1);
  WAITV(11); BAR();
  STAMP(2);
  bf16x8 fw[7], fh;
#pragma unroll
  for (int T = 0; T < 7; ++T) fw[T] = lds_b128(aw + T * 1024);
  fh = ln_frag(lds_b128(ah), rinv, nmr);
  unsigned slot = 0;
#pragma unroll 1
  for (int s = 0; s < 10; ++s) {
    const int st = s + 2 < 10 ? s + 2 : 9;
    const u64 mask = s + 2 < 10 ? ~0ull : 1ull;
    const unsigned nslot = slot == 2 * K4_ST ? 0 : slot + K4_ST;
    const unsigned pslot = slot == 0 ? 2 * K4_ST : slot - K4_ST;
    const char* wgp = wsrc + (size_t)st * (432 * 1024);
    const char* hgp = hsrc + st * 4096;
#pragma unroll
    for (int ks = 0; ks < 4; ++ks) {
      if (ks == 3) { WAITV(11); WAITL0(); BAR(); }
      SCHED();
      const unsigned wa = ks < 3 ? aw + slot + (ks + 1) * 7168 : aw + nslot;
      const bf16x8 hraw = lds_b128(ks < 3 ? ah + slot + (ks + 1) * 1024 : ah + nslot);
      const u32x4 hu = __builtin_bit_cast(u32x4, hraw);
      u32x4 hn;
      bf16x8 nw[7];
#pragma unroll
      for (int T = 0; T < 7; ++T) {
        acc[T] = __builtin_amdgcn_mfma_f32_32x32x16_bf16(fw[T], fh, acc[T], 0, 0, 0);
        nw[T] = lds_b128(wa + T * 1024);
        if (T >= 3) hn[T - 3] = pack2(fmaf(bflo(hu[T - 3]), rinv, nmr), fmaf(bfhi(hu[T - 3]), rinv, nmr));
        if (ks < 3 && !(T & 1)) {
          const int jj = ks * 4 + (T >> 1);
          if (jj < 4) dma1m(v16, hgp + jj * 1024, dh + pslot + jj * 1024, mask);
          else if (jj < 11) dma1m(v16, wgp + (jj - 4) * 1024, dw + pslot + (jj - 4) * 1024, mask);
        }
        SCHED();
      }
#pragma unroll
      for (int T = 0; T < 7; ++T) fw[T] = nw[T];
      fh = __builtin_bit_cast(bf16x8, hn);
    }
    slot = nslot;
  }
  STAMP(3);
  const rsrc_t wr = mkrsrc(ws);
  const unsigned ab = (tok >> 4) * (54 * 1024) + (tok & 15) * 16 + h * 256;
#pragma unroll
  for (int T = 0; T < 7; ++T) {
    u32x4 o;
#pragma unroll
    for (int i = 0; i < 4; ++i) o[i] = pack2(fmaxf(acc[T][2 * i], 0.f) * acc[T][8 + 2 * i], fmaxf(acc[T][2 * i + 1], 0.f) * acc[T][9 + 2 * i]);
    if (T < 6 || nft == 7) st16(wr, ab + T * 512, WS_A + ft0 * 512, o);
  }
  WAITV(0);
  ST_OUT(ws, 3);
}

extern "C" void kernel_launch(void* const* d_in, const int* in_sizes, int n_in, void* d_out, int out_size, void* d_ws, size_t ws_size,
                              hipStream_t stream) {
  const char* x = (const char*)d_in[0];
  const char* wqkv = (const char*)d_in[1];
  const char* wo = (const char*)d_in[2];
  const char* wg = (const char*)d_in[4];
  const char* wu = (const char*)d_in[5];
  const char* wd = (const char*)d_in[6];
  char* ws = (char*)d_ws;
  (void)hipFuncSetAttribute((const void*)k1_qkv, hipFuncAttributeMaxDynamicSharedMemorySize, K1_LDS);
  (void)hipFuncSetAttribute((const void*)k2_attn, hipFuncAttributeMaxDynamicSharedMemorySize, K2_LDS);
  (void)hipFuncSetAttribute((const void*)k4_gu, hipFuncAttributeMaxDynamicSharedMemorySize, K4_LDS);
  k1_qkv<<<240, 256, K1_LDS, stream>>>(x, wqkv, ws);
  k2_attn<<<256, 576, K2_LDS, stream>>>(wo, wg, wu, wd, ws);
  k3_wo<<<256, 256, KS_LDS, stream>>>(x, ws);
  k4_gu<<<256, 256, K4_LDS, stream>>>(ws);
  k5_down<<<256, 256, KS_LDS, stream>>>(ws, (char*)d_out);
}
```

```cpp
#include <hip/hip_runtime.h>
#include <hip/hip_bf16.h>

typedef __attribute__((ext_vector_type(8))) __bf16 bf16x8;
typedef __attribute__((ext_vector_type(16))) __bf16 bf16x16;
typedef __attribute__((ext_vector_type(2))) __bf16 bf16x2;
typedef __attribute__((ext_vector_type(4))) short s16x4;
typedef __attribute__((ext_vector_type(8))) short s16x8;
typedef __attribute__((ext_vector_type(16))) float f32x16;
typedef __attribute__((ext_vector_type(8))) float f32x8;
typedef __attribute__((ext_vector_type(4))) float f32x4;
typedef __attribute__((ext_vector_type(2))) float f32x2;
typedef __attribute__((ext_vector_type(4))) unsigned u32x4;
typedef __attribute__((ext_vector_type(2))) unsigned u32x2;
typedef unsigned long long u64;
#define LAS __attribute__((address_space(3)))
#define DEV __device__ __forceinline__
#define SCHED() __builtin_amdgcn_sched_barrier(0)

constexpr int S_ = 2048, D_ = 640, F_ = 1728;
constexpr int QT = 1280;
constexpr int KVT = 2624;
constexpr size_t WS_Q = 0;
constexpr size_t WS_KV = WS_Q + (size_t)32 * 64 * QT;
constexpr size_t WS_O = WS_KV + (size_t)32 * 64 * KVT;
constexpr size_t WS_H = WS_O + (size_t)S_ * D_ * 2;
constexpr size_t WS_ST = WS_H + (size_t)S_ * D_ * 2;
constexpr size_t WS_A = WS_ST + (size_t)S_ * 64;
constexpr size_t WS_WO = WS_A + (size_t)S_ * F_ * 2;
constexpr size_t WS_WD = WS_WO + (size_t)D_ * D_ * 2;
constexpr size_t WS_WGU = WS_WD + (size_t)F_ * D_ * 2;
constexpr size_t WS_END = WS_WGU + (size_t)2 * F_ * D_ * 2;

DEV int swap23(int r) { return (r & 0x13) | ((r & 4) << 1) | ((r & 8) >> 1); }
DEV unsigned pack2(float a, float b) { f32x2 v = {a, b}; bf16x2 r = __builtin_convertvector(v, bf16x2); return __builtin_bit_cast(unsigned, r); }
DEV float bflo(unsigned u) { return __builtin_bit_cast(float, u << 16); }
DEV float bfhi(unsigned u) { return __builtin_bit_cast(float, u & 0xffff0000u); }
DEV float rbf(float a) { return bflo(pack2(a, a)); }
DEV bf16x8 lds_b128(unsigned a) { return *(const LAS bf16x8*)a; }
DEV s16x4 lds_tr(unsigned a) { return __builtin_amdgcn_ds_read_tr16_b64_v4i16((LAS s16x4*)a); }
DEV bf16x8 cat(s16x4 a, s16x4 b) { s16x8 r = __builtin_shufflevector(a, b, 0, 1, 2, 3, 4, 5, 6, 7); return __builtin_bit_cast(bf16x8, r); }
#define AUX_ST 16
typedef __amdgpu_buffer_rsrc_t rsrc_t;
DEV rsrc_t mkrsrc(const void* p) { return __builtin_amdgcn_make_buffer_rsrc((void*)p, 0, 0x7fffffff, 0x00020000); }
DEV void st16(rsrc_t r, unsigned v, unsigned s, u32x4 d) { __builtin_amdgcn_raw_buffer_store_b128(d, r, v, s, AUX_ST); }
DEV void st8(rsrc_t r, unsigned v, unsigned s, u32x2 d) { __builtin_amdgcn_raw_buffer_store_b64(d, r, v, s, AUX_ST); }
DEV void ln_scalars(float sum, float sq, float& mean, float& rinv) {
  float m = sum * (1.0f / D_);
  float v = fmaxf(sq * (1.0f / D_) - m * m, 0.0f);
  float sd = rbf(__builtin_sqrtf(rbf(rbf(v) + 1e-5f)));
  mean = rbf(m);
  rinv = 1.0f / sd;
}
DEV void dma1(unsigned voff, const void* sbase, unsigned dst) {
  asm volatile("s_mov_b32 m0, %2\n\ts_nop 0\n\tglobal_load_lds_dwordx4 %0, %1" ::"v"(voff), "s"(sbase), "s"(dst) : "memory");
}
DEV void dma1m(unsigned voff, const void* sbase, unsigned dst, u64 mask) {
  asm volatile("s_mov_b32 m0, %2\n\ts_mov_b64 exec, %3\n\tglobal_load_lds_dwordx4 %0, %1\n\ts_mov_b64 exec, -1" ::"v"(voff), "s"(sbase), "s"(dst), "s"(mask) : "memory");
}
#define ST_DECL
#define STAMP(i)
#define ST_OUT(wsp, k)
#define WAITV(n) asm volatile("s_waitcnt vmcnt(" #n ")" ::: "memory")
#define WAITL0() asm volatile("s_waitcnt lgkmcnt(0)" ::: "memory")
#define BAR() do { asm volatile("" ::: "memory"); __builtin_amdgcn_s_barrier(); asm volatile("" ::: "memory"); } while (0)

constexpr size_t WS_FL = 48u << 20;
typedef __attribute__((address_space(1))) unsigned gu32;
DEV void signal(char* ws, int idx) { WAITV(0); BAR(); if (threadIdx.x == 0) __hip_atomic_fetch_add((gu32*)(ws + WS_FL + idx * 256), 1u, __ATOMIC_RELAXED, __HIP_MEMORY_SCOPE_AGENT); }
DEV void await(char* ws, int idx, unsigned n) {
  if (threadIdx.x == 0) { int spins = 0; while (__hip_atomic_load((gu32*)(ws + WS_FL + idx * 256), __ATOMIC_RELAXED, __HIP_MEMORY_SCOPE_AGENT) < n && ++spins < 100000) __builtin_amdgcn_s_sleep(1); }
  BAR();
}
constexpr int K1_LDS = 4 * 32768 + 2048;
template <bool TR>
DEV void k1_body(const char* xs, const char* ws, char* wsp, int rb, int cb) {
  extern __shared__ __attribute__((aligned(1024))) char lds_raw[];
  const unsigned lds0 = (unsigned)(size_t)(LAS char*)lds_raw;
  const int tid = threadIdx.x, lane = tid & 63, wv = __builtin_amdgcn_readfirstlane(tid >> 6);
  const int wm = wv >> 1, wn = wv & 1, h = lane >> 5, rho = lane & 31, sw = swap23(rho);
  ST_DECL STAMP(0);
  unsigned vx[4], vw[4];
  {
    int r = 8 * wv + (lane >> 3);
    unsigned o = r * 1280 + (((lane & 7) ^ ((r >> 1) & 7)) << 4);
    int kr = 4 * wv + (lane >> 4);
    unsigned ow = kr * 3840 + (((lane & 15) ^ ((lane >> 4) << 2)) << 4);
#pragma unroll
    for (int n = 0; n < 4; ++n) { vx[n] = o + n * 32 * 1280; vw[n] = ow + n * 16 * 3840; }
  }
  const unsigned dst0 = lds0 + wv * 1024;
#pragma unroll
  for (int s = 0; s < 3; ++s) {
#pragma unroll
    for (int n = 0; n < 4; ++n) { dma1(vx[n], xs + s * 128, dst0 + s * 32768 + n * 4096); dma1(vw[n], ws + s * 64 * 3840, dst0 + s * 32768 + 16384 + n * 4096); }
  }
  unsigned ax[2][4], aw[2];
#pragma unroll
  for (int t = 0; t < 2; ++t) {
    int row = wm * 64 + ((t ^ wn) << 5) + sw;
#pragma unroll
    for (int ks = 0; ks < 4; ++ks) ax[t][ks] = lds0 + row * 128 + (((2 * ks + h) ^ ((row >> 1) & 7)) << 4);
    int q = (lane & 15) >> 2, p = lane & 3, blk = (lane >> 4) & 1;
    int ch = wn * 8 + ((t ^ wm) << 2) + 2 * blk + (p & 1);
    aw[t] = lds0 + 16384 + (8 * h + q) * 256 + ((ch ^ (q << 2)) << 4) + 8 * (p >> 1);
  }
  bf16x8 sel;
  { __bf16 one = (__bf16)((((lane >> 4) ^ (lane >> 2)) & 1) ? 0.0f : 1.0f);
#pragma unroll
    for (int j = 0; j < 8; ++j) sel[j] = one; }
  f32x16 acc[2][2] = {};
  f32x4 sx = {}, sww = {};
  float q0 = 0.f, q1 = 0.f;
  STAMP(1);
  WAITV(16); BAR();
  STAMP(2);
  unsigned slot = 0;
  bf16x8 fx[2], fw[2];
#pragma unroll
  for (int t = 0; t < 2; ++t) { fx[t] = lds_b128(ax[t][0]); fw[t] = cat(lds_tr(aw[t]), lds_tr(aw[t] + 1024)); }
#pragma unroll 1
  for (int s = 0; s < 10; ++s) {
    const int st = s + 3 < 10 ? s + 3 : 9;
    const u64 mask = s + 3 < 10 ? ~0ull : 1ull;
    const char* xg = xs + st * 128;
    const char* wg = ws + st * (64 * 3840);
    const unsigned dst = dst0 + ((s + 3) & 3) * 32768;
    const unsigned nslot = ((s + 1) & 3) * 32768;
#pragma unroll
    for (int ks = 0; ks < 4; ++ks) {
      bf16x8 nx[2], nw[2];
      if (ks == 3) { WAITV(16); WAITL0(); BAR(); }
      SCHED();
#pragma unroll
      for (int t = 0; t < 2; ++t) {
        if (ks < 3) { nx[t] = lds_b128(ax[t][ks + 1] + slot); nw[t] = cat(lds_tr(aw[t] + slot + (ks + 1) * 4096), lds_tr(aw[t] + slot + (ks + 1) * 4096 + 1024)); }
        else { nx[t] = lds_b128(ax[t][0] + nslot); nw[t] = cat(lds_tr(aw[t] + nslot), lds_tr(aw[t] + nslot + 1024)); }
      }
      u32x4 xu = __builtin_bit_cast(u32x4, fx[0]);
#pragma unroll
      for (int i = 0; i < 4; ++i) {
        const int tm = i >> 1, tn = i & 1;
        if (TR) acc[tm][tn] = __builtin_amdgcn_mfma_f32_32x32x16_bf16(fw[tn], fx[tm], acc[tm][tn], 0, 0, 0);
        else acc[tm][tn] = __builtin_amdgcn_mfma_f32_32x32x16_bf16(fx[tm], fw[tn], acc[tm][tn], 0, 0, 0);
        float lo = bflo(xu[i]), hi = bfhi(xu[i]);
        q0 = fmaf(lo, lo, q0); q1 = fmaf(hi, hi, q1);
        asm volatile("" : "+v"(q0), "+v"(q1));
        if (ks < 3 && i < 3) {
          const int j = ks * 3 + i;
          if (j < 8) { if (j & 1) dma1m(vw[j >> 1], wg, dst + 16384 + (j >> 1) * 4096, mask); else dma1m(vx[j >> 1], xg, dst + (j >> 1) * 4096, mask); }
        }
        SCHED();
      }
      sx = __builtin_amdgcn_mfma_f32_16x16x32_bf16(sel, fx[0], sx, 0, 0, 0);
      sww = __builtin_amdgcn_mfma_f32_16x16x32_bf16(sel, fw[0], sww, 0, 0, 0);
#pragma unroll
      for (int t = 0; t < 2; ++t) { fx[t] = nx[t]; fw[t] = nw[t]; }
    }
    slot = nslot;
  }
  STAMP(3);
  LAS float* stf = (LAS float*)(lds0 + 4 * 32768);
  {
    float sq = q0 + q1;
    sq += __shfl_xor(sq, 32);
    float mean, rinv;
    ln_scalars(sx[0], sq, mean, rinv);
    if (h == 0) {
      int row = wm * 64 + (wn << 5) + sw;
      stf[row] = rinv; stf[128 + row] = mean * rinv;
      stf[256 + wn * 64 + (wm << 5) + sw] = sww[0];
    }
  }
  WAITL0(); BAR();
  STAMP(4);
  const rsrc_t wr = mkrsrc(wsp);
  if (TR) {
    const bool isq = cb < 5;
    const float sc = isq ? 0.32259580f : 1.0f;
    const unsigned base = isq ? WS_Q : WS_KV;
    const int tstride = isq ? QT : KVT;
    const int cc0 = (isq ? cb : cb - 5) * 16 + wn * 8 + h;
#pragma unroll
    for (int tm = 0; tm < 2; ++tm) {
      const int row = wm * 64 + ((tm ^ wn) << 5) + sw;
      const float rinv = stf[row] * sc, mr = stf[128 + row] * sc;
      const int tile = rb * 4 + wm * 2 + (tm ^ wn);
#pragma unroll
      for (int tn = 0; tn < 2; ++tn)
#pragma unroll
        for (int s = 0; s < 2; ++s) {
          const int cl = ((tn ^ wm) << 2) + 2 * s;
          const LAS f32x4* cs = (const LAS f32x4*)(stf + 256 + wn * 64 + (cl + h) * 8);
          const f32x4 c0 = cs[0], c1 = cs[1];
          u32x4 o;
#pragma unroll
          for (int j = 0; j < 2; ++j) {
            o[j] = pack2(acc[tm][tn][8 * s + 2 * j] * rinv - mr * c0[2 * j], acc[tm][tn][8 * s + 2 * j + 1] * rinv - mr * c0[2 * j + 1]);
            o[2 + j] = pack2(acc[tm][tn][8 * s + 4 + 2 * j] * rinv - mr * c1[2 * j], acc[tm][tn][8 * s + 5 + 2 * j] * rinv - mr * c1[2 * j + 1]);
          }
          const int cc = cc0 + cl, p = (cc * 205) >> 10, r = cc - 5 * p;
          if (r == 2) {
            const unsigned a = (2 * p * 64 + tile) * tstride + 1024 + sw * 8;
            st8(wr, a, base, u32x2{o[0], o[1]});
            st8(wr, a, base + 64 * tstride, u32x2{o[2], o[3]});
          } else {
            const int odd = r > 2, rr = r - 3 * odd;
            st16(wr, ((2 * p + odd) * 64 + tile) * tstride + rr * 512 + sw * 16, base, o);
          }
        }
    }
  } else {
#pragma unroll
    for (int tn = 0; tn < 2; ++tn) {
      const int cl = wn * 64 + ((tn ^ wm) << 5) + sw;
      const float cs = stf[256 + cl];
      const int f = (cb - 10) * 128 + cl, head = (f * 3277) >> 16, d = f - head * 20;
      const int dp = (head & 1) ? (d >= 4 ? d - 4 : d + 16) : d;
#pragma unroll
      for (int tm = 0; tm < 2; ++tm) {
        const int tile = rb * 4 + wm * 2 + (tm ^ wn);
        const unsigned a = (head * 64 + tile) * KVT + QT + (h * 21 + dp) * 16;
#pragma unroll
        for (int s = 0; s < 2; ++s) {
          const int row = wm * 64 + ((tm ^ wn) << 5) + 16 * s + 8 * h;
          const LAS f32x4* rp = (const LAS f32x4*)(stf + row);
          const f32x4 r0 = rp[0], r1 = rp[1], m0 = rp[32], m1 = rp[33];
          u32x4 o;
#pragma unroll
          for (int j = 0; j < 2; ++j) {
            o[j] = pack2(acc[tm][tn][8 * s + 2 * j] * r0[2 * j] - m0[2 * j] * cs, acc[tm][tn][8 * s + 2 * j + 1] * r0[2 * j + 1] - m0[2 * j + 1] * cs);
            o[2 + j] = pack2(acc[tm][tn][8 * s + 4 + 2 * j] * r1[2 * j] - m1[2 * j] * cs, acc[tm][tn][8 * s + 5 + 2 * j] * r1[2 * j + 1] - m1[2 * j + 1] * cs);
          }
          st16(wr, a + s * 42 * 16, WS_KV, o);
          const unsigned one = h ? 0u : 0x3f803f80u;
          if (dp == 0) st16(wr, a + s * 42 * 16 + 320, WS_KV, u32x4{one, one, one, one});
        }
      }
    }
  }
  WAITV(0);
  ST_OUT(wsp, 0);
}

__global__ __launch_bounds__(256, 1) void k1_qkv(const char* __restrict__ x, const char* __restrict__ w, char* __restrict__ ws) {
  const int b = blockIdx.x, t = (b & 7) * 30 + (b >> 3), half = t >= 120, u = t - 120 * half;
  const int cb = u >> 3, rb = half * 8 + (u & 7);
  if (b == 0 && threadIdx.x < 32) *(unsigned*)(ws + WS_FL + threadIdx.x * 256) = 0;
  const char* xs = x + (size_t)rb * 128 * 1280;
  const char* wsrc = w + cb * 256;
  if (cb < 10) k1_body<true>(xs, wsrc, ws, rb, cb);
  else k1_body<false>(xs, wsrc, ws, rb, cb);
}

constexpr int PH = 16 * KVT;
constexpr int K2_LDS = 3 * PH + 3 * 5120 + 5 * 4096;

DEV f32x16 qk(bf16x8 k0, s16x4 kt, bf16x8 q0, s16x4 qt) {
  f32x16 z = {};
  f32x16 s = __builtin_amdgcn_mfma_f32_32x32x16_bf16(k0, q0, z, 0, 0, 0);
  return __builtin_amdgcn_mfma_f32_32x32x8bf16_1k(kt, qt, s, 0, 0, 0);
}
DEV bf16x16 phalf(f32x16 s, int i) {
  f32x8 r = i ? __builtin_shufflevector(s, s, 8, 9, 10, 11, 12, 13, 14, 15) : __builtin_shufflevector(s, s, 0, 1, 2, 3, 4, 5, 6, 7);
  return __builtin_bit_cast(bf16x16, r);
}

#define R_SA 32
#define R_SB 48
#define R_O 64
#define R_K0 80
#define R_VA 84
#define R_VB 88
#define R_V1 92
#define R_Q0 96
#define R_KT 100
#define R_QT 102
#define R_PH 104
#define STR_(x) #x
#define STR(x) STR_(x)
#define VR(b, lo, hi) "v[" STR(b) "+" #lo ":" STR(b) "+" #hi "]"
#define EX(X, i) "v_exp_f32 v[" STR(X) "+" #i "], v[" STR(X) "+" #i "]\n"
#define EH(X, i) "v_exp_f32 v[" STR(R_PH) "+" #i "], v[" STR(X) "+8+" #i "]\n"
#define TILE(X, Y, VC, VN, t, AK, AT, tk, AV, tv) \
  "s_waitcnt lgkmcnt(0)\n" \
  "v_mfma_f32_32x32x16_bf16 " VR(Y, 0, 15) ", " VR(R_K0, 0, 3) ", " VR(R_Q0, 0, 3) ", 0\n" \
  "ds_read_b128 " VR(R_K0, 0, 3) ", %[" #AK "] offset:" #tk "*2624\n" \
  EX(X, 0) EX(X, 1) EX(X, 2) EX(X, 3) \
  "v_mfma_f32_32x32x8_bf16 " VR(Y, 0, 15) ", " VR(R_KT, 0, 1) ", " VR(R_QT, 0, 1) ", " VR(Y, 0, 15) "\n" \
  "ds_read_b64 " VR(R_KT, 0, 1) ", %[" #AT "] offset:" #tk "*2624\n" \
  EX(X, 4) EX(X, 5) EX(X, 6) EX(X, 7) \
  "v_smfmac_f32_32x32x32_bf16 " VR(R_O, 0, 15) ", " VR(R_V1, 0, 3) ", " VR(R_PH, 0, 7) ", %[idx]\n" \
  "ds_read_b128 " VR(R_V1, 0, 3) ", %[av] offset:" #t "*2624+672\n" \
  "ds_read_b128 " VR(VN, 0, 3) ", %[" #AV "] offset:" #tv "*2624\n" \
  EH(X, 0) EH(X, 1) EH(X, 2) EH(X, 3) \
  "v_smfmac_f32_32x32x32_bf16 " VR(R_O, 0, 15) ", " VR(VC, 0, 3) ", " VR(X, 0, 7) ", %[idx]\n" \
  EH(X, 4) EH(X, 5) EH(X, 6) EH(X, 7)
#define TILE2(t0, t1, t2, t3) TILE(R_SA, R_SB, R_VA, R_VB, t0, ak, at, t2, av, t1) TILE(R_SB, R_SA, R_VB, R_VA, t1, ak, at, t3, av, t2)
#define FIX(b, n) "{v[" STR(b) ":" STR(n) "]}"
template <int N> DEV void dman(unsigned voff, const void* sbase, unsigned dst) {
  if (N == 1) asm volatile("s_mov_b32 m0, %2\n\ts_nop 0\n\tglobal_load_lds_dwordx4 %0, %1" ::"v"(voff), "s"(sbase), "s"(dst) : "memory");
  if (N == 2) asm volatile("s_mov_b32 m0, %2\n\ts_nop 0\n\tglobal_load_lds_dwordx4 %0, %1\n\tglobal_load_lds_dwordx4 %0, %1 offset:1024" ::"v"(voff), "s"(sbase), "s"(dst) : "memory");
  if (N == 3) asm volatile("s_mov_b32 m0, %2\n\ts_nop 0\n\tglobal_load_lds_dwordx4 %0, %1\n\tglobal_load_lds_dwordx4 %0, %1 offset:1024\n\t"
                           "global_load_lds_dwordx4 %0, %1 offset:2048" ::"v"(voff), "s"(sbase), "s"(dst) : "memory");
  if (N == 4) asm volatile("s_mov_b32 m0, %2\n\ts_nop 0\n\tglobal_load_lds_dwordx4 %0, %1\n\tglobal_load_lds_dwordx4 %0, %1 offset:1024\n\t"
                           "global_load_lds_dwordx4 %0, %1 offset:2048\n\tglobal_load_lds_dwordx4 %0, %1 offset:3072" ::"v"(voff), "s"(sbase), "s"(dst) : "memory");
}

DEV void k2_loader(const char* wo, const char* wg, const char* wu, const char* wd, char* ws, const char* kv, unsigned lds0, int lane, int b) {
  const unsigned v16 = lane * 16, tl = lds0 + 3 * PH;
  __builtin_amdgcn_s_setprio(3);
  BAR();
#pragma unroll 1
  for (int i = 0; i < 19; ++i) { const int c = (i < 18 ? 48 + 4 * i : 119) * 1024; dman<4>(v16, kv + c, lds0 + c); }
  WAITV(0); BAR();
#pragma unroll 1
  for (int i = 0; i < 11; ++i) { const int c = (i < 10 ? 4 * i : 37) * 1024; dman<4>(v16, kv + 3 * PH + c, lds0 + c); }
  const int na = b < 80 ? 3 : 2, ua = b * 2 + (b < 80 ? b : 80);
  const int nb = b < 56 ? 5 : 4, ub = b * 4 + (b < 56 ? b : 56);
  unsigned vo[5];
#pragma unroll
  for (int n = 0; n < 5; ++n) { int c = lane + 64 * n, r = (c * 205) >> 11; vo[n] = r * 1280 + (c - 10 * r) * 16; }
#pragma unroll 1
  for (int i = 0; i < na; ++i) {
    const int unit = ua + i, isd = unit >= 160, u = unit - 160 * isd;
    const char* src = (isd ? wd : wo) + (size_t)(u >> 3) * 32 * 1280 + (u & 7) * 160;
#pragma unroll
    for (int n = 0; n < 5; ++n) dma1(vo[n], src, tl + i * 5120 + n * 1024);
  }
  const unsigned vg = (lane >> 3) * (F_ * 2) + (lane & 7) * 16;
#pragma unroll 1
  for (int i = 0; i < nb; ++i) {
    const int unit = ub + i, ks = (unit * 2428) >> 16, fg = unit - 27 * ks;
    const size_t so = (size_t)ks * 16 * (F_ * 2) + fg * 128;
#pragma unroll
    for (int n = 0; n < 4; ++n) dma1(vg, ((n >> 1) ? wu : wg) + so + (n & 1) * 8 * (F_ * 2), tl + 15360 + i * 4096 + n * 1024);
  }
  WAITV(26); BAR();
  WAITV(0);
  const rsrc_t wr = mkrsrc(ws);
  const int q = (lane & 15) >> 2, pp = lane & 3;
#pragma unroll 1
  for (int i = 0; i < na; ++i) {
    const int unit = ua + i, isd = unit >= 160, u = unit - 160 * isd;
    const unsigned dst = isd ? WS_WD + ((u & 7) * 54 + (u >> 3)) * 5120 : WS_WO + ((u & 7) * 20 + (u >> 3)) * 5120;
    const unsigned a = tl + i * 5120 + (8 * (lane >> 4) + q) * 160;
#pragma unroll
    for (int T = 0; T < 5; ++T) {
      const unsigned c = a + (T < 4 ? (T >> 1) * 64 + (T & 1) * 8 + pp * 16 : 128 + pp * 8);
      st16(wr, v16 + (T & 3) * 1024, dst + (T >> 2) * 4096, __builtin_bit_cast(u32x4, cat(lds_tr(c), lds_tr(c + 640))));
    }
  }
#pragma unroll 1
  for (int i = 0; i < nb; ++i) {
    const unsigned a = tl + 15360 + i * 4096 + ((lane >> 4) & 1) * 2048 + (8 * (lane >> 5) + q) * 128 + (pp & 1) * 16 + (pp >> 1) * 8;
#pragma unroll
    for (int T = 0; T < 4; ++T) st16(wr, v16 + T * 1024, WS_WGU + (ub + i) * 4096, __builtin_bit_cast(u32x4, cat(lds_tr(a + T * 32), lds_tr(a + T * 32 + 512))));
  }
}

__global__ __launch_bounds__(576) void k2_attn(const char* __restrict__ wo, const char* __restrict__ wg, const char* __restrict__ wu,
                                               const char* __restrict__ wd, char* __restrict__ ws) {
  extern __shared__ __attribute__((aligned(1024))) char lds_raw[];
  const unsigned lds0 = (unsigned)(size_t)(LAS char*)lds_raw;
  const int tid = threadIdx.x, lane = tid & 63, wv = __builtin_amdgcn_readfirstlane(tid >> 6);
  const int h = lane >> 5, rho = lane & 31, sw = swap23(rho);
  ST_DECL STAMP(0);
  const int b = blockIdx.x, head = (b & 7) * 4 + ((b >> 3) & 3), qtile = (b >> 5) * 8 + wv;
  const char* kv = ws + WS_KV + (size_t)head * 64 * KVT;
  if (wv == 8) { k2_loader(wo, wg, wu, wd, ws, kv, lds0, lane, b); return; }
  const char* qp = ws + WS_Q + (size_t)(head * 64 + qtile) * QT;
  const unsigned v16 = lane * 16;
  bf16x8 q0;
  s16x4 qt;
  asm volatile("global_load_dwordx4 %0, %2, %4\n\tv_mov_b32 v[" STR(R_QT) "], 0\n\tv_mov_b32 v[" STR(R_QT) "+1], 0\n\ts_mov_b32 exec_hi, 0\n\t"
               "global_load_dwordx2 %1, %3, %4 offset:1024\n\ts_mov_b32 exec_hi, -1"
               : "=&" FIX(R_Q0, 99)(q0), "=&" FIX(R_QT, 103)(qt) : "v"(v16), "v"(lane * 8), "s"(qp) : "memory");
  dman<4>(v16, kv + wv * 4096, lds0 + wv * 4096);
  dman<2>(v16, kv + 32768 + wv * 2048, lds0 + 32768 + wv * 2048);
  int idx = 0xDDDD;
  unsigned ak = lds0 + h * 512 + rho * 16, at = lds0 + (h ? QT + 41 * 16 : 1024 + rho * 8);
  unsigned av = lds0 + QT + (sw < 20 ? h * 21 + sw : (sw & 7) == 4 ? 20 : 41) * 16;
  const rsrc_t wr = mkrsrc(ws);
  f32x16 o = {}, sb;
  f32x8 ph = {};
  bf16x8 vb, v1 = {};
  asm volatile("" : "+" FIX(R_O, 79)(o), "+" FIX(R_PH, 111)(ph), "+" FIX(R_V1, 95)(v1), "+v"(ak), "+v"(at), "+v"(av), "+v"(idx));
  asm volatile("s_waitcnt vmcnt(0)" : "+" FIX(R_Q0, 99)(q0), "+" FIX(R_QT, 103)(qt) :: "memory");
  BAR();
  STAMP(1);
  f32x16 sa = qk(lds_b128(ak), *(const LAS s16x4*)at, q0, qt);
  bf16x8 k0 = lds_b128(ak + KVT), va = lds_b128(av);
  s16x4 kt = *(const LAS s16x4*)(at + KVT);
  asm volatile("s_nop 7\n\ts_nop 3" : "+" FIX(R_SA, 47)(sa));
  unsigned slot = 0;
#pragma unroll 1
  for (int p = 0; p < 4; ++p) {
    const unsigned nslot = p >= 2 ? 0 : slot + PH;
    if (p == 1 || p == 2) BAR();
    asm volatile("s_setprio 3\n" TILE2(0, 1, 2, 3) TILE2(2, 3, 4, 5) TILE2(4, 5, 6, 7) TILE2(6, 7, 8, 9) TILE2(8, 9, 10, 11) "s_setprio 2\n" TILE2(10, 11, 12, 13) TILE2(12, 13, 14, 15)
                 "s_setprio 1\n" TILE(R_SA, R_SB, R_VA, R_VB, 14, nk, nt, 0, av, 15) "s_setprio 0\n" TILE(R_SB, R_SA, R_VB, R_VA, 15, nk, nt, 1, nv, 0)
                 "s_waitcnt lgkmcnt(0)"
                 : "+" FIX(R_SA, 47)(sa), "=&" FIX(R_SB, 63)(sb), "+" FIX(R_O, 79)(o), "+" FIX(R_K0, 83)(k0), "+" FIX(R_VA, 87)(va), "=&" FIX(R_VB, 91)(vb),
                   "+" FIX(R_V1, 95)(v1), "+" FIX(R_KT, 101)(kt), "+" FIX(R_PH, 111)(ph)
                 : FIX(R_Q0, 99)(q0), FIX(R_QT, 103)(qt), [idx] "v"(idx), [ak] "v"(ak + slot), [at] "v"(at + slot), [av] "v"(av + slot),
                   [nk] "v"(ak + nslot), [nt] "v"(at + nslot), [nv] "v"(av + nslot)
                 : "memory");
    slot = nslot;
  }
  asm volatile("s_nop 1\n\tv_smfmac_f32_32x32x32_bf16 " VR(R_O, 0, 15) ", " VR(R_V1, 0, 3) ", " VR(R_PH, 0, 7) ", %[idx]\n\ts_nop 7\n\ts_nop 3"
               : "+" FIX(R_O, 79)(o) : FIX(R_PH, 111)(ph), FIX(R_V1, 95)(v1), [idx] "v"(idx));
  STAMP(3);
  {
    float mx = 0.f;
#pragma unroll
    for (int i = 0; i < 12; ++i) mx = fmaxf(mx, fabsf(o[i]));
    float chk = 0.f;
#pragma unroll
    for (int i = 0; i < 12; ++i) chk += o[i] * 0.0f;
    const float l = o[12];
    const bool ok = (l >= 5.421010862e-20f) && (l <= 1.8446744e19f) && (chk == 0.f) && (mx <= 3.0e38f);
    if (__builtin_amdgcn_ballot_w64(!ok) != 0) {
      float m = -3.0e38f;
      o = f32x16{};
      const char* g = kv;
#pragma unroll 1
      for (int t = 0; t < 64; ++t, g += KVT) {
        const bf16x8 k0 = *(const bf16x8*)(g + (ak - lds0));
        const s16x4 kt = *(const s16x4*)(g + (at - lds0));
        const bf16x8 w0 = *(const bf16x8*)(g + (av - lds0)), w1 = *(const bf16x8*)(g + (av - lds0) + 42 * 16);
        f32x16 s = qk(k0, kt, q0, qt);
        float mn = m;
#pragma unroll
        for (int i = 0; i < 16; ++i) mn = fmaxf(mn, s[i]);
        mn = fmaxf(mn, __shfl_xor(mn, 32));
        const float alpha = __builtin_amdgcn_exp2f(m - mn);
        m = mn;
#pragma unroll
        for (int i = 0; i < 16; ++i) { s[i] = __builtin_amdgcn_exp2f(s[i] - mn); o[i] *= alpha; }
        o = __builtin_amdgcn_smfmac_f32_32x32x32_bf16(w0, phalf(s, 0), o, idx, 0, 0);
        o = __builtin_amdgcn_smfmac_f32_32x32x32_bf16(w1, phalf(s, 1), o, idx, 0, 0);
      }
    }
  }
  const float il = 1.0f / o[12];
  const int tok = qtile * 32 + rho, odd = head & 1;
  const unsigned ob = (tok >> 4) * 20480 + (tok & 15) * 16;
  const int cc = (head >> 1) * 5 + 3 * odd + h, c2 = (head >> 1) * 5 + 2;
  st16(wr, ob + cc * 256, WS_O, u32x4{pack2(o[0] * il, o[1] * il), pack2(o[2] * il, o[3] * il), pack2(o[4] * il, o[5] * il), pack2(o[6] * il, o[7] * il)});
  if (h == 0) st8(wr, ob, WS_O + c2 * 256 + odd * 8, u32x2{pack2(o[8] * il, o[9] * il), pack2(o[10] * il, o[11] * il)});
  ST_OUT(ws, 1);
}

constexpr int KS_LDS = 4 * 3 * 5 * 1024;
template <int NK, int DEPTH>
DEV void ksplit_gemm(const char* act, const char* wt, int wv, int lane, f32x4 (&res)[5]
#define ST_ARG
) {
  extern __shared__ __attribute__((aligned(1024))) char lds_raw[];
  const unsigned lds0 = (unsigned)(size_t)(LAS char*)lds_raw;
  constexpr int NI = (NK + 3) / 4;
  const char* ap[4];
#pragma unroll
  for (int m = 0; m < 4; ++m) ap[m] = act + (size_t)((m ^ wv) * NK + wv) * 1024;
  const char* wp = wt + wv * 5120;
  const unsigned v16 = lane * 16;
  const bool last = (NI - 1) * 4 + wv < NK;
  bf16x8 fa[NI][4], fw[NI][5];
  f32x4 acc[4][5] = {};
#pragma unroll
  for (int i = 0; i < NI + DEPTH; ++i) {
    if (i < NI) {
      const int ii = (i == NI - 1 && NK % 4) ? (last ? i : i - 1) : i;
#pragma unroll
      for (int m = 0; m < 4; ++m) {
        fa[i][m] = *(const bf16x8*)(ap[m] + (size_t)ii * 4096 + v16);
        fw[i][m] = *(const bf16x8*)(wp + (size_t)ii * 20480 + m * 1024 + v16);
      }
      fw[i][4] = *(const bf16x8*)(wp + (size_t)ii * 20480 + 4096 + v16);
    }
    SCHED();
    const int c = i - DEPTH;
    if (c == -1) STAMP(1);
    if (c == 1) STAMP(2);
    if (c >= 0 && (c < NI - 1 || NK % 4 == 0 || last)) {
#pragma unroll
      for (int m = 0; m < 4; ++m)
#pragma unroll
        for (int T = 0; T < 5; ++T) acc[m][T] = __builtin_amdgcn_mfma_f32_16x16x32_bf16(fw[c][T], fa[c][m], acc[m][T], 0, 0, 0);
    }
    SCHED();
  }
  STAMP(3);
#pragma unroll
  for (int m = 1; m < 4; ++m)
#pragma unroll
    for (int T = 0; T < 5; ++T) *(LAS f32x4*)(lds0 + (((m ^ wv) * 3 + m - 1) * 5 + T) * 1024 + v16) = acc[m][T];
  WAITL0(); BAR();
  STAMP(4);
#pragma unroll
  for (int T = 0; T < 5; ++T) {
    f32x4 r = acc[0][T];
#pragma unroll
    for (int m = 0; m < 3; ++m) r += *(const LAS f32x4*)(lds0 + ((wv * 3 + m) * 5 + T) * 1024 + v16);
    res[T] = r;
  }
}
DEV unsigned h_off(int tok, int chunk) { return (tok >> 5) * 40960 + chunk * 512 + (tok & 31) * 16; }
DEV unsigned addres(unsigned r, float a, float b) { return pack2(bflo(r) + rbf(a), bfhi(r) + rbf(b)); }

DEV void k3_wo(const char* __restrict__ x, char* __restrict__ ws) {
  const int tid = threadIdx.x, lane = tid & 63, wv = __builtin_amdgcn_readfirstlane(tid >> 6);
  ST_DECL STAMP(0);
  const int b = blockIdx.x, xc = b & 7, j = b >> 3, rb = (xc >> 1) * 8 + (j & 7), cb = (xc & 1) * 4 + (j >> 3);
  const int tok = rb * 64 + wv * 16 + (lane & 15), g = lane >> 4;
  const char* xr = x + (size_t)tok * 1280 + cb * 160;
  const u32x4 r0 = *(const u32x4*)(xr + g * 16), r1 = *(const u32x4*)(xr + 64 + g * 16);
  const u32x2 r2 = *(const u32x2*)(xr + 128 + g * 8);
  f32x4 res[5];
  ksplit_gemm<20, 5>(ws + WS_O + (size_t)rb * 4 * 20480, ws + WS_WO + (size_t)cb * 20 * 5120, wv, lane, res ST_ARG);
  unsigned o[10];
#pragma unroll
  for (int i = 0; i < 4; ++i) {
    o[i] = addres(r0[i], res[i >> 1][2 * (i & 1)], res[i >> 1][2 * (i & 1) + 1]);
    o[4 + i] = addres(r1[i], res[2 + (i >> 1)][2 * (i & 1)], res[2 + (i >> 1)][2 * (i & 1) + 1]);
  }
  o[8] = addres(r2[0], res[4][0], res[4][1]); o[9] = addres(r2[1], res[4][2], res[4][3]);
  const rsrc_t wr = mkrsrc(ws);
  st16(wr, h_off(tok, cb * 10 + g), WS_H, u32x4{o[0], o[1], o[2], o[3]});
  st16(wr, h_off(tok, cb * 10 + 4 + g), WS_H, u32x4{o[4], o[5], o[6], o[7]});
  st8(wr, h_off(tok, cb * 10 + 8 + (g >> 1)) + (g & 1) * 8, WS_H, u32x2{o[8], o[9]});
  float sm = 0.f, sq = 0.f;
#pragma unroll
  for (int i = 0; i < 10; ++i) { float a = bflo(o[i]), c = bfhi(o[i]); sm += a + c; sq = fmaf(a, a, fmaf(c, c, sq)); }
  sm += __shfl_xor(sm, 16); sq += __shfl_xor(sq, 16);
  sm += __shfl_xor(sm, 32); sq += __shfl_xor(sq, 32);
  if (g == 0) st8(wr, tok * 64 + cb * 8, WS_ST, __builtin_bit_cast(u32x2, f32x2{sm, sq}));
  signal(ws, rb >> 1);
  ST_OUT(ws, 2);
}

DEV void k5_down(char* __restrict__ ws, char* __restrict__ out) {
  const int tid = threadIdx.x, lane = tid & 63, wv = __builtin_amdgcn_readfirstlane(tid >> 6);
  ST_DECL STAMP(0);
  const int b = blockIdx.x, xc = b & 7, j = b >> 3, rb = (xc >> 1) * 8 + (j & 7), cb = (xc & 1) * 4 + (j >> 3);
  const int tok = rb * 64 + wv * 16 + (lane & 15), g = lane >> 4;
  await(ws, 16 + (rb >> 1), 16);
  const char* hb = ws + WS_H;
  const u32x4 r0 = *(const u32x4*)(hb + h_off(tok, cb * 10 + g)), r1 = *(const u32x4*)(hb + h_off(tok, cb * 10 + 4 + g));
  const u32x2 r2 = *(const u32x2*)(hb + h_off(tok, cb * 10 + 8 + (g >> 1)) + (g & 1) * 8);
  f32x4 res[5];
  ksplit_gemm<54, 5>(ws + WS_A + (size_t)rb * 4 * 54 * 1024, ws + WS_WD + (size_t)cb * 54 * 5120, wv, lane, res ST_ARG);
  unsigned o[10];
#pragma unroll
  for (int i = 0; i < 4; ++i) {
    o[i] = addres(r0[i], res[i >> 1][2 * (i & 1)], res[i >> 1][2 * (i & 1) + 1]);
    o[4 + i] = addres(r1[i], res[2 + (i >> 1)][2 * (i & 1)], res[2 + (i >> 1)][2 * (i & 1) + 1]);
  }
  o[8] = addres(r2[0], res[4][0], res[4][1]); o[9] = addres(r2[1], res[4][2], res[4][3]);
  const rsrc_t wr = mkrsrc(out);
  st16(wr, tok * 1280 + g * 16, cb * 160, u32x4{o[0], o[1], o[2], o[3]});
  st16(wr, tok * 1280 + 64 + g * 16, cb * 160, u32x4{o[4], o[5], o[6], o[7]});
  st8(wr, tok * 1280 + 128 + g * 8, cb * 160, u32x2{o[8], o[9]});
  ST_OUT(ws, 4);
}

constexpr int K4_ST = 28672 + 16384;
constexpr int K4_LDS = 3 * K4_ST;
DEV void dma4m(unsigned voff, const void* sbase, unsigned dst, u64 mask) {
  asm volatile("s_mov_b32 m0, %2\n\ts_mov_b64 exec, %3\n\tglobal_load_lds_dwordx4 %0, %1\n\tglobal_load_lds_dwordx4 %0, %1 offset:1024\n\t"
               "global_load_lds_dwordx4 %0, %1 offset:2048\n\tglobal_load_lds_dwordx4 %0, %1 offset:3072\n\ts_mov_b64 exec, -1" ::"v"(voff), "s"(sbase), "s"(dst), "s"(mask) : "memory");
}
DEV bf16x8 ln_frag(bf16x8 f, float rinv, float nmr) {
  u32x4 u = __builtin_bit_cast(u32x4, f), r;
#pragma unroll
  for (int i = 0; i < 4; ++i) r[i] = pack2(fmaf(bflo(u[i]), rinv, nmr), fmaf(bfhi(u[i]), rinv, nmr));
  return __builtin_bit_cast(bf16x8, r);
}
DEV void k4_gu(char* __restrict__ ws) {
  extern __shared__ __attribute__((aligned(1024))) char lds_raw[];
  const unsigned lds0 = (unsigned)(size_t)(LAS char*)lds_raw;
  const int tid = threadIdx.x, lane = tid & 63, wv = __builtin_amdgcn_readfirstlane(tid >> 6);
  const int h = lane >> 5, rho = lane & 31;
  ST_DECL STAMP(0);
  const int b = blockIdx.x, xc = b & 7, j = b >> 3, tb = (xc & 1) * 8 + (j & 7), fb = (xc >> 1) * 4 + (j >> 3);
  const int ft0 = (27 * fb) >> 2, nft = ((27 * (fb + 1)) >> 2) - ft0;
  const int tok = tb * 128 + wv * 32 + rho;
  const unsigned v16 = lane * 16;
  await(ws, tb, 16);
  STAMP(4);
  const f32x4* sp = (const f32x4*)(ws + WS_ST + (size_t)tok * 64 + h * 32);
  const f32x4 s0 = sp[0], s1 = sp[1];
  const char* wsrc = ws + WS_WGU + (size_t)(wv * 108 + ft0) * 1024;
  const char* hsrc = ws + WS_H + (size_t)(tb * 4 + wv) * 40960;
  const unsigned dw = lds0 + wv * 7168, dh = lds0 + 28672 + wv * 4096;
#pragma unroll
  for (int s = 0; s < 2; ++s) {
#pragma unroll
    for (int n = 0; n < 4; ++n) dma1(v16, hsrc + s * 4096 + n * 1024, dh + s * K4_ST + n * 1024);
#pragma unroll
    for (int n = 0; n < 7; ++n) dma1(v16, wsrc + s * (432 * 1024) + n * 1024, dw + s * K4_ST + n * 1024);
  }
  float mean, rinv;
  {
    float sm = (s0[0] + s0[2]) + (s1[0] + s1[2]), sq = (s0[1] + s0[3]) + (s1[1] + s1[3]);
    sm += __shfl_xor(sm, 32); sq += __shfl_xor(sq, 32);
    ln_scalars(sm, sq, mean, rinv);
  }
  const float nmr = -mean * rinv;
  const unsigned aw = lds0 + v16, ah = dh + v16;
  f32x16 acc[7] = {};
  STAMP(1);
  WAITV(11); BAR();
  STAMP(2);
  bf16x8 fw[7], fh;
#pragma unroll
  for (int T = 0; T < 7; ++T) fw[T] = lds_b128(aw + T * 1024);
  fh = ln_frag(lds_b128(ah), rinv, nmr);
  unsigned slot = 0;
#pragma unroll 1
  for (int s = 0; s < 10; ++s) {
    const int st = s + 2 < 10 ? s + 2 : 9;
    const u64 mask = s + 2 < 10 ? ~0ull : 1ull;
    const unsigned nslot = slot == 2 * K4_ST ? 0 : slot + K4_ST;
    const unsigned pslot = slot == 0 ? 2 * K4_ST : slot - K4_ST;
    const char* wgp = wsrc + (size_t)st * (432 * 1024);
    const char* hgp = hsrc + st * 4096;
#pragma unroll
    for (int ks = 0; ks < 4; ++ks) {
      if (ks == 3) { WAITV(11); WAITL0(); BAR(); }
      SCHED();
      const unsigned wa = ks < 3 ? aw + slot + (ks + 1) * 7168 : aw + nslot;
      const bf16x8 hraw = lds_b128(ks < 3 ? ah + slot + (ks + 1) * 1024 : ah + nslot);
      const u32x4 hu = __builtin_bit_cast(u32x4, hraw);
      u32x4 hn;
      bf16x8 nw[7];
#pragma unroll
      for (int T = 0; T < 7; ++T) {
        acc[T] = __builtin_amdgcn_mfma_f32_32x32x16_bf16(fw[T], fh, acc[T], 0, 0, 0);
        nw[T] = lds_b128(wa + T * 1024);
        if (T >= 3) hn[T - 3] = pack2(fmaf(bflo(hu[T - 3]), rinv, nmr), fmaf(bfhi(hu[T - 3]), rinv, nmr));
        if (ks < 3 && !(T & 1)) {
          const int jj = ks * 4 + (T >> 1);
          if (jj < 4) dma1m(v16, hgp + jj * 1024, dh + pslot + jj * 1024, mask);
          else if (jj < 11) dma1m(v16, wgp + (jj - 4) * 1024, dw + pslot + (jj - 4) * 1024, mask);
        }
        SCHED();
      }
#pragma unroll
      for (int T = 0; T < 7; ++T) fw[T] = nw[T];
      fh = __builtin_bit_cast(bf16x8, hn);
    }
    slot = nslot;
  }
  STAMP(3);
  const rsrc_t wr = mkrsrc(ws);
  const unsigned ab = (tok >> 4) * (54 * 1024) + (tok & 15) * 16 + h * 256;
#pragma unroll
  for (int T = 0; T < 7; ++T) {
    u32x4 o;
#pragma unroll
    for (int i = 0; i < 4; ++i) o[i] = pack2(fmaxf(acc[T][2 * i], 0.f) * acc[T][8 + 2 * i], fmaxf(acc[T][2 * i + 1], 0.f) * acc[T][9 + 2 * i]);
    if (T < 6 || nft == 7) st16(wr, ab + T * 512, WS_A + ft0 * 512, o);
  }
  signal(ws, 16 + tb);
  ST_OUT(ws, 3);
}

__global__ __launch_bounds__(256, 1) void k345(const char* __restrict__ x, char* __restrict__ ws, char* __restrict__ out) {
  k3_wo(x, ws); k4_gu(ws); k5_down(ws, out);
}
extern "C" void kernel_launch(void* const* d_in, const int* in_sizes, int n_in, void* d_out, int out_size, void* d_ws, size_t ws_size,
                              hipStream_t stream) {
  const char* x = (const char*)d_in[0];
  const char* wqkv = (const char*)d_in[1];
  const char* wo = (const char*)d_in[2];
  const char* wg = (const char*)d_in[4];
  const char* wu = (const char*)d_in[5];
  const char* wd = (const char*)d_in[6];
  char* ws = (char*)d_ws;
  (void)hipFuncSetAttribute((const void*)k1_qkv, hipFuncAttributeMaxDynamicSharedMemorySize, K1_LDS);
  (void)hipFuncSetAttribute((const void*)k2_attn, hipFuncAttributeMaxDynamicSharedMemorySize, K2_LDS);
  (void)hipFuncSetAttribute((const void*)k345, hipFuncAttributeMaxDynamicSharedMemorySize, K4_LDS);
  k1_qkv<<<240, 256, K1_LDS, stream>>>(x, wqkv, ws);
  k2_attn<<<256, 576, K2_LDS, stream>>>(wo, wg, wu, wd, ws);
  k345<<<256, 256, K4_LDS, stream>>>(x, ws, (char*)d_out);
}
```

```cpp
#include <hip/hip_runtime.h>
#include <hip/hip_bf16.h>

typedef __attribute__((ext_vector_type(8))) __bf16 bf16x8;
typedef __attribute__((ext_vector_type(16))) __bf16 bf16x16;
typedef __attribute__((ext_vector_type(2))) __bf16 bf16x2;
typedef __attribute__((ext_vector_type(4))) short s16x4;
typedef __attribute__((ext_vector_type(8))) short s16x8;
typedef __attribute__((ext_vector_type(32))) float f32x32;
typedef __attribute__((ext_vector_type(16))) float f32x16;
typedef __attribute__((ext_vector_type(8))) float f32x8;
typedef __attribute__((ext_vector_type(4))) float f32x4;
typedef __attribute__((ext_vector_type(2))) float f32x2;
typedef __attribute__((ext_vector_type(16))) unsigned u32x16;
typedef __attribute__((ext_vector_type(8))) unsigned u32x8;
typedef __attribute__((ext_vector_type(4))) unsigned u32x4;
typedef __attribute__((ext_vector_type(2))) unsigned u32x2;
typedef unsigned long long u64;
#define LAS __attribute__((address_space(3)))
#define DEV __device__ __forceinline__

constexpr int S_ = 2048, D_ = 640, F_ = 1728;
constexpr int QT = 1280;
constexpr int KVT = 2688;
constexpr unsigned WS_Q = 0;
constexpr unsigned WS_KV = WS_Q + 32 * 64 * QT;
constexpr unsigned WS_O = WS_KV + 32 * 64 * KVT;
constexpr unsigned WS_H = WS_O + S_ * D_ * 2;
constexpr unsigned WS_ST = WS_H + S_ * D_ * 2;
constexpr unsigned WS_A = WS_ST + S_ * 64;
constexpr unsigned WS_WO = WS_A + S_ * F_ * 2;
constexpr unsigned WS_WD = WS_WO + D_ * D_ * 2;
constexpr unsigned WS_WGU = WS_WD + F_ * D_ * 2;
constexpr unsigned WS_CS = WS_WGU + 2 * F_ * D_ * 2;
constexpr unsigned WS_FL = WS_CS + 216 * 256;
constexpr unsigned C_FQ = 0, C_F1 = 256, C_F2 = 768, C_F3 = 1024, C_F4 = 1280, C_WO = 1536, C_WG = 1544, C_WD = 1560, C_ACK = 1600, C_HB = 1664, C_END = 1728;
constexpr unsigned WS_END = WS_FL + C_END * 256;
constexpr unsigned L_OCT = 8 * KVT, L_STG = 131072, L_MISC = 161792, L_FLG = L_MISC + 1920, L_WR = 2 * L_OCT, L_TOTAL = 163840;

DEV int swap23(int r) { return (r & 0x13) | ((r & 4) << 1) | ((r & 8) >> 1); }
DEV unsigned pack2(float a, float b) { f32x2 v = {a, b}; bf16x2 r = __builtin_convertvector(v, bf16x2); return __builtin_bit_cast(unsigned, r); }
DEV float bflo(unsigned u) { return __builtin_bit_cast(float, u << 16); }
DEV float bfhi(unsigned u) { return __builtin_bit_cast(float, u & 0xffff0000u); }
DEV float rbf(float a) { return bflo(pack2(a, a)); }
DEV s16x4 lds_tr(unsigned a) { return __builtin_amdgcn_ds_read_tr16_b64_v4i16((LAS s16x4*)a); }
DEV bf16x8 cat(s16x4 a, s16x4 b) { s16x8 r = __builtin_shufflevector(a, b, 0, 1, 2, 3, 4, 5, 6, 7); return __builtin_bit_cast(bf16x8, r); }
typedef __amdgpu_buffer_rsrc_t rsrc_t;
DEV rsrc_t mkrsrc(const void* p, unsigned n) { return __builtin_amdgcn_make_buffer_rsrc((void*)p, 0, n, 0x00020000); }
DEV void st16(rsrc_t r, unsigned v, unsigned s, u32x4 d) { __builtin_amdgcn_raw_buffer_store_b128(d, r, v, s, 16); }
DEV void st8(rsrc_t r, unsigned v, unsigned s, u32x2 d) { __builtin_amdgcn_raw_buffer_store_b64(d, r, v, s, 16); }
DEV void st4(rsrc_t r, unsigned v, unsigned s, unsigned d) { __builtin_amdgcn_raw_buffer_store_b32(d, r, v, s, 16); }
#define XSUM(W, a, b) do { float c_, d_; asm("v_mov_b32 %2, %0\n\tv_mov_b32 %3, %1\n\ts_nop 0\n\tv_permlane" #W "_swap_b32 %0, %2\n\tv_permlane" #W "_swap_b32 %1, %3" \
    : "+v"(a), "+v"(b), "=&v"(c_), "=&v"(d_)); a += c_; b += d_; } while (0)
DEV void ln_scalars(float sum, float sq, float& mean, float& rinv) {
  float m = sum * (1.0f / D_);
  float v = fmaxf(sq * (1.0f / D_) - m * m, 0.0f);
  float sd = rbf(__builtin_amdgcn_sqrtf(rbf(rbf(v) + 1e-5f)));
  mean = rbf(m);
  rinv = __builtin_amdgcn_rcpf(sd);
}
DEV void dma1(unsigned voff, const void* sbase, unsigned dst) {
  asm volatile("s_mov_b32 m0, %2\n\ts_nop 0\n\tglobal_load_lds_dwordx4 %0, %1" ::"v"(voff), "s"(sbase), "s"(dst) : "memory");
}
#define WAITV(n) asm volatile("s_waitcnt vmcnt(" #n ")" ::: "memory")
#define WAITL0() asm volatile("s_waitcnt lgkmcnt(0)" ::: "memory")
#define BAR() do { asm volatile("" ::: "memory"); __builtin_amdgcn_s_barrier(); asm volatile("" ::: "memory"); } while (0)
constexpr int SPIN = 200000;
constexpr int CLM = 1;
constexpr int HP = 0;
constexpr int SL0 = 18;
constexpr int PFC = 1;
DEV void cnt_add(const char* fl, unsigned idx, unsigned v) { asm volatile("s_nop 4\n\tglobal_atomic_add %0, %1, %2 sc1" ::"v"(idx * 256), "v"(v), "s"(fl) : "memory"); }
DEV unsigned cnt_get(const char* fl, unsigned idx) {
  unsigned r;
  asm volatile("s_nop 4\n\tglobal_atomic_add %0, %1, %2, %3 sc0 sc1\n\ts_waitcnt vmcnt(0)" : "=&v"(r) : "v"(idx * 256), "v"(0), "s"(fl) : "memory");
  return r;
}
DEV void hb_check(const char* fl, int id, int lane) {
  if (lane == 0) {
    const unsigned h0 = cnt_get(fl, C_HB + id);
    for (int i = 0; i < SPIN && cnt_get(fl, C_HB + id) == h0; ++i);
    cnt_add(fl, C_ACK + id, 1);
  }
}
DEV void hb_lead(char* ws, int lane) {
  const rsrc_t wr = mkrsrc(ws, WS_END);
#pragma unroll
  for (int i = 0; i < 26; ++i) st4(wr, lane * 256, WS_FL + i * 16384, (i == 25 && lane == 63) ? 1 : 0);
  WAITV(0);
  for (int i = 0; i < SPIN; ++i) {
    cnt_add(ws + WS_FL, C_HB + lane, 1);
    if (__builtin_amdgcn_ballot_w64(cnt_get(ws + WS_FL, C_ACK + lane) < 16) == 0) break;
  }
}
DEV int lane_id() { int l; asm volatile("v_mbcnt_lo_u32_b32 %0, -1, 0\n\tv_mbcnt_hi_u32_b32 %0, -1, %0" : "=v"(l)); return l & 63; }
DEV unsigned lds_flag(unsigned a) { return __builtin_amdgcn_readfirstlane(*(volatile LAS unsigned*)a); }
DEV void lds_set(unsigned a, unsigned v) { *(volatile LAS unsigned*)a = v; }

DEV u32x4 rawrsrc(const void* p, unsigned n) { return u32x4{(unsigned)(size_t)p, (unsigned)((size_t)p >> 32) & 0xffff, n, 0x00020000}; }
DEV void p1_loader(u32x4 rx, u32x4 rw, u64 rs, int rb, int cb, int rot, int hw, int id, int lane) {
  const bool tr = cb < 10;
  const int L = 8 * hw + (lane >> 3), r = L & 15;
  const int tok = tr ? L : 8 * (r >> 2) + 4 * (L >> 4) + (r & 3);
  const unsigned o = (rb * 128 + tok) * 1280 + (((lane & 7) ^ ((L >> 1) & 7)) << 4);
  const int kr = 4 * hw + (lane >> 4);
  const unsigned ow = kr * 3840 + cb * 256 + (((lane & 15) ^ (((kr & 3) << 2) | (((kr >> 3) & 1) << (tr ? 0 : 1)))) << 4);
  u32x8 v;
#pragma unroll
  for (int n = 0; n < 4; ++n) { v[n] = o + n * 32 * 1280; v[4 + n] = ow + n * 16 * 3840; }
  int sx = rot * 128, sw;
  asm volatile(
    "s_waitcnt lgkmcnt(0)\n"
    "s_add_u32 m0, %[d], 0\n"
    "s_mul_i32 %[sw], %[sx], 1920\n"
    "buffer_load_dwordx4 v8, %[rx], %[sx] offen lds\n"
    "s_add_u32 m0, m0, 16384\n"
    "s_nop 0\n"
    "buffer_load_dwordx4 v12, %[rw], %[sw] offen lds\n"
    "s_add_u32 m0, %[d], 4096\n"
    "s_nop 0\n"
    "buffer_load_dwordx4 v9, %[rx], %[sx] offen lds\n"
    "s_add_u32 m0, m0, 16384\n"
    "s_nop 0\n"
    "buffer_load_dwordx4 v13, %[rw], %[sw] offen lds\n"
    "s_add_u32 m0, %[d], 8192\n"
    "s_nop 0\n"
    "buffer_load_dwordx4 v10, %[rx], %[sx] offen lds\n"
    "s_add_u32 m0, m0, 16384\n"
    "s_nop 0\n"
    "buffer_load_dwordx4 v14, %[rw], %[sw] offen lds\n"
    "s_add_u32 m0, %[d], 12288\n"
    "s_nop 0\n"
    "buffer_load_dwordx4 v11, %[rx], %[sx] offen lds\n"
    "s_add_u32 m0, m0, 16384\n"
    "s_add_u32 %[sx], %[sx], 128\n"
    "buffer_load_dwordx4 v15, %[rw], %[sw] offen lds\n"
    "s_cmp_eq_u32 %[sx], 1280\n"
    "s_cmov_b32 %[sx], 0\n"
    "v_mov_b32 v18, 0\n"
    "s_add_u32 m0, %[d], 32768\n"
    "s_mul_i32 %[sw], %[sx], 1920\n"
    "buffer_load_dwordx4 v8, %[rx], %[sx] offen lds\n"
    "s_add_u32 m0, m0, 16384\n"
    "s_nop 0\n"
    "buffer_load_dwordx4 v12, %[rw], %[sw] offen lds\n"
    "s_add_u32 m0, %[d], 36864\n"
    "s_nop 0\n"
    "buffer_load_dwordx4 v9, %[rx], %[sx] offen lds\n"
    "s_add_u32 m0, m0, 16384\n"
    "s_nop 0\n"
    "buffer_load_dwordx4 v13, %[rw], %[sw] offen lds\n"
    "s_add_u32 m0, %[d], 40960\n"
    "s_nop 0\n"
    "buffer_load_dwordx4 v10, %[rx], %[sx] offen lds\n"
    "s_add_u32 m0, m0, 16384\n"
    "s_nop 0\n"
    "buffer_load_dwordx4 v14, %[rw], %[sw] offen lds\n"
    "s_add_u32 m0, %[d], 45056\n"
    "s_nop 0\n"
    "buffer_load_dwordx4 v11, %[rx], %[sx] offen lds\n"
    "s_add_u32 m0, m0, 16384\n"
    "s_add_u32 %[sx], %[sx], 128\n"
    "buffer_load_dwordx4 v15, %[rw], %[sw] offen lds\n"
    "s_cmp_eq_u32 %[sx], 1280\n"
    "s_cmov_b32 %[sx], 0\n"
    "s_waitcnt vmcnt(8)\n"
    "s_barrier\n"
    "s_add_u32 m0, %[d], 65536\n"
    "s_mul_i32 %[sw], %[sx], 1920\n"
    "buffer_load_dwordx4 v8, %[rx], %[sx] offen lds\n"
    "s_add_u32 m0, m0, 16384\n"
    "s_nop 0\n"
    "buffer_load_dwordx4 v12, %[rw], %[sw] offen lds\n"
    "s_add_u32 m0, %[d], 69632\n"
    "s_nop 0\n"
    "buffer_load_dwordx4 v9, %[rx], %[sx] offen lds\n"
    "s_add_u32 m0, m0, 16384\n"
    "s_nop 0\n"
    "buffer_load_dwordx4 v13, %[rw], %[sw] offen lds\n"
    "s_add_u32 m0, %[d], 73728\n"
    "s_nop 0\n"
    "buffer_load_dwordx4 v10, %[rx], %[sx] offen lds\n"
    "s_add_u32 m0, m0, 16384\n"
    "s_nop 0\n"
    "buffer_load_dwordx4 v14, %[rw], %[sw] offen lds\n"
    "s_add_u32 m0, %[d], 77824\n"
    "s_nop 0\n"
    "buffer_load_dwordx4 v11, %[rx], %[sx] offen lds\n"
    "s_add_u32 m0, m0, 16384\n"
    "s_add_u32 %[sx], %[sx], 128\n"
    "buffer_load_dwordx4 v15, %[rw], %[sw] offen lds\n"
    "s_cmp_eq_u32 %[sx], 1280\n"
    "s_cmov_b32 %[sx], 0\n"
    "s_mov_b64 exec, 1\n"
    "global_atomic_add v16, %[ho], v18, s[92:93] sc0 sc1\n"
    "s_mov_b64 exec, -1\n"
    "s_waitcnt vmcnt(9)\n"
    "s_barrier\n"
    "s_add_u32 m0, %[d], 98304\n"
    "s_mul_i32 %[sw], %[sx], 1920\n"
    "buffer_load_dwordx4 v8, %[rx], %[sx] offen lds\n"
    "s_add_u32 m0, m0, 16384\n"
    "s_nop 0\n"
    "buffer_load_dwordx4 v12, %[rw], %[sw] offen lds\n"
    "s_add_u32 m0, %[d], 102400\n"
    "s_nop 0\n"
    "buffer_load_dwordx4 v9, %[rx], %[sx] offen lds\n"
    "s_add_u32 m0, m0, 16384\n"
    "s_nop 0\n"
    "buffer_load_dwordx4 v13, %[rw], %[sw] offen lds\n"
    "s_add_u32 m0, %[d], 106496\n"
    "s_nop 0\n"
    "buffer_load_dwordx4 v10, %[rx], %[sx] offen lds\n"
    "s_add_u32 m0, m0, 16384\n"
    "s_nop 0\n"
    "buffer_load_dwordx4 v14, %[rw], %[sw] offen lds\n"
    "s_add_u32 m0, %[d], 110592\n"
    "s_nop 0\n"
    "buffer_load_dwordx4 v11, %[rx], %[sx] offen lds\n"
    "s_add_u32 m0, m0, 16384\n"
    "s_add_u32 %[sx], %[sx], 128\n"
    "buffer_load_dwordx4 v15, %[rw], %[sw] offen lds\n"
    "s_cmp_eq_u32 %[sx], 1280\n"
    "s_cmov_b32 %[sx], 0\n"
    "s_waitcnt vmcnt(9)\n"
    "s_barrier\n"
    "s_add_u32 m0, %[d], 0\n"
    "s_mul_i32 %[sw], %[sx], 1920\n"
    "buffer_load_dwordx4 v8, %[rx], %[sx] offen lds\n"
    "s_add_u32 m0, m0, 16384\n"
    "s_nop 0\n"
    "buffer_load_dwordx4 v12, %[rw], %[sw] offen lds\n"
    "s_add_u32 m0, %[d], 4096\n"
    "s_nop 0\n"
    "buffer_load_dwordx4 v9, %[rx], %[sx] offen lds\n"
    "s_add_u32 m0, m0, 16384\n"
    "s_nop 0\n"
    "buffer_load_dwordx4 v13, %[rw], %[sw] offen lds\n"
    "s_add_u32 m0, %[d], 8192\n"
    "s_nop 0\n"
    "buffer_load_dwordx4 v10, %[rx], %[sx] offen lds\n"
    "s_add_u32 m0, m0, 16384\n"
    "s_nop 0\n"
    "buffer_load_dwordx4 v14, %[rw], %[sw] offen lds\n"
    "s_add_u32 m0, %[d], 12288\n"
    "s_nop 0\n"
    "buffer_load_dwordx4 v11, %[rx], %[sx] offen lds\n"
    "s_add_u32 m0, m0, 16384\n"
    "s_add_u32 %[sx], %[sx], 128\n"
    "buffer_load_dwordx4 v15, %[rw], %[sw] offen lds\n"
    "s_cmp_eq_u32 %[sx], 1280\n"
    "s_cmov_b32 %[sx], 0\n"
    "s_waitcnt vmcnt(8)\n"
    "s_barrier\n"
    "s_add_u32 m0, %[d], 32768\n"
    "s_mul_i32 %[sw], %[sx], 1920\n"
    "buffer_load_dwordx4 v8, %[rx], %[sx] offen lds\n"
    "s_add_u32 m0, m0, 16384\n"
    "s_nop 0\n"
    "buffer_load_dwordx4 v12, %[rw], %[sw] offen lds\n"
    "s_add_u32 m0, %[d], 36864\n"
    "s_nop 0\n"
    "buffer_load_dwordx4 v9, %[rx], %[sx] offen lds\n"
    "s_add_u32 m0, m0, 16384\n"
    "s_nop 0\n"
    "buffer_load_dwordx4 v13, %[rw], %[sw] offen lds\n"
    "s_add_u32 m0, %[d], 40960\n"
    "s_nop 0\n"
    "buffer_load_dwordx4 v10, %[rx], %[sx] offen lds\n"
    "s_add_u32 m0, m0, 16384\n"
    "s_nop 0\n"
    "buffer_load_dwordx4 v14, %[rw], %[sw] offen lds\n"
    "s_add_u32 m0, %[d], 45056\n"
    "s_nop 0\n"
    "buffer_load_dwordx4 v11, %[rx], %[sx] offen lds\n"
    "s_add_u32 m0, m0, 16384\n"
    "s_add_u32 %[sx], %[sx], 128\n"
    "buffer_load_dwordx4 v15, %[rw], %[sw] offen lds\n"
    "s_cmp_eq_u32 %[sx], 1280\n"
    "s_cmov_b32 %[sx], 0\n"
    "s_waitcnt vmcnt(8)\n"
    "s_barrier\n"
    "s_add_u32 m0, %[d], 65536\n"
    "s_mul_i32 %[sw], %[sx], 1920\n"
    "buffer_load_dwordx4 v8, %[rx], %[sx] offen lds\n"
    "s_add_u32 m0, m0, 16384\n"
    "s_nop 0\n"
    "buffer_load_dwordx4 v12, %[rw], %[sw] offen lds\n"
    "s_add_u32 m0, %[d], 69632\n"
    "s_nop 0\n"
    "buffer_load_dwordx4 v9, %[rx], %[sx] offen lds\n"
    "s_add_u32 m0, m0, 16384\n"
    "s_nop 0\n"
    "buffer_load_dwordx4 v13, %[rw], %[sw] offen lds\n"
    "s_add_u32 m0, %[d], 73728\n"
    "s_nop 0\n"
    "buffer_load_dwordx4 v10, %[rx], %[sx] offen lds\n"
    "s_add_u32 m0, m0, 16384\n"
    "s_nop 0\n"
    "buffer_load_dwordx4 v14, %[rw], %[sw] offen lds\n"
    "s_add_u32 m0, %[d], 77824\n"
    "s_nop 0\n"
    "buffer_load_dwordx4 v11, %[rx], %[sx] offen lds\n"
    "s_add_u32 m0, m0, 16384\n"
    "s_add_u32 %[sx], %[sx], 128\n"
    "buffer_load_dwordx4 v15, %[rw], %[sw] offen lds\n"
    "s_cmp_eq_u32 %[sx], 1280\n"
    "s_cmov_b32 %[sx], 0\n"
    "s_waitcnt vmcnt(8)\n"
    "s_barrier\n"
    "s_add_u32 m0, %[d], 98304\n"
    "s_mul_i32 %[sw], %[sx], 1920\n"
    "buffer_load_dwordx4 v8, %[rx], %[sx] offen lds\n"
    "s_add_u32 m0, m0, 16384\n"
    "s_nop 0\n"
    "buffer_load_dwordx4 v12, %[rw], %[sw] offen lds\n"
    "s_add_u32 m0, %[d], 102400\n"
    "s_nop 0\n"
    "buffer_load_dwordx4 v9, %[rx], %[sx] offen lds\n"
    "s_add_u32 m0, m0, 16384\n"
    "s_nop 0\n"
    "buffer_load_dwordx4 v13, %[rw], %[sw] offen lds\n"
    "s_add_u32 m0, %[d], 106496\n"
    "s_nop 0\n"
    "buffer_load_dwordx4 v10, %[rx], %[sx] offen lds\n"
    "s_add_u32 m0, m0, 16384\n"
    "s_nop 0\n"
    "buffer_load_dwordx4 v14, %[rw], %[sw] offen lds\n"
    "s_add_u32 m0, %[d], 110592\n"
    "s_nop 0\n"
    "buffer_load_dwordx4 v11, %[rx], %[sx] offen lds\n"
    "s_add_u32 m0, m0, 16384\n"
    "s_add_u32 %[sx], %[sx], 128\n"
    "buffer_load_dwordx4 v15, %[rw], %[sw] offen lds\n"
    "s_cmp_eq_u32 %[sx], 1280\n"
    "s_cmov_b32 %[sx], 0\n"
    "s_waitcnt vmcnt(8)\n"
    "s_barrier\n"
    "s_add_u32 m0, %[d], 0\n"
    "s_mul_i32 %[sw], %[sx], 1920\n"
    "buffer_load_dwordx4 v8, %[rx], %[sx] offen lds\n"
    "s_add_u32 m0, m0, 16384\n"
    "s_nop 0\n"
    "buffer_load_dwordx4 v12, %[rw], %[sw] offen lds\n"
    "s_add_u32 m0, %[d], 4096\n"
    "s_nop 0\n"
    "buffer_load_dwordx4 v9, %[rx], %[sx] offen lds\n"
    "s_add_u32 m0, m0, 16384\n"
    "s_nop 0\n"
    "buffer_load_dwordx4 v13, %[rw], %[sw] offen lds\n"
    "s_add_u32 m0, %[d], 8192\n"
    "s_nop 0\n"
    "buffer_load_dwordx4 v10, %[rx], %[sx] offen lds\n"
    "s_add_u32 m0, m0, 16384\n"
    "s_nop 0\n"
    "buffer_load_dwordx4 v14, %[rw], %[sw] offen lds\n"
    "s_add_u32 m0, %[d], 12288\n"
    "s_nop 0\n"
    "buffer_load_dwordx4 v11, %[rx], %[sx] offen lds\n"
    "s_add_u32 m0, m0, 16384\n"
    "s_add_u32 %[sx], %[sx], 128\n"
    "buffer_load_dwordx4 v15, %[rw], %[sw] offen lds\n"
    "s_cmp_eq_u32 %[sx], 1280\n"
    "s_cmov_b32 %[sx], 0\n"
    "s_waitcnt vmcnt(8)\n"
    "s_barrier\n"
    "s_add_u32 m0, %[d], 32768\n"
    "s_mul_i32 %[sw], %[sx], 1920\n"
    "buffer_load_dwordx4 v8, %[rx], %[sx] offen lds\n"
    "s_add_u32 m0, m0, 16384\n"
    "s_nop 0\n"
    "buffer_load_dwordx4 v12, %[rw], %[sw] offen lds\n"
    "s_add_u32 m0, %[d], 36864\n"
    "s_nop 0\n"
    "buffer_load_dwordx4 v9, %[rx], %[sx] offen lds\n"
    "s_add_u32 m0, m0, 16384\n"
    "s_nop 0\n"
    "buffer_load_dwordx4 v13, %[rw], %[sw] offen lds\n"
    "s_add_u32 m0, %[d], 40960\n"
    "s_nop 0\n"
    "buffer_load_dwordx4 v10, %[rx], %[sx] offen lds\n"
    "s_add_u32 m0, m0, 16384\n"
    "s_nop 0\n"
    "buffer_load_dwordx4 v14, %[rw], %[sw] offen lds\n"
    "s_add_u32 m0, %[d], 45056\n"
    "s_nop 0\n"
    "buffer_load_dwordx4 v11, %[rx], %[sx] offen lds\n"
    "s_add_u32 m0, m0, 16384\n"
    "s_add_u32 %[sx], %[sx], 128\n"
    "buffer_load_dwordx4 v15, %[rw], %[sw] offen lds\n"
    "s_cmp_eq_u32 %[sx], 1280\n"
    "s_cmov_b32 %[sx], 0\n"
    "s_mov_b64 exec, 1\n"
    "global_atomic_add v17, %[ho], v18, s[92:93] sc0 sc1\n"
    "s_mov_b64 exec, -1\n"
    "s_waitcnt vmcnt(9)\n"
    "s_barrier\n"
    "s_cmp_lt_u32 %[cb], 10\n"
    "s_cbranch_scc1 3f\n"
    "s_sub_u32 %[sw], %[cb], 10\n"
    "s_mul_i32 %[sw], %[sw], 410\n"
    "s_lshr_b32 %[sw], %[sw], 6\n"
    "s_mul_i32 %[sx], %[t4], 2688\n"
    "v_mbcnt_lo_u32_b32 v20, -1, 0\n"
    "v_mbcnt_hi_u32_b32 v20, -1, v20\n"
    "s_add_u32 %[sx], %[sx], %[kc]\n"
    "v_lshrrev_b32 v21, 3, v20\n"
    "v_add_u32 v21, %[sw], v21\n"
    "v_min_u32 v21, 31, v21\n"
    "v_mul_u32_u24 v21, 172032, v21\n"
    "v_bfe_i32 v22, v20, 0, 1\n"
    "v_and_b32 v20, 7, v20\n"
    "v_lshl_add_u32 v20, v20, 4, %[sx]\n"
    "v_not_b32 v22, v22\n"
    "v_add_u32 v20, v20, v21\n"
    "v_and_b32 v22, 0x3f803f80, v22\n"
    "v_mov_b32 v23, v22\n"
    "v_mov_b32 v24, v22\n"
    "v_mov_b32 v25, v22\n"
    "global_store_dwordx4 v20, v[22:25], s[92:93] sc1\n"
    "s_waitcnt vmcnt(2)\n"
    "s_branch 4f\n"
    "3:\n"
    "s_waitcnt vmcnt(1)\n"
    "4:\n"
    "s_barrier\n"
    "s_mov_b64 exec, 1\n"
    "s_mov_b32 %[sw], 200000\n"
    "s_waitcnt vmcnt(0)\n"
    "1:\n"
    "v_cmp_eq_u32 vcc, v16, v17\n"
    "s_cbranch_vccz 2f\n"
    "global_atomic_add v17, %[ho], v18, s[92:93] sc0 sc1\n"
    "s_waitcnt vmcnt(0)\n"
    "s_sub_u32 %[sw], %[sw], 1\n"
    "s_cbranch_scc0 1b\n"
    "2:\n"
    "v_mov_b32 v18, 1\n"
    "v_subrev_u32 v16, 16384, %[ho]\n"
    "global_atomic_add v16, v18, s[92:93] sc1\n"
    "s_mov_b64 exec, -1\n"
    : [sx] "+s"(sx), [sw] "=&s"(sw)
    : [rx] "{s[84:87]}"(rx), [rw] "{s[88:91]}"(rw), "{s[92:93]}"(rs), "{v[8:15]}"(v), [d] "s"(hw * 1024), [ho] "{v19}"(WS_FL + (C_HB + id) * 256), [cb] "s"(cb), [t4] "s"(rb * 4 + hw), [kc] "n"(WS_KV + 2 * QT)
    : "vcc", "scc", "memory", "v16", "v17", "v18", "v20", "v21", "v22", "v23", "v24", "v25");
}
struct P1Out { u32x2 s; u64 sm; };
template <bool TR>
DEV void p1_compute(char* wsp, int rb, int cb, int wv, int lane, P1Out& p) {
  const int wm = wv >> 1, wn = wv & 1, r = lane & 15, g = lane >> 4;
  u32x16 v;
#pragma unroll
  for (int i = 0; i < 4; ++i) {
    v[i] = (wm * 64 + (((i & 1) ^ wn) << 5) + r) * 128 + (((4 * (i >> 1) + g) ^ (r >> 1)) << 4);
    const int T = i ^ (wm << 1), q = r >> 2, c = r & 3;
    const int ch = TR ? wn * 8 + (T >> 1) * 4 + c : wn * 8 + T * 2 + (c >> 1), hf = TR ? (T ^ c) & 1 : c & 1;
    v[4 + i] = 16384 + (8 * g + q) * 256 + ((ch ^ ((q << 2) | ((g & 1) << (TR ? 0 : 1)))) << 4) + hf * 8;
  }
  v[8] = L_MISC + 1024 + (wn * 64 + wm * 32 + (g & 1) * 16 + r) * 4;
  v[9] = L_MISC + (wm * 64 + wn * 32 + (g & 1) * 16 + r) * 4;
  v[10] = L_MISC + (wm * 64 + wn * 32 + (TR ? r : 4 * g)) * 4;
  v[11] = v[10] ^ 128;
  v[12] = L_MISC + 1024 + (wn * 64 + wm * 32 + (TR ? 4 * g : r)) * 4;
  v[13] = v[12] ^ 128;
  v[14] = ((r >> 1) & 1) == (g >> 1) ? 0x3f803f80u : 0u;
  v[15] = (r & 1) ? 0xEEEEu : 0x4444u;
  const bool isq = cb < 5;
  const unsigned tstride = isq ? QT : KVT, base = (isq ? WS_Q : TR ? WS_KV : WS_KV + QT) + (rb * 4 + wm * 2) * tstride;
  u32x4 a;
  u64 sp[2] = {0, 0};
  if (TR) {
#pragma unroll
    for (int j = 0; j < 2; ++j) {
      const int cc = (isq ? cb : cb - 5) * 16 + wn * 8 + ((j ^ wm) << 2) + g, pp = (cc * 205) >> 10, q = cc - 5 * pp, odd = q > 2;
      sp[j] = __builtin_amdgcn_ballot_w64(q == 2);
      a[2 * j] = q == 2 ? (2 * pp + (g & 1)) * 64 * tstride + 1024 + r * 16 : (2 * pp + odd) * 64 * tstride + (q - 3 * odd) * 512 + r * 16;
      a[2 * j + 1] = a[2 * j] + (q != 2 ? 256 : (g & 1) ? -64 * tstride : 64 * tstride);
    }
  } else {
#pragma unroll
    for (int t = 0; t < 4; ++t) {
      const int f = (cb - 10) * 128 + wn * 64 + ((t ^ (wm << 1)) << 4) + r, head = (f * 3277) >> 16, d = f - head * 20;
      const int dp = (head & 1) ? (d >= 4 ? d - 4 : d + 16) : d;
      a[t] = head * 64 * KVT + dp * 16 + (dp < 16 ? g * 256 : 768 + g * 64);
    }
  }
  const int c0 = (cb - (cb >= 10 ? 10 : cb >= 5 ? 5 : 0)) * 128 + wn * 64, hh = ((c0 * 3277) >> 16) + (lane & 7);
  p.s[1] = ((hh * 20 >= c0 && hh * 20 + 20 <= c0 + 64) ? 2 : 1);
  p.s[0] = ((isq ? C_FQ : C_F1) + (lane >> 3) * 256 + hh * 8 + (rb >> 1)) * 256;
  p.sm = __builtin_amdgcn_ballot_w64(hh * 20 < c0 + 64) & (isq ? 0xff : 0xffff);
  asm volatile("" : "+{v[252:253]}"(p.s));
  int cnt, dl;
#define P1_OPS : "+{v[48:63]}"(v), [cnt] "=&s"(cnt), [dl] "=&s"(dl) \
      : "{v[224:227]}"(a), [sp0] "s"(sp[0]), [sp1] "s"(sp[1]), [sc] "s"(isq ? 0x3ea52b47u : 0x3f800000u), [rs] "s"(rawrsrc(wsp, WS_END)), [tb0] "s"(base + wn * tstride), [tb1] "s"(base + (wn ^ 1) * tstride) \
      : "scc", "memory", "s72", "s73", "s74", "s75", "s76", "s77", "s78", "s79", "s80", "s81", "v0", "v1", "v2", "v3", "v4", "v5", "v6", "v7", "v8", "v9", "v10", "v11", "v12", "v13", "v14", "v15", "v16", "v17", "v18", "v19", "v20", "v21", "v22", "v23", "v24", "v25", "v26", "v27", "v28", "v29", "v30", "v31", "v32", "v33", "v34", "v35", "v36", "v37", "v38", "v39", "v40", "v41", "v42", "v43", "v44", "v45", "v46", "v47", "v64", "v65", "v66", "v67", "v68", "v69", "v70", "v71", "v72", "v73", "v74", "v75", "v76", "v77", "v78", "v79", "v80", "v81", "v82", "v83", "v84", "v85", "v86", "v87", "v88", "v89", "v90", "v91", "v92", "v93", "v94", "v95", "v96", "v97", "v98", "v99", "v100", "v101", "v102", "v103", "v104", "v105", "v106", "v107", "v108", "v109", "v110", "v111", "v112", "v113", "v114", "v115", "v116", "v117", "v118", "v119", "v120", "v121", "v122", "v123", "v124", "v125", "v126", "v127", "v128", "v129", "v130", "v131", "v132", "v133", "v134", "v135", "v136", "v137", "v138", "v139", "v140", "v141", "v142", "v143", "v144", "v145", "v146", "v147", "v148", "v149", "v150", "v151", "v152", "v153", "v154", "v155", "v156", "v157", "v158", "v159", "v160", "v161", "v162", "v163", "v164", "v165", "v166", "v167", "v168", "v169", "v170", "v171", "v172", "v173", "v174", "v175", "v176", "v177", "v178", "v179", "v180", "v181", "v182", "v183", "v184", "v185", "v186", "v187", "v188", "v189", "v190", "v191", "v192", "v193", "v194", "v195", "v196", "v197", "v198", "v199", "v200", "v201", "v202", "v203", "v204", "v205", "v206", "v207", "v208", "v209", "v210", "v211", "v212", "v213", "v214", "v215", "v216", "v217", "v218", "v219", "v228", "v229", "v230", "v231", "v232", "v233", "v234", "v235", "v236", "v237", "v238", "v239", "v240", "v241", "v242", "v243"
  if (TR) asm volatile(
    "v_mov_b32 v64, 0\n"
    "v_mov_b32 v65, 0\n"
    "v_mov_b32 v66, 0\n"
    "v_mov_b32 v67, 0\n"
    "v_mov_b32 v68, 0\n"
    "v_mov_b32 v69, 0\n"
    "v_mov_b32 v70, 0\n"
    "v_mov_b32 v71, 0\n"
    "v_mov_b32 v72, 0\n"
    "v_mov_b32 v73, 0\n"
    "v_mov_b32 v74, 0\n"
    "v_mov_b32 v75, 0\n"
    "v_mov_b32 v76, 0\n"
    "v_mov_b32 v77, 0\n"
    "v_mov_b32 v78, 0\n"
    "v_mov_b32 v79, 0\n"
    "v_mov_b32 v80, 0\n"
    "v_mov_b32 v81, 0\n"
    "v_mov_b32 v82, 0\n"
    "v_mov_b32 v83, 0\n"
    "v_mov_b32 v84, 0\n"
    "v_mov_b32 v85, 0\n"
    "v_mov_b32 v86, 0\n"
    "v_mov_b32 v87, 0\n"
    "v_mov_b32 v88, 0\n"
    "v_mov_b32 v89, 0\n"
    "v_mov_b32 v90, 0\n"
    "v_mov_b32 v91, 0\n"
    "v_mov_b32 v92, 0\n"
    "v_mov_b32 v93, 0\n"
    "v_mov_b32 v94, 0\n"
    "v_mov_b32 v95, 0\n"
    "v_mov_b32 v96, 0\n"
    "v_mov_b32 v97, 0\n"
    "v_mov_b32 v98, 0\n"
    "v_mov_b32 v99, 0\n"
    "v_mov_b32 v100, 0\n"
    "v_mov_b32 v101, 0\n"
    "v_mov_b32 v102, 0\n"
    "v_mov_b32 v103, 0\n"
    "v_mov_b32 v104, 0\n"
    "v_mov_b32 v105, 0\n"
    "v_mov_b32 v106, 0\n"
    "v_mov_b32 v107, 0\n"
    "v_mov_b32 v108, 0\n"
    "v_mov_b32 v109, 0\n"
    "v_mov_b32 v110, 0\n"
    "v_mov_b32 v111, 0\n"
    "v_mov_b32 v112, 0\n"
    "v_mov_b32 v113, 0\n"
    "v_mov_b32 v114, 0\n"
    "v_mov_b32 v115, 0\n"
    "v_mov_b32 v116, 0\n"
    "v_mov_b32 v117, 0\n"
    "v_mov_b32 v118, 0\n"
    "v_mov_b32 v119, 0\n"
    "v_mov_b32 v120, 0\n"
    "v_mov_b32 v121, 0\n"
    "v_mov_b32 v122, 0\n"
    "v_mov_b32 v123, 0\n"
    "v_mov_b32 v124, 0\n"
    "v_mov_b32 v125, 0\n"
    "v_mov_b32 v126, 0\n"
    "v_mov_b32 v127, 0\n"
    "v_mov_b32 v128, 0\n"
    "v_mov_b32 v129, 0\n"
    "v_mov_b32 v130, 0\n"
    "v_mov_b32 v131, 0\n"
    "v_mov_b32 v132, 0\n"
    "v_mov_b32 v133, 0\n"
    "v_mov_b32 v134, 0\n"
    "v_mov_b32 v135, 0\n"
    "v_mov_b32 v136, 0\n"
    "v_mov_b32 v137, 0\n"
    "v_mov_b32 v138, 0\n"
    "v_mov_b32 v139, 0\n"
    "v_mov_b32 v140, 0\n"
    "v_mov_b32 v141, 0\n"
    "v_mov_b32 v142, 0\n"
    "v_mov_b32 v143, 0\n"
    "v_mov_b32 v216, v62\n"
    "v_mov_b32 v217, v62\n"
    "v_mov_b32 v218, v62\n"
    "v_mov_b32 v219, v62\n"
    "s_mov_b32 s72, 0xaaaaaaaa\n"
    "s_mov_b32 s73, 0xaaaaaaaa\n"
    "s_mov_b32 s74, 0xcccccccc\n"
    "s_mov_b32 s75, 0xcccccccc\n"
    "s_mov_b32 s76, 0xf0f0f0f0\n"
    "s_mov_b32 s77, 0xf0f0f0f0\n"
    "s_mov_b32 s78, 0xffff0000\n"
    "s_mov_b32 s79, 0xffff0000\n"
    "s_mov_b32 s80, 0xff00ff\n"
    "s_mov_b32 s81, 0xff00ff00\n"
    "s_mov_b32 %[cnt], 0\n"
    "s_barrier\n"
    "ds_read_b128 v[152:155], v48 offset:0\n"
    "ds_read_b128 v[184:187], v50 offset:0\n"
    "ds_read_b128 v[156:159], v48 offset:2048\n"
    "ds_read_b128 v[188:191], v50 offset:2048\n"
    "ds_read_b64_tr_b16 v[168:169], v52 offset:0\n"
    "ds_read_b64_tr_b16 v[170:171], v52 offset:1024\n"
    "ds_read_b64_tr_b16 v[200:201], v52 offset:8192\n"
    "ds_read_b64_tr_b16 v[202:203], v52 offset:9216\n"
    "ds_read_b64_tr_b16 v[172:173], v53 offset:0\n"
    "ds_read_b64_tr_b16 v[174:175], v53 offset:1024\n"
    "ds_read_b64_tr_b16 v[204:205], v53 offset:8192\n"
    "ds_read_b64_tr_b16 v[206:207], v53 offset:9216\n"
    "ds_read_b64_tr_b16 v[176:177], v54 offset:0\n"
    "ds_read_b64_tr_b16 v[178:179], v54 offset:1024\n"
    "s_branch 2f\n"
    ".p2align 5\n"
    "1:\n"
    "s_waitcnt lgkmcnt(12)\n"
    "v_mfma_f32_16x16x32_bf16 v[132:135], v[0:3], v[0:3], v[132:135]\n"
    "ds_read_b64_tr_b16 v[236:237], v54 offset:8192\n"
    "s_add_u32 %[cnt], %[cnt], 1\n"
    "v_mfma_f32_16x16x32_bf16 v[132:135], v[32:35], v[32:35], v[132:135]\n"
    "ds_read_b64_tr_b16 v[238:239], v54 offset:9216\n"
    "s_mov_b32 %[dl], 32768\n"
    "s_waitcnt lgkmcnt(12)\n"
    "v_mfma_f32_16x16x32_bf16 v[136:139], v[4:7], v[4:7], v[136:139]\n"
    "ds_read_b64_tr_b16 v[28:29], v55 offset:0\n"
    "s_bitcmp1_b32 0x111, %[cnt]\n"
    "v_mfma_f32_16x16x32_bf16 v[136:139], v[36:39], v[36:39], v[136:139]\n"
    "ds_read_b64_tr_b16 v[30:31], v55 offset:1024\n"
    "s_cmov_b32 %[dl], -98304\n"
    "v_smfmac_f32_16x16x64_bf16 v[128:131], v[216:219], v[0:7], v63\n"
    "ds_read_b64_tr_b16 v[240:241], v55 offset:8192\n"
    "v_smfmac_f32_16x16x64_bf16 v[128:131], v[216:219], v[32:39], v63\n"
    "ds_read_b64_tr_b16 v[242:243], v55 offset:9216\n"
    "s_waitcnt lgkmcnt(8)\n"
    "v_smfmac_f32_16x16x64_bf16 v[140:143], v[216:219], v[16:23], v63\n"
    "ds_read_b128 v[8:11], v49 offset:0\n"
    "v_smfmac_f32_16x16x64_bf16 v[140:143], v[216:219], v[228:235], v63\n"
    "ds_read_b128 v[40:43], v51 offset:0\n"
    "v_mfma_f32_16x16x32_bf16 v[64:67], v[16:19], v[0:3], v[64:67]\n"
    "ds_read_b128 v[12:15], v49 offset:2048\n"
    "v_mfma_f32_16x16x32_bf16 v[64:67], v[228:231], v[32:35], v[64:67]\n"
    "ds_read_b128 v[44:47], v51 offset:2048\n"
    "v_mfma_f32_16x16x32_bf16 v[68:71], v[20:23], v[0:3], v[68:71]\n"
    "v_mfma_f32_16x16x32_bf16 v[68:71], v[232:235], v[32:35], v[68:71]\n"
    "s_waitcnt lgkmcnt(8)\n"
    "v_mfma_f32_16x16x32_bf16 v[72:75], v[24:27], v[0:3], v[72:75]\n"
    "v_mfma_f32_16x16x32_bf16 v[72:75], v[236:239], v[32:35], v[72:75]\n"
    "s_waitcnt lgkmcnt(4)\n"
    "v_mfma_f32_16x16x32_bf16 v[76:79], v[28:31], v[0:3], v[76:79]\n"
    "v_mfma_f32_16x16x32_bf16 v[76:79], v[240:243], v[32:35], v[76:79]\n"
    "v_mfma_f32_16x16x32_bf16 v[80:83], v[16:19], v[4:7], v[80:83]\n"
    "v_add_u32 v52, %[dl], v52\n"
    "v_mfma_f32_16x16x32_bf16 v[80:83], v[228:231], v[36:39], v[80:83]\n"
    "v_add_u32 v53, %[dl], v53\n"
    "v_mfma_f32_16x16x32_bf16 v[84:87], v[20:23], v[4:7], v[84:87]\n"
    "v_add_u32 v54, %[dl], v54\n"
    "v_mfma_f32_16x16x32_bf16 v[84:87], v[232:235], v[36:39], v[84:87]\n"
    "v_add_u32 v55, %[dl], v55\n"
    "v_mfma_f32_16x16x32_bf16 v[88:91], v[24:27], v[4:7], v[88:91]\n"
    "v_add_u32 v48, %[dl], v48\n"
    "v_mfma_f32_16x16x32_bf16 v[88:91], v[236:239], v[36:39], v[88:91]\n"
    "v_add_u32 v50, %[dl], v50\n"
    "v_mfma_f32_16x16x32_bf16 v[92:95], v[28:31], v[4:7], v[92:95]\n"
    "v_add_u32 v49, %[dl], v49\n"
    "v_mfma_f32_16x16x32_bf16 v[92:95], v[240:243], v[36:39], v[92:95]\n"
    "v_add_u32 v51, %[dl], v51\n"
    "s_waitcnt lgkmcnt(2)\n"
    "v_mfma_f32_16x16x32_bf16 v[96:99], v[16:19], v[8:11], v[96:99]\n"
    "s_cmp_lt_u32 %[cnt], 9\n"
    "v_mfma_f32_16x16x32_bf16 v[96:99], v[228:231], v[40:43], v[96:99]\n"
    "s_waitcnt lgkmcnt(0)\n"
    "s_barrier\n"
    "v_mfma_f32_16x16x32_bf16 v[100:103], v[20:23], v[8:11], v[100:103]\n"
    "ds_read_b128 v[152:155], v48 offset:0\n"
    "v_mfma_f32_16x16x32_bf16 v[100:103], v[232:235], v[40:43], v[100:103]\n"
    "ds_read_b128 v[184:187], v50 offset:0\n"
    "v_mfma_f32_16x16x32_bf16 v[104:107], v[24:27], v[8:11], v[104:107]\n"
    "ds_read_b128 v[156:159], v48 offset:2048\n"
    "v_mfma_f32_16x16x32_bf16 v[104:107], v[236:239], v[40:43], v[104:107]\n"
    "ds_read_b128 v[188:191], v50 offset:2048\n"
    "v_mfma_f32_16x16x32_bf16 v[108:111], v[28:31], v[8:11], v[108:111]\n"
    "ds_read_b64_tr_b16 v[168:169], v52 offset:0\n"
    "v_mfma_f32_16x16x32_bf16 v[108:111], v[240:243], v[40:43], v[108:111]\n"
    "ds_read_b64_tr_b16 v[170:171], v52 offset:1024\n"
    "v_mfma_f32_16x16x32_bf16 v[112:115], v[16:19], v[12:15], v[112:115]\n"
    "ds_read_b64_tr_b16 v[200:201], v52 offset:8192\n"
    "v_mfma_f32_16x16x32_bf16 v[112:115], v[228:231], v[44:47], v[112:115]\n"
    "ds_read_b64_tr_b16 v[202:203], v52 offset:9216\n"
    "v_mfma_f32_16x16x32_bf16 v[116:119], v[20:23], v[12:15], v[116:119]\n"
    "ds_read_b64_tr_b16 v[172:173], v53 offset:0\n"
    "v_mfma_f32_16x16x32_bf16 v[116:119], v[232:235], v[44:47], v[116:119]\n"
    "ds_read_b64_tr_b16 v[174:175], v53 offset:1024\n"
    "v_mfma_f32_16x16x32_bf16 v[120:123], v[24:27], v[12:15], v[120:123]\n"
    "ds_read_b64_tr_b16 v[204:205], v53 offset:8192\n"
    "v_mfma_f32_16x16x32_bf16 v[120:123], v[236:239], v[44:47], v[120:123]\n"
    "ds_read_b64_tr_b16 v[206:207], v53 offset:9216\n"
    "v_mfma_f32_16x16x32_bf16 v[124:127], v[28:31], v[12:15], v[124:127]\n"
    "ds_read_b64_tr_b16 v[176:177], v54 offset:0\n"
    "v_mfma_f32_16x16x32_bf16 v[124:127], v[240:243], v[44:47], v[124:127]\n"
    "ds_read_b64_tr_b16 v[178:179], v54 offset:1024\n"
    "2:\n"
    "s_waitcnt lgkmcnt(12)\n"
    "v_mfma_f32_16x16x32_bf16 v[132:135], v[152:155], v[152:155], v[132:135]\n"
    "ds_read_b64_tr_b16 v[208:209], v54 offset:8192\n"
    "s_add_u32 %[cnt], %[cnt], 1\n"
    "v_mfma_f32_16x16x32_bf16 v[132:135], v[184:187], v[184:187], v[132:135]\n"
    "ds_read_b64_tr_b16 v[210:211], v54 offset:9216\n"
    "s_mov_b32 %[dl], 32768\n"
    "s_waitcnt lgkmcnt(12)\n"
    "v_mfma_f32_16x16x32_bf16 v[136:139], v[156:159], v[156:159], v[136:139]\n"
    "ds_read_b64_tr_b16 v[180:181], v55 offset:0\n"
    "s_bitcmp1_b32 0x111, %[cnt]\n"
    "v_mfma_f32_16x16x32_bf16 v[136:139], v[188:191], v[188:191], v[136:139]\n"
    "ds_read_b64_tr_b16 v[182:183], v55 offset:1024\n"
    "s_cmov_b32 %[dl], -98304\n"
    "v_smfmac_f32_16x16x64_bf16 v[128:131], v[216:219], v[152:159], v63\n"
    "ds_read_b64_tr_b16 v[212:213], v55 offset:8192\n"
    "v_smfmac_f32_16x16x64_bf16 v[128:131], v[216:219], v[184:191], v63\n"
    "ds_read_b64_tr_b16 v[214:215], v55 offset:9216\n"
    "s_waitcnt lgkmcnt(8)\n"
    "v_smfmac_f32_16x16x64_bf16 v[140:143], v[216:219], v[168:175], v63\n"
    "ds_read_b128 v[160:163], v49 offset:0\n"
    "v_smfmac_f32_16x16x64_bf16 v[140:143], v[216:219], v[200:207], v63\n"
    "ds_read_b128 v[192:195], v51 offset:0\n"
    "v_mfma_f32_16x16x32_bf16 v[64:67], v[168:171], v[152:155], v[64:67]\n"
    "ds_read_b128 v[164:167], v49 offset:2048\n"
    "v_mfma_f32_16x16x32_bf16 v[64:67], v[200:203], v[184:187], v[64:67]\n"
    "ds_read_b128 v[196:199], v51 offset:2048\n"
    "v_mfma_f32_16x16x32_bf16 v[68:71], v[172:175], v[152:155], v[68:71]\n"
    "v_mfma_f32_16x16x32_bf16 v[68:71], v[204:207], v[184:187], v[68:71]\n"
    "s_waitcnt lgkmcnt(8)\n"
    "v_mfma_f32_16x16x32_bf16 v[72:75], v[176:179], v[152:155], v[72:75]\n"
    "v_mfma_f32_16x16x32_bf16 v[72:75], v[208:211], v[184:187], v[72:75]\n"
    "s_waitcnt lgkmcnt(4)\n"
    "v_mfma_f32_16x16x32_bf16 v[76:79], v[180:183], v[152:155], v[76:79]\n"
    "v_mfma_f32_16x16x32_bf16 v[76:79], v[212:215], v[184:187], v[76:79]\n"
    "v_mfma_f32_16x16x32_bf16 v[80:83], v[168:171], v[156:159], v[80:83]\n"
    "v_add_u32 v52, %[dl], v52\n"
    "v_mfma_f32_16x16x32_bf16 v[80:83], v[200:203], v[188:191], v[80:83]\n"
    "v_add_u32 v53, %[dl], v53\n"
    "v_mfma_f32_16x16x32_bf16 v[84:87], v[172:175], v[156:159], v[84:87]\n"
    "v_add_u32 v54, %[dl], v54\n"
    "v_mfma_f32_16x16x32_bf16 v[84:87], v[204:207], v[188:191], v[84:87]\n"
    "v_add_u32 v55, %[dl], v55\n"
    "v_mfma_f32_16x16x32_bf16 v[88:91], v[176:179], v[156:159], v[88:91]\n"
    "v_add_u32 v48, %[dl], v48\n"
    "v_mfma_f32_16x16x32_bf16 v[88:91], v[208:211], v[188:191], v[88:91]\n"
    "v_add_u32 v50, %[dl], v50\n"
    "v_mfma_f32_16x16x32_bf16 v[92:95], v[180:183], v[156:159], v[92:95]\n"
    "v_add_u32 v49, %[dl], v49\n"
    "v_mfma_f32_16x16x32_bf16 v[92:95], v[212:215], v[188:191], v[92:95]\n"
    "v_add_u32 v51, %[dl], v51\n"
    "s_waitcnt lgkmcnt(2)\n"
    "v_mfma_f32_16x16x32_bf16 v[96:99], v[168:171], v[160:163], v[96:99]\n"
    "s_cmp_lt_u32 %[cnt], 9\n"
    "v_mfma_f32_16x16x32_bf16 v[96:99], v[200:203], v[192:195], v[96:99]\n"
    "s_waitcnt lgkmcnt(0)\n"
    "s_barrier\n"
    "v_mfma_f32_16x16x32_bf16 v[100:103], v[172:175], v[160:163], v[100:103]\n"
    "ds_read_b128 v[0:3], v48 offset:0\n"
    "v_mfma_f32_16x16x32_bf16 v[100:103], v[204:207], v[192:195], v[100:103]\n"
    "ds_read_b128 v[32:35], v50 offset:0\n"
    "v_mfma_f32_16x16x32_bf16 v[104:107], v[176:179], v[160:163], v[104:107]\n"
    "ds_read_b128 v[4:7], v48 offset:2048\n"
    "v_mfma_f32_16x16x32_bf16 v[104:107], v[208:211], v[192:195], v[104:107]\n"
    "ds_read_b128 v[36:39], v50 offset:2048\n"
    "v_mfma_f32_16x16x32_bf16 v[108:111], v[180:183], v[160:163], v[108:111]\n"
    "ds_read_b64_tr_b16 v[16:17], v52 offset:0\n"
    "v_mfma_f32_16x16x32_bf16 v[108:111], v[212:215], v[192:195], v[108:111]\n"
    "ds_read_b64_tr_b16 v[18:19], v52 offset:1024\n"
    "v_mfma_f32_16x16x32_bf16 v[112:115], v[168:171], v[164:167], v[112:115]\n"
    "ds_read_b64_tr_b16 v[228:229], v52 offset:8192\n"
    "v_mfma_f32_16x16x32_bf16 v[112:115], v[200:203], v[196:199], v[112:115]\n"
    "ds_read_b64_tr_b16 v[230:231], v52 offset:9216\n"
    "v_mfma_f32_16x16x32_bf16 v[116:119], v[172:175], v[164:167], v[116:119]\n"
    "ds_read_b64_tr_b16 v[20:21], v53 offset:0\n"
    "v_mfma_f32_16x16x32_bf16 v[116:119], v[204:207], v[196:199], v[116:119]\n"
    "ds_read_b64_tr_b16 v[22:23], v53 offset:1024\n"
    "v_mfma_f32_16x16x32_bf16 v[120:123], v[176:179], v[164:167], v[120:123]\n"
    "ds_read_b64_tr_b16 v[232:233], v53 offset:8192\n"
    "v_mfma_f32_16x16x32_bf16 v[120:123], v[208:211], v[196:199], v[120:123]\n"
    "ds_read_b64_tr_b16 v[234:235], v53 offset:9216\n"
    "v_mfma_f32_16x16x32_bf16 v[124:127], v[180:183], v[164:167], v[124:127]\n"
    "ds_read_b64_tr_b16 v[24:25], v54 offset:0\n"
    "v_mfma_f32_16x16x32_bf16 v[124:127], v[212:215], v[196:199], v[124:127]\n"
    "ds_read_b64_tr_b16 v[26:27], v54 offset:1024\n"
    "s_cbranch_scc1 1b\n"
    "s_waitcnt lgkmcnt(12)\n"
    "v_mfma_f32_16x16x32_bf16 v[132:135], v[0:3], v[0:3], v[132:135]\n"
    "ds_read_b64_tr_b16 v[236:237], v54 offset:8192\n"
    "v_mfma_f32_16x16x32_bf16 v[132:135], v[32:35], v[32:35], v[132:135]\n"
    "ds_read_b64_tr_b16 v[238:239], v54 offset:9216\n"
    "s_waitcnt lgkmcnt(12)\n"
    "v_mfma_f32_16x16x32_bf16 v[136:139], v[4:7], v[4:7], v[136:139]\n"
    "ds_read_b64_tr_b16 v[28:29], v55 offset:0\n"
    "v_mfma_f32_16x16x32_bf16 v[136:139], v[36:39], v[36:39], v[136:139]\n"
    "ds_read_b64_tr_b16 v[30:31], v55 offset:1024\n"
    "v_smfmac_f32_16x16x64_bf16 v[128:131], v[216:219], v[0:7], v63\n"
    "ds_read_b64_tr_b16 v[240:241], v55 offset:8192\n"
    "v_smfmac_f32_16x16x64_bf16 v[128:131], v[216:219], v[32:39], v63\n"
    "ds_read_b64_tr_b16 v[242:243], v55 offset:9216\n"
    "s_waitcnt lgkmcnt(8)\n"
    "v_smfmac_f32_16x16x64_bf16 v[140:143], v[216:219], v[16:23], v63\n"
    "ds_read_b128 v[8:11], v49 offset:0\n"
    "v_smfmac_f32_16x16x64_bf16 v[140:143], v[216:219], v[228:235], v63\n"
    "ds_read_b128 v[40:43], v51 offset:0\n"
    "v_mfma_f32_16x16x32_bf16 v[64:67], v[16:19], v[0:3], v[64:67]\n"
    "ds_read_b128 v[12:15], v49 offset:2048\n"
    "v_cndmask_b32 v144, v132, v133, s[72:73]\n"
    "v_cndmask_b32 v146, v134, v135, s[72:73]\n"
    "v_mfma_f32_16x16x32_bf16 v[64:67], v[228:231], v[32:35], v[64:67]\n"
    "ds_read_b128 v[44:47], v51 offset:2048\n"
    "v_cndmask_b32 v145, v136, v137, s[72:73]\n"
    "v_cndmask_b32 v147, v138, v139, s[72:73]\n"
    "v_mfma_f32_16x16x32_bf16 v[68:71], v[20:23], v[0:3], v[68:71]\n"
    "v_cndmask_b32 v144, v144, v146, s[74:75]\n"
    "v_cndmask_b32 v145, v145, v147, s[74:75]\n"
    "v_mfma_f32_16x16x32_bf16 v[68:71], v[232:235], v[32:35], v[68:71]\n"
    "v_cndmask_b32 v147, v128, v130, s[78:79]\n"
    "v_cndmask_b32 v146, v129, v131, s[78:79]\n"
    "s_waitcnt lgkmcnt(8)\n"
    "v_mfma_f32_16x16x32_bf16 v[72:75], v[24:27], v[0:3], v[72:75]\n"
    "v_permlane16_swap_b32 v144, v145\n"
    "v_add_f32 v147, v147, v146\n"
    "v_mfma_f32_16x16x32_bf16 v[72:75], v[236:239], v[32:35], v[72:75]\n"
    "v_cndmask_b32 v148, v140, v142, s[78:79]\n"
    "v_cndmask_b32 v144, v144, v145, s[76:77]\n"
    "s_waitcnt lgkmcnt(4)\n"
    "v_mfma_f32_16x16x32_bf16 v[76:79], v[28:31], v[0:3], v[76:79]\n"
    "v_mul_f32 v147, 0x3acccccd, v147\n"
    "v_mul_f32 v144, 0x3acccccd, v144\n"
    "v_mfma_f32_16x16x32_bf16 v[76:79], v[240:243], v[32:35], v[76:79]\n"
    "v_cndmask_b32 v146, v141, v143, s[78:79]\n"
    "v_fma_f32 v144, -v147, v147, v144\n"
    "v_mfma_f32_16x16x32_bf16 v[80:83], v[16:19], v[4:7], v[80:83]\n"
    "v_add_f32 v148, v148, v146\n"
    "v_max_f32 v144, 0, v144\n"
    "v_mfma_f32_16x16x32_bf16 v[80:83], v[228:231], v[36:39], v[80:83]\n"
    "ds_write_b32 v56, v148\n"
    "v_cvt_pk_bf16_f32 v144, v144, v144\n"
    "v_mfma_f32_16x16x32_bf16 v[84:87], v[20:23], v[4:7], v[84:87]\n"
    "v_lshlrev_b32 v144, 16, v144\n"
    "v_add_f32 v144, 0x3727c5ac, v144\n"
    "v_mfma_f32_16x16x32_bf16 v[84:87], v[232:235], v[36:39], v[84:87]\n"
    "v_cvt_pk_bf16_f32 v144, v144, v144\n"
    "v_lshlrev_b32 v144, 16, v144\n"
    "v_mfma_f32_16x16x32_bf16 v[88:91], v[24:27], v[4:7], v[88:91]\n"
    "v_sqrt_f32 v144, v144\n"
    "v_cvt_pk_bf16_f32 v147, -v147, -v147\n"
    "v_mfma_f32_16x16x32_bf16 v[88:91], v[236:239], v[36:39], v[88:91]\n"
    "v_lshlrev_b32 v147, 16, v147\n"
    "v_cvt_pk_bf16_f32 v144, v144, v144\n"
    "v_mfma_f32_16x16x32_bf16 v[92:95], v[28:31], v[4:7], v[92:95]\n"
    "v_lshlrev_b32 v144, 16, v144\n"
    "v_rcp_f32 v144, v144\n"
    "v_mfma_f32_16x16x32_bf16 v[92:95], v[240:243], v[36:39], v[92:95]\n"
    "v_mul_f32 v144, %[sc], v144\n"
    "s_mov_b64 exec, s[80:81]\n"
    "ds_write2_b32 v57, v147, v144 offset1:128\n"
    "s_mov_b64 exec, -1\n"
    "s_waitcnt lgkmcnt(4)\n"
    "v_mfma_f32_16x16x32_bf16 v[96:99], v[16:19], v[8:11], v[96:99]\n"
    "v_mfma_f32_16x16x32_bf16 v[96:99], v[228:231], v[40:43], v[96:99]\n"
    "s_waitcnt lgkmcnt(0)\n"
    "s_barrier\n"
    "v_mfma_f32_16x16x32_bf16 v[100:103], v[20:23], v[8:11], v[100:103]\n"
    "ds_read2_b32 v[152:153], v58 offset0:0 offset1:128\n"
    "ds_read_b128 v[168:171], v60 offset:0\n"
    "v_mfma_f32_16x16x32_bf16 v[100:103], v[232:235], v[40:43], v[100:103]\n"
    "ds_read_b128 v[172:175], v60 offset:64\n"
    "ds_read2_b32 v[154:155], v58 offset0:16 offset1:144\n"
    "v_mfma_f32_16x16x32_bf16 v[104:107], v[24:27], v[8:11], v[104:107]\n"
    "ds_read_b128 v[176:179], v61 offset:0\n"
    "ds_read_b128 v[180:183], v61 offset:64\n"
    "v_mfma_f32_16x16x32_bf16 v[104:107], v[236:239], v[40:43], v[104:107]\n"
    "ds_read2_b32 v[156:157], v59 offset0:0 offset1:128\n"
    "ds_read2_b32 v[158:159], v59 offset0:16 offset1:144\n"
    "v_mfma_f32_16x16x32_bf16 v[108:111], v[28:31], v[8:11], v[108:111]\n"
    "v_mfma_f32_16x16x32_bf16 v[108:111], v[240:243], v[40:43], v[108:111]\n"
    "v_mfma_f32_16x16x32_bf16 v[112:115], v[16:19], v[12:15], v[112:115]\n"
    "v_mfma_f32_16x16x32_bf16 v[112:115], v[228:231], v[44:47], v[112:115]\n"
    "v_mfma_f32_16x16x32_bf16 v[116:119], v[20:23], v[12:15], v[116:119]\n"
    "v_mfma_f32_16x16x32_bf16 v[116:119], v[232:235], v[44:47], v[116:119]\n"
    "v_mfma_f32_16x16x32_bf16 v[120:123], v[24:27], v[12:15], v[120:123]\n"
    "v_mfma_f32_16x16x32_bf16 v[120:123], v[236:239], v[44:47], v[120:123]\n"
    "v_mfma_f32_16x16x32_bf16 v[124:127], v[28:31], v[12:15], v[124:127]\n"
    "v_mfma_f32_16x16x32_bf16 v[124:127], v[240:243], v[44:47], v[124:127]\n"
    "s_nop 0\n"
    "s_waitcnt lgkmcnt(4)\n"
    "v_pk_fma_f32 v[64:65], v[168:169], v[152:153], v[64:65] op_sel_hi:[1,0,1]\n"
    "v_pk_fma_f32 v[66:67], v[170:171], v[152:153], v[66:67] op_sel_hi:[1,0,1]\n"
    "v_pk_fma_f32 v[68:69], v[172:173], v[152:153], v[68:69] op_sel_hi:[1,0,1]\n"
    "v_pk_fma_f32 v[70:71], v[174:175], v[152:153], v[70:71] op_sel_hi:[1,0,1]\n"
    "v_pk_fma_f32 v[80:81], v[168:169], v[154:155], v[80:81] op_sel_hi:[1,0,1]\n"
    "v_pk_fma_f32 v[82:83], v[170:171], v[154:155], v[82:83] op_sel_hi:[1,0,1]\n"
    "v_pk_fma_f32 v[84:85], v[172:173], v[154:155], v[84:85] op_sel_hi:[1,0,1]\n"
    "v_pk_fma_f32 v[86:87], v[174:175], v[154:155], v[86:87] op_sel_hi:[1,0,1]\n"
    "v_pk_mul_f32 v[64:65], v[64:65], v[152:153] op_sel:[0,1] op_sel_hi:[1,1]\n"
    "v_pk_mul_f32 v[66:67], v[66:67], v[152:153] op_sel:[0,1] op_sel_hi:[1,1]\n"
    "v_pk_mul_f32 v[68:69], v[68:69], v[152:153] op_sel:[0,1] op_sel_hi:[1,1]\n"
    "v_pk_mul_f32 v[70:71], v[70:71], v[152:153] op_sel:[0,1] op_sel_hi:[1,1]\n"
    "v_pk_mul_f32 v[80:81], v[80:81], v[154:155] op_sel:[0,1] op_sel_hi:[1,1]\n"
    "v_pk_mul_f32 v[82:83], v[82:83], v[154:155] op_sel:[0,1] op_sel_hi:[1,1]\n"
    "v_pk_mul_f32 v[84:85], v[84:85], v[154:155] op_sel:[0,1] op_sel_hi:[1,1]\n"
    "v_pk_mul_f32 v[86:87], v[86:87], v[154:155] op_sel:[0,1] op_sel_hi:[1,1]\n"
    "v_cvt_pk_bf16_f32 v184, v64, v65\n"
    "v_cvt_pk_bf16_f32 v185, v66, v67\n"
    "v_cvt_pk_bf16_f32 v186, v68, v69\n"
    "v_cvt_pk_bf16_f32 v187, v70, v71\n"
    "v_cvt_pk_bf16_f32 v188, v80, v81\n"
    "v_cvt_pk_bf16_f32 v189, v82, v83\n"
    "v_cvt_pk_bf16_f32 v190, v84, v85\n"
    "v_cvt_pk_bf16_f32 v191, v86, v87\n"
    "s_mov_b64 exec, %[sp0]\n"
    "v_swap_b32 v186, v188\n"
    "v_swap_b32 v187, v189\n"
    "s_mov_b64 exec, -1\n"
    "buffer_store_dwordx4 v[184:187], v224, %[rs], %[tb0] offen offset:0 sc1\n"
    "buffer_store_dwordx4 v[188:191], v225, %[rs], %[tb0] offen offset:0 sc1\n"
    "s_waitcnt lgkmcnt(2)\n"
    "v_pk_fma_f32 v[72:73], v[176:177], v[152:153], v[72:73] op_sel_hi:[1,0,1]\n"
    "v_pk_fma_f32 v[74:75], v[178:179], v[152:153], v[74:75] op_sel_hi:[1,0,1]\n"
    "v_pk_fma_f32 v[76:77], v[180:181], v[152:153], v[76:77] op_sel_hi:[1,0,1]\n"
    "v_pk_fma_f32 v[78:79], v[182:183], v[152:153], v[78:79] op_sel_hi:[1,0,1]\n"
    "v_pk_fma_f32 v[88:89], v[176:177], v[154:155], v[88:89] op_sel_hi:[1,0,1]\n"
    "v_pk_fma_f32 v[90:91], v[178:179], v[154:155], v[90:91] op_sel_hi:[1,0,1]\n"
    "v_pk_fma_f32 v[92:93], v[180:181], v[154:155], v[92:93] op_sel_hi:[1,0,1]\n"
    "v_pk_fma_f32 v[94:95], v[182:183], v[154:155], v[94:95] op_sel_hi:[1,0,1]\n"
    "v_pk_mul_f32 v[72:73], v[72:73], v[152:153] op_sel:[0,1] op_sel_hi:[1,1]\n"
    "v_pk_mul_f32 v[74:75], v[74:75], v[152:153] op_sel:[0,1] op_sel_hi:[1,1]\n"
    "v_pk_mul_f32 v[76:77], v[76:77], v[152:153] op_sel:[0,1] op_sel_hi:[1,1]\n"
    "v_pk_mul_f32 v[78:79], v[78:79], v[152:153] op_sel:[0,1] op_sel_hi:[1,1]\n"
    "v_pk_mul_f32 v[88:89], v[88:89], v[154:155] op_sel:[0,1] op_sel_hi:[1,1]\n"
    "v_pk_mul_f32 v[90:91], v[90:91], v[154:155] op_sel:[0,1] op_sel_hi:[1,1]\n"
    "v_pk_mul_f32 v[92:93], v[92:93], v[154:155] op_sel:[0,1] op_sel_hi:[1,1]\n"
    "v_pk_mul_f32 v[94:95], v[94:95], v[154:155] op_sel:[0,1] op_sel_hi:[1,1]\n"
    "v_cvt_pk_bf16_f32 v192, v72, v73\n"
    "v_cvt_pk_bf16_f32 v193, v74, v75\n"
    "v_cvt_pk_bf16_f32 v194, v76, v77\n"
    "v_cvt_pk_bf16_f32 v195, v78, v79\n"
    "v_cvt_pk_bf16_f32 v196, v88, v89\n"
    "v_cvt_pk_bf16_f32 v197, v90, v91\n"
    "v_cvt_pk_bf16_f32 v198, v92, v93\n"
    "v_cvt_pk_bf16_f32 v199, v94, v95\n"
    "s_mov_b64 exec, %[sp1]\n"
    "v_swap_b32 v194, v196\n"
    "v_swap_b32 v195, v197\n"
    "s_mov_b64 exec, -1\n"
    "buffer_store_dwordx4 v[192:195], v226, %[rs], %[tb0] offen offset:0 sc1\n"
    "buffer_store_dwordx4 v[196:199], v227, %[rs], %[tb0] offen offset:0 sc1\n"
    "s_waitcnt lgkmcnt(0)\n"
    "v_pk_fma_f32 v[96:97], v[168:169], v[156:157], v[96:97] op_sel_hi:[1,0,1]\n"
    "v_pk_fma_f32 v[98:99], v[170:171], v[156:157], v[98:99] op_sel_hi:[1,0,1]\n"
    "v_pk_fma_f32 v[100:101], v[172:173], v[156:157], v[100:101] op_sel_hi:[1,0,1]\n"
    "v_pk_fma_f32 v[102:103], v[174:175], v[156:157], v[102:103] op_sel_hi:[1,0,1]\n"
    "v_pk_fma_f32 v[112:113], v[168:169], v[158:159], v[112:113] op_sel_hi:[1,0,1]\n"
    "v_pk_fma_f32 v[114:115], v[170:171], v[158:159], v[114:115] op_sel_hi:[1,0,1]\n"
    "v_pk_fma_f32 v[116:117], v[172:173], v[158:159], v[116:117] op_sel_hi:[1,0,1]\n"
    "v_pk_fma_f32 v[118:119], v[174:175], v[158:159], v[118:119] op_sel_hi:[1,0,1]\n"
    "v_pk_mul_f32 v[96:97], v[96:97], v[156:157] op_sel:[0,1] op_sel_hi:[1,1]\n"
    "v_pk_mul_f32 v[98:99], v[98:99], v[156:157] op_sel:[0,1] op_sel_hi:[1,1]\n"
    "v_pk_mul_f32 v[100:101], v[100:101], v[156:157] op_sel:[0,1] op_sel_hi:[1,1]\n"
    "v_pk_mul_f32 v[102:103], v[102:103], v[156:157] op_sel:[0,1] op_sel_hi:[1,1]\n"
    "v_pk_mul_f32 v[112:113], v[112:113], v[158:159] op_sel:[0,1] op_sel_hi:[1,1]\n"
    "v_pk_mul_f32 v[114:115], v[114:115], v[158:159] op_sel:[0,1] op_sel_hi:[1,1]\n"
    "v_pk_mul_f32 v[116:117], v[116:117], v[158:159] op_sel:[0,1] op_sel_hi:[1,1]\n"
    "v_pk_mul_f32 v[118:119], v[118:119], v[158:159] op_sel:[0,1] op_sel_hi:[1,1]\n"
    "v_cvt_pk_bf16_f32 v200, v96, v97\n"
    "v_cvt_pk_bf16_f32 v201, v98, v99\n"
    "v_cvt_pk_bf16_f32 v202, v100, v101\n"
    "v_cvt_pk_bf16_f32 v203, v102, v103\n"
    "v_cvt_pk_bf16_f32 v204, v112, v113\n"
    "v_cvt_pk_bf16_f32 v205, v114, v115\n"
    "v_cvt_pk_bf16_f32 v206, v116, v117\n"
    "v_cvt_pk_bf16_f32 v207, v118, v119\n"
    "s_mov_b64 exec, %[sp0]\n"
    "v_swap_b32 v202, v204\n"
    "v_swap_b32 v203, v205\n"
    "s_mov_b64 exec, -1\n"
    "buffer_store_dwordx4 v[200:203], v224, %[rs], %[tb1] offen offset:0 sc1\n"
    "buffer_store_dwordx4 v[204:207], v225, %[rs], %[tb1] offen offset:0 sc1\n"
    "s_waitcnt lgkmcnt(0)\n"
    "v_pk_fma_f32 v[104:105], v[176:177], v[156:157], v[104:105] op_sel_hi:[1,0,1]\n"
    "v_pk_fma_f32 v[106:107], v[178:179], v[156:157], v[106:107] op_sel_hi:[1,0,1]\n"
    "v_pk_fma_f32 v[108:109], v[180:181], v[156:157], v[108:109] op_sel_hi:[1,0,1]\n"
    "v_pk_fma_f32 v[110:111], v[182:183], v[156:157], v[110:111] op_sel_hi:[1,0,1]\n"
    "v_pk_fma_f32 v[120:121], v[176:177], v[158:159], v[120:121] op_sel_hi:[1,0,1]\n"
    "v_pk_fma_f32 v[122:123], v[178:179], v[158:159], v[122:123] op_sel_hi:[1,0,1]\n"
    "v_pk_fma_f32 v[124:125], v[180:181], v[158:159], v[124:125] op_sel_hi:[1,0,1]\n"
    "v_pk_fma_f32 v[126:127], v[182:183], v[158:159], v[126:127] op_sel_hi:[1,0,1]\n"
    "v_pk_mul_f32 v[104:105], v[104:105], v[156:157] op_sel:[0,1] op_sel_hi:[1,1]\n"
    "v_pk_mul_f32 v[106:107], v[106:107], v[156:157] op_sel:[0,1] op_sel_hi:[1,1]\n"
    "v_pk_mul_f32 v[108:109], v[108:109], v[156:157] op_sel:[0,1] op_sel_hi:[1,1]\n"
    "v_pk_mul_f32 v[110:111], v[110:111], v[156:157] op_sel:[0,1] op_sel_hi:[1,1]\n"
    "v_pk_mul_f32 v[120:121], v[120:121], v[158:159] op_sel:[0,1] op_sel_hi:[1,1]\n"
    "v_pk_mul_f32 v[122:123], v[122:123], v[158:159] op_sel:[0,1] op_sel_hi:[1,1]\n"
    "v_pk_mul_f32 v[124:125], v[124:125], v[158:159] op_sel:[0,1] op_sel_hi:[1,1]\n"
    "v_pk_mul_f32 v[126:127], v[126:127], v[158:159] op_sel:[0,1] op_sel_hi:[1,1]\n"
    "v_cvt_pk_bf16_f32 v208, v104, v105\n"
    "v_cvt_pk_bf16_f32 v209, v106, v107\n"
    "v_cvt_pk_bf16_f32 v210, v108, v109\n"
    "v_cvt_pk_bf16_f32 v211, v110, v111\n"
    "v_cvt_pk_bf16_f32 v212, v120, v121\n"
    "v_cvt_pk_bf16_f32 v213, v122, v123\n"
    "v_cvt_pk_bf16_f32 v214, v124, v125\n"
    "v_cvt_pk_bf16_f32 v215, v126, v127\n"
    "s_mov_b64 exec, %[sp1]\n"
    "v_swap_b32 v210, v212\n"
    "v_swap_b32 v211, v213\n"
    "s_mov_b64 exec, -1\n"
    "buffer_store_dwordx4 v[208:211], v226, %[rs], %[tb1] offen offset:0 sc1\n"
    "buffer_store_dwordx4 v[212:215], v227, %[rs], %[tb1] offen offset:0 sc1\n"
    P1_OPS);
  else asm volatile(
    "v_mov_b32 v64, 0\n"
    "v_mov_b32 v65, 0\n"
    "v_mov_b32 v66, 0\n"
    "v_mov_b32 v67, 0\n"
    "v_mov_b32 v68, 0\n"
    "v_mov_b32 v69, 0\n"
    "v_mov_b32 v70, 0\n"
    "v_mov_b32 v71, 0\n"
    "v_mov_b32 v72, 0\n"
    "v_mov_b32 v73, 0\n"
    "v_mov_b32 v74, 0\n"
    "v_mov_b32 v75, 0\n"
    "v_mov_b32 v76, 0\n"
    "v_mov_b32 v77, 0\n"
    "v_mov_b32 v78, 0\n"
    "v_mov_b32 v79, 0\n"
    "v_mov_b32 v80, 0\n"
    "v_mov_b32 v81, 0\n"
    "v_mov_b32 v82, 0\n"
    "v_mov_b32 v83, 0\n"
    "v_mov_b32 v84, 0\n"
    "v_mov_b32 v85, 0\n"
    "v_mov_b32 v86, 0\n"
    "v_mov_b32 v87, 0\n"
    "v_mov_b32 v88, 0\n"
    "v_mov_b32 v89, 0\n"
    "v_mov_b32 v90, 0\n"
    "v_mov_b32 v91, 0\n"
    "v_mov_b32 v92, 0\n"
    "v_mov_b32 v93, 0\n"
    "v_mov_b32 v94, 0\n"
    "v_mov_b32 v95, 0\n"
    "v_mov_b32 v96, 0\n"
    "v_mov_b32 v97, 0\n"
    "v_mov_b32 v98, 0\n"
    "v_mov_b32 v99, 0\n"
    "v_mov_b32 v100, 0\n"
    "v_mov_b32 v101, 0\n"
    "v_mov_b32 v102, 0\n"
    "v_mov_b32 v103, 0\n"
    "v_mov_b32 v104, 0\n"
    "v_mov_b32 v105, 0\n"
    "v_mov_b32 v106, 0\n"
    "v_mov_b32 v107, 0\n"
    "v_mov_b32 v108, 0\n"
    "v_mov_b32 v109, 0\n"
    "v_mov_b32 v110, 0\n"
    "v_mov_b32 v111, 0\n"
    "v_mov_b32 v112, 0\n"
    "v_mov_b32 v113, 0\n"
    "v_mov_b32 v114, 0\n"
    "v_mov_b32 v115, 0\n"
    "v_mov_b32 v116, 0\n"
    "v_mov_b32 v117, 0\n"
    "v_mov_b32 v118, 0\n"
    "v_mov_b32 v119, 0\n"
    "v_mov_b32 v120, 0\n"
    "v_mov_b32 v121, 0\n"
    "v_mov_b32 v122, 0\n"
    "v_mov_b32 v123, 0\n"
    "v_mov_b32 v124, 0\n"
    "v_mov_b32 v125, 0\n"
    "v_mov_b32 v126, 0\n"
    "v_mov_b32 v127, 0\n"
    "v_mov_b32 v128, 0\n"
    "v_mov_b32 v129, 0\n"
    "v_mov_b32 v130, 0\n"
    "v_mov_b32 v131, 0\n"
    "v_mov_b32 v132, 0\n"
    "v_mov_b32 v133, 0\n"
    "v_mov_b32 v134, 0\n"
    "v_mov_b32 v135, 0\n"
    "v_mov_b32 v136, 0\n"
    "v_mov_b32 v137, 0\n"
    "v_mov_b32 v138, 0\n"
    "v_mov_b32 v139, 0\n"
    "v_mov_b32 v140, 0\n"
    "v_mov_b32 v141, 0\n"
    "v_mov_b32 v142, 0\n"
    "v_mov_b32 v143, 0\n"
    "v_mov_b32 v216, v62\n"
    "v_mov_b32 v217, v62\n"
    "v_mov_b32 v218, v62\n"
    "v_mov_b32 v219, v62\n"
    "s_mov_b32 s72, 0xaaaaaaaa\n"
    "s_mov_b32 s73, 0xaaaaaaaa\n"
    "s_mov_b32 s74, 0xcccccccc\n"
    "s_mov_b32 s75, 0xcccccccc\n"
    "s_mov_b32 s76, 0xf0f0f0f0\n"
    "s_mov_b32 s77, 0xf0f0f0f0\n"
    "s_mov_b32 s78, 0xffff0000\n"
    "s_mov_b32 s79, 0xffff0000\n"
    "s_mov_b32 s80, 0xff00ff\n"
    "s_mov_b32 s81, 0xff00ff00\n"
    "s_mov_b32 %[cnt], 0\n"
    "s_barrier\n"
    "ds_read_b128 v[152:155], v48 offset:0\n"
    "ds_read_b128 v[184:187], v50 offset:0\n"
    "ds_read_b128 v[156:159], v48 offset:2048\n"
    "ds_read_b128 v[188:191], v50 offset:2048\n"
    "ds_read_b64_tr_b16 v[168:169], v52 offset:0\n"
    "ds_read_b64_tr_b16 v[170:171], v52 offset:1024\n"
    "ds_read_b64_tr_b16 v[200:201], v52 offset:8192\n"
    "ds_read_b64_tr_b16 v[202:203], v52 offset:9216\n"
    "ds_read_b64_tr_b16 v[172:173], v53 offset:0\n"
    "ds_read_b64_tr_b16 v[174:175], v53 offset:1024\n"
    "ds_read_b64_tr_b16 v[204:205], v53 offset:8192\n"
    "ds_read_b64_tr_b16 v[206:207], v53 offset:9216\n"
    "ds_read_b64_tr_b16 v[176:177], v54 offset:0\n"
    "ds_read_b64_tr_b16 v[178:179], v54 offset:1024\n"
    "s_branch 2f\n"
    ".p2align 5\n"
    "1:\n"
    "s_waitcnt lgkmcnt(12)\n"
    "v_mfma_f32_16x16x32_bf16 v[132:135], v[0:3], v[0:3], v[132:135]\n"
    "ds_read_b64_tr_b16 v[236:237], v54 offset:8192\n"
    "s_add_u32 %[cnt], %[cnt], 1\n"
    "v_mfma_f32_16x16x32_bf16 v[132:135], v[32:35], v[32:35], v[132:135]\n"
    "ds_read_b64_tr_b16 v[238:239], v54 offset:9216\n"
    "s_mov_b32 %[dl], 32768\n"
    "s_waitcnt lgkmcnt(12)\n"
    "v_mfma_f32_16x16x32_bf16 v[136:139], v[4:7], v[4:7], v[136:139]\n"
    "ds_read_b64_tr_b16 v[28:29], v55 offset:0\n"
    "s_bitcmp1_b32 0x111, %[cnt]\n"
    "v_mfma_f32_16x16x32_bf16 v[136:139], v[36:39], v[36:39], v[136:139]\n"
    "ds_read_b64_tr_b16 v[30:31], v55 offset:1024\n"
    "s_cmov_b32 %[dl], -98304\n"
    "v_smfmac_f32_16x16x64_bf16 v[128:131], v[216:219], v[0:7], v63\n"
    "ds_read_b64_tr_b16 v[240:241], v55 offset:8192\n"
    "v_smfmac_f32_16x16x64_bf16 v[128:131], v[216:219], v[32:39], v63\n"
    "ds_read_b64_tr_b16 v[242:243], v55 offset:9216\n"
    "s_waitcnt lgkmcnt(8)\n"
    "v_smfmac_f32_16x16x64_bf16 v[140:143], v[216:219], v[16:23], v63\n"
    "ds_read_b128 v[8:11], v49 offset:0\n"
    "v_smfmac_f32_16x16x64_bf16 v[140:143], v[216:219], v[228:235], v63\n"
    "ds_read_b128 v[40:43], v51 offset:0\n"
    "v_mfma_f32_16x16x32_bf16 v[64:67], v[0:3], v[16:19], v[64:67]\n"
    "ds_read_b128 v[12:15], v49 offset:2048\n"
    "v_mfma_f32_16x16x32_bf16 v[64:67], v[32:35], v[228:231], v[64:67]\n"
    "ds_read_b128 v[44:47], v51 offset:2048\n"
    "v_mfma_f32_16x16x32_bf16 v[68:71], v[0:3], v[20:23], v[68:71]\n"
    "v_mfma_f32_16x16x32_bf16 v[68:71], v[32:35], v[232:235], v[68:71]\n"
    "s_waitcnt lgkmcnt(8)\n"
    "v_mfma_f32_16x16x32_bf16 v[72:75], v[0:3], v[24:27], v[72:75]\n"
    "v_mfma_f32_16x16x32_bf16 v[72:75], v[32:35], v[236:239], v[72:75]\n"
    "s_waitcnt lgkmcnt(4)\n"
    "v_mfma_f32_16x16x32_bf16 v[76:79], v[0:3], v[28:31], v[76:79]\n"
    "v_mfma_f32_16x16x32_bf16 v[76:79], v[32:35], v[240:243], v[76:79]\n"
    "v_mfma_f32_16x16x32_bf16 v[80:83], v[4:7], v[16:19], v[80:83]\n"
    "v_add_u32 v52, %[dl], v52\n"
    "v_mfma_f32_16x16x32_bf16 v[80:83], v[36:39], v[228:231], v[80:83]\n"
    "v_add_u32 v53, %[dl], v53\n"
    "v_mfma_f32_16x16x32_bf16 v[84:87], v[4:7], v[20:23], v[84:87]\n"
    "v_add_u32 v54, %[dl], v54\n"
    "v_mfma_f32_16x16x32_bf16 v[84:87], v[36:39], v[232:235], v[84:87]\n"
    "v_add_u32 v55, %[dl], v55\n"
    "v_mfma_f32_16x16x32_bf16 v[88:91], v[4:7], v[24:27], v[88:91]\n"
    "v_add_u32 v48, %[dl], v48\n"
    "v_mfma_f32_16x16x32_bf16 v[88:91], v[36:39], v[236:239], v[88:91]\n"
    "v_add_u32 v50, %[dl], v50\n"
    "v_mfma_f32_16x16x32_bf16 v[92:95], v[4:7], v[28:31], v[92:95]\n"
    "v_add_u32 v49, %[dl], v49\n"
    "v_mfma_f32_16x16x32_bf16 v[92:95], v[36:39], v[240:243], v[92:95]\n"
    "v_add_u32 v51, %[dl], v51\n"
    "s_waitcnt lgkmcnt(2)\n"
    "v_mfma_f32_16x16x32_bf16 v[96:99], v[8:11], v[16:19], v[96:99]\n"
    "s_cmp_lt_u32 %[cnt], 9\n"
    "v_mfma_f32_16x16x32_bf16 v[96:99], v[40:43], v[228:231], v[96:99]\n"
    "s_waitcnt lgkmcnt(0)\n"
    "s_barrier\n"
    "v_mfma_f32_16x16x32_bf16 v[100:103], v[8:11], v[20:23], v[100:103]\n"
    "ds_read_b128 v[152:155], v48 offset:0\n"
    "v_mfma_f32_16x16x32_bf16 v[100:103], v[40:43], v[232:235], v[100:103]\n"
    "ds_read_b128 v[184:187], v50 offset:0\n"
    "v_mfma_f32_16x16x32_bf16 v[104:107], v[8:11], v[24:27], v[104:107]\n"
    "ds_read_b128 v[156:159], v48 offset:2048\n"
    "v_mfma_f32_16x16x32_bf16 v[104:107], v[40:43], v[236:239], v[104:107]\n"
    "ds_read_b128 v[188:191], v50 offset:2048\n"
    "v_mfma_f32_16x16x32_bf16 v[108:111], v[8:11], v[28:31], v[108:111]\n"
    "ds_read_b64_tr_b16 v[168:169], v52 offset:0\n"
    "v_mfma_f32_16x16x32_bf16 v[108:111], v[40:43], v[240:243], v[108:111]\n"
    "ds_read_b64_tr_b16 v[170:171], v52 offset:1024\n"
    "v_mfma_f32_16x16x32_bf16 v[112:115], v[12:15], v[16:19], v[112:115]\n"
    "ds_read_b64_tr_b16 v[200:201], v52 offset:8192\n"
    "v_mfma_f32_16x16x32_bf16 v[112:115], v[44:47], v[228:231], v[112:115]\n"
    "ds_read_b64_tr_b16 v[202:203], v52 offset:9216\n"
    "v_mfma_f32_16x16x32_bf16 v[116:119], v[12:15], v[20:23], v[116:119]\n"
    "ds_read_b64_tr_b16 v[172:173], v53 offset:0\n"
    "v_mfma_f32_16x16x32_bf16 v[116:119], v[44:47], v[232:235], v[116:119]\n"
    "ds_read_b64_tr_b16 v[174:175], v53 offset:1024\n"
    "v_mfma_f32_16x16x32_bf16 v[120:123], v[12:15], v[24:27], v[120:123]\n"
    "ds_read_b64_tr_b16 v[204:205], v53 offset:8192\n"
    "v_mfma_f32_16x16x32_bf16 v[120:123], v[44:47], v[236:239], v[120:123]\n"
    "ds_read_b64_tr_b16 v[206:207], v53 offset:9216\n"
    "v_mfma_f32_16x16x32_bf16 v[124:127], v[12:15], v[28:31], v[124:127]\n"
    "ds_read_b64_tr_b16 v[176:177], v54 offset:0\n"
    "v_mfma_f32_16x16x32_bf16 v[124:127], v[44:47], v[240:243], v[124:127]\n"
    "ds_read_b64_tr_b16 v[178:179], v54 offset:1024\n"
    "2:\n"
    "s_waitcnt lgkmcnt(12)\n"
    "v_mfma_f32_16x16x32_bf16 v[132:135], v[152:155], v[152:155], v[132:135]\n"
    "ds_read_b64_tr_b16 v[208:209], v54 offset:8192\n"
    "s_add_u32 %[cnt], %[cnt], 1\n"
    "v_mfma_f32_16x16x32_bf16 v[132:135], v[184:187], v[184:187], v[132:135]\n"
    "ds_read_b64_tr_b16 v[210:211], v54 offset:9216\n"
    "s_mov_b32 %[dl], 32768\n"
    "s_waitcnt lgkmcnt(12)\n"
    "v_mfma_f32_16x16x32_bf16 v[136:139], v[156:159], v[156:159], v[136:139]\n"
    "ds_read_b64_tr_b16 v[180:181], v55 offset:0\n"
    "s_bitcmp1_b32 0x111, %[cnt]\n"
    "v_mfma_f32_16x16x32_bf16 v[136:139], v[188:191], v[188:191], v[136:139]\n"
    "ds_read_b64_tr_b16 v[182:183], v55 offset:1024\n"
    "s_cmov_b32 %[dl], -98304\n"
    "v_smfmac_f32_16x16x64_bf16 v[128:131], v[216:219], v[152:159], v63\n"
    "ds_read_b64_tr_b16 v[212:213], v55 offset:8192\n"
    "v_smfmac_f32_16x16x64_bf16 v[128:131], v[216:219], v[184:191], v63\n"
    "ds_read_b64_tr_b16 v[214:215], v55 offset:9216\n"
    "s_waitcnt lgkmcnt(8)\n"
    "v_smfmac_f32_16x16x64_bf16 v[140:143], v[216:219], v[168:175], v63\n"
    "ds_read_b128 v[160:163], v49 offset:0\n"
    "v_smfmac_f32_16x16x64_bf16 v[140:143], v[216:219], v[200:207], v63\n"
    "ds_read_b128 v[192:195], v51 offset:0\n"
    "v_mfma_f32_16x16x32_bf16 v[64:67], v[152:155], v[168:171], v[64:67]\n"
    "ds_read_b128 v[164:167], v49 offset:2048\n"
    "v_mfma_f32_16x16x32_bf16 v[64:67], v[184:187], v[200:203], v[64:67]\n"
    "ds_read_b128 v[196:199], v51 offset:2048\n"
    "v_mfma_f32_16x16x32_bf16 v[68:71], v[152:155], v[172:175], v[68:71]\n"
    "v_mfma_f32_16x16x32_bf16 v[68:71], v[184:187], v[204:207], v[68:71]\n"
    "s_waitcnt lgkmcnt(8)\n"
    "v_mfma_f32_16x16x32_bf16 v[72:75], v[152:155], v[176:179], v[72:75]\n"
    "v_mfma_f32_16x16x32_bf16 v[72:75], v[184:187], v[208:211], v[72:75]\n"
    "s_waitcnt lgkmcnt(4)\n"
    "v_mfma_f32_16x16x32_bf16 v[76:79], v[152:155], v[180:183], v[76:79]\n"
    "v_mfma_f32_16x16x32_bf16 v[76:79], v[184:187], v[212:215], v[76:79]\n"
    "v_mfma_f32_16x16x32_bf16 v[80:83], v[156:159], v[168:171], v[80:83]\n"
    "v_add_u32 v52, %[dl], v52\n"
    "v_mfma_f32_16x16x32_bf16 v[80:83], v[188:191], v[200:203], v[80:83]\n"
    "v_add_u32 v53, %[dl], v53\n"
    "v_mfma_f32_16x16x32_bf16 v[84:87], v[156:159], v[172:175], v[84:87]\n"
    "v_add_u32 v54, %[dl], v54\n"
    "v_mfma_f32_16x16x32_bf16 v[84:87], v[188:191], v[204:207], v[84:87]\n"
    "v_add_u32 v55, %[dl], v55\n"
    "v_mfma_f32_16x16x32_bf16 v[88:91], v[156:159], v[176:179], v[88:91]\n"
    "v_add_u32 v48, %[dl], v48\n"
    "v_mfma_f32_16x16x32_bf16 v[88:91], v[188:191], v[208:211], v[88:91]\n"
    "v_add_u32 v50, %[dl], v50\n"
    "v_mfma_f32_16x16x32_bf16 v[92:95], v[156:159], v[180:183], v[92:95]\n"
    "v_add_u32 v49, %[dl], v49\n"
    "v_mfma_f32_16x16x32_bf16 v[92:95], v[188:191], v[212:215], v[92:95]\n"
    "v_add_u32 v51, %[dl], v51\n"
    "s_waitcnt lgkmcnt(2)\n"
    "v_mfma_f32_16x16x32_bf16 v[96:99], v[160:163], v[168:171], v[96:99]\n"
    "s_cmp_lt_u32 %[cnt], 9\n"
    "v_mfma_f32_16x16x32_bf16 v[96:99], v[192:195], v[200:203], v[96:99]\n"
    "s_waitcnt lgkmcnt(0)\n"
    "s_barrier\n"
    "v_mfma_f32_16x16x32_bf16 v[100:103], v[160:163], v[172:175], v[100:103]\n"
    "ds_read_b128 v[0:3], v48 offset:0\n"
    "v_mfma_f32_16x16x32_bf16 v[100:103], v[192:195], v[204:207], v[100:103]\n"
    "ds_read_b128 v[32:35], v50 offset:0\n"
    "v_mfma_f32_16x16x32_bf16 v[104:107], v[160:163], v[176:179], v[104:107]\n"
    "ds_read_b128 v[4:7], v48 offset:2048\n"
    "v_mfma_f32_16x16x32_bf16 v[104:107], v[192:195], v[208:211], v[104:107]\n"
    "ds_read_b128 v[36:39], v50 offset:2048\n"
    "v_mfma_f32_16x16x32_bf16 v[108:111], v[160:163], v[180:183], v[108:111]\n"
    "ds_read_b64_tr_b16 v[16:17], v52 offset:0\n"
    "v_mfma_f32_16x16x32_bf16 v[108:111], v[192:195], v[212:215], v[108:111]\n"
    "ds_read_b64_tr_b16 v[18:19], v52 offset:1024\n"
    "v_mfma_f32_16x16x32_bf16 v[112:115], v[164:167], v[168:171], v[112:115]\n"
    "ds_read_b64_tr_b16 v[228:229], v52 offset:8192\n"
    "v_mfma_f32_16x16x32_bf16 v[112:115], v[196:199], v[200:203], v[112:115]\n"
    "ds_read_b64_tr_b16 v[230:231], v52 offset:9216\n"
    "v_mfma_f32_16x16x32_bf16 v[116:119], v[164:167], v[172:175], v[116:119]\n"
    "ds_read_b64_tr_b16 v[20:21], v53 offset:0\n"
    "v_mfma_f32_16x16x32_bf16 v[116:119], v[196:199], v[204:207], v[116:119]\n"
    "ds_read_b64_tr_b16 v[22:23], v53 offset:1024\n"
    "v_mfma_f32_16x16x32_bf16 v[120:123], v[164:167], v[176:179], v[120:123]\n"
    "ds_read_b64_tr_b16 v[232:233], v53 offset:8192\n"
    "v_mfma_f32_16x16x32_bf16 v[120:123], v[196:199], v[208:211], v[120:123]\n"
    "ds_read_b64_tr_b16 v[234:235], v53 offset:9216\n"
    "v_mfma_f32_16x16x32_bf16 v[124:127], v[164:167], v[180:183], v[124:127]\n"
    "ds_read_b64_tr_b16 v[24:25], v54 offset:0\n"
    "v_mfma_f32_16x16x32_bf16 v[124:127], v[196:199], v[212:215], v[124:127]\n"
    "ds_read_b64_tr_b16 v[26:27], v54 offset:1024\n"
    "s_cbranch_scc1 1b\n"
    "s_waitcnt lgkmcnt(12)\n"
    "v_mfma_f32_16x16x32_bf16 v[132:135], v[0:3], v[0:3], v[132:135]\n"
    "ds_read_b64_tr_b16 v[236:237], v54 offset:8192\n"
    "v_mfma_f32_16x16x32_bf16 v[132:135], v[32:35], v[32:35], v[132:135]\n"
    "ds_read_b64_tr_b16 v[238:239], v54 offset:9216\n"
    "s_waitcnt lgkmcnt(12)\n"
    "v_mfma_f32_16x16x32_bf16 v[136:139], v[4:7], v[4:7], v[136:139]\n"
    "ds_read_b64_tr_b16 v[28:29], v55 offset:0\n"
    "v_mfma_f32_16x16x32_bf16 v[136:139], v[36:39], v[36:39], v[136:139]\n"
    "ds_read_b64_tr_b16 v[30:31], v55 offset:1024\n"
    "v_smfmac_f32_16x16x64_bf16 v[128:131], v[216:219], v[0:7], v63\n"
    "ds_read_b64_tr_b16 v[240:241], v55 offset:8192\n"
    "v_smfmac_f32_16x16x64_bf16 v[128:131], v[216:219], v[32:39], v63\n"
    "ds_read_b64_tr_b16 v[242:243], v55 offset:9216\n"
    "s_waitcnt lgkmcnt(8)\n"
    "v_smfmac_f32_16x16x64_bf16 v[140:143], v[216:219], v[16:23], v63\n"
    "ds_read_b128 v[8:11], v49 offset:0\n"
    "v_smfmac_f32_16x16x64_bf16 v[140:143], v[216:219], v[228:235], v63\n"
    "ds_read_b128 v[40:43], v51 offset:0\n"
    "v_mfma_f32_16x16x32_bf16 v[64:67], v[0:3], v[16:19], v[64:67]\n"
    "ds_read_b128 v[12:15], v49 offset:2048\n"
    "v_cndmask_b32 v144, v132, v133, s[72:73]\n"
    "v_cndmask_b32 v146, v134, v135, s[72:73]\n"
    "v_mfma_f32_16x16x32_bf16 v[64:67], v[32:35], v[228:231], v[64:67]\n"
    "ds_read_b128 v[44:47], v51 offset:2048\n"
    "v_cndmask_b32 v145, v136, v137, s[72:73]\n"
    "v_cndmask_b32 v147, v138, v139, s[72:73]\n"
    "v_mfma_f32_16x16x32_bf16 v[68:71], v[0:3], v[20:23], v[68:71]\n"
    "v_cndmask_b32 v144, v144, v146, s[74:75]\n"
    "v_cndmask_b32 v145, v145, v147, s[74:75]\n"
    "v_mfma_f32_16x16x32_bf16 v[68:71], v[32:35], v[232:235], v[68:71]\n"
    "v_cndmask_b32 v147, v128, v130, s[78:79]\n"
    "v_cndmask_b32 v146, v129, v131, s[78:79]\n"
    "s_waitcnt lgkmcnt(8)\n"
    "v_mfma_f32_16x16x32_bf16 v[72:75], v[0:3], v[24:27], v[72:75]\n"
    "v_permlane16_swap_b32 v144, v145\n"
    "v_add_f32 v147, v147, v146\n"
    "v_mfma_f32_16x16x32_bf16 v[72:75], v[32:35], v[236:239], v[72:75]\n"
    "v_cndmask_b32 v148, v140, v142, s[78:79]\n"
    "v_cndmask_b32 v144, v144, v145, s[76:77]\n"
    "s_waitcnt lgkmcnt(4)\n"
    "v_mfma_f32_16x16x32_bf16 v[76:79], v[0:3], v[28:31], v[76:79]\n"
    "v_mul_f32 v147, 0x3acccccd, v147\n"
    "v_mul_f32 v144, 0x3acccccd, v144\n"
    "v_mfma_f32_16x16x32_bf16 v[76:79], v[32:35], v[240:243], v[76:79]\n"
    "v_cndmask_b32 v146, v141, v143, s[78:79]\n"
    "v_fma_f32 v144, -v147, v147, v144\n"
    "v_mfma_f32_16x16x32_bf16 v[80:83], v[4:7], v[16:19], v[80:83]\n"
    "v_add_f32 v148, v148, v146\n"
    "v_max_f32 v144, 0, v144\n"
    "v_mfma_f32_16x16x32_bf16 v[80:83], v[36:39], v[228:231], v[80:83]\n"
    "ds_write_b32 v56, v148\n"
    "v_cvt_pk_bf16_f32 v144, v144, v144\n"
    "v_mfma_f32_16x16x32_bf16 v[84:87], v[4:7], v[20:23], v[84:87]\n"
    "v_lshlrev_b32 v144, 16, v144\n"
    "v_add_f32 v144, 0x3727c5ac, v144\n"
    "v_mfma_f32_16x16x32_bf16 v[84:87], v[36:39], v[232:235], v[84:87]\n"
    "v_cvt_pk_bf16_f32 v144, v144, v144\n"
    "v_lshlrev_b32 v144, 16, v144\n"
    "v_mfma_f32_16x16x32_bf16 v[88:91], v[4:7], v[24:27], v[88:91]\n"
    "v_sqrt_f32 v144, v144\n"
    "v_cvt_pk_bf16_f32 v147, -v147, -v147\n"
    "v_mfma_f32_16x16x32_bf16 v[88:91], v[36:39], v[236:239], v[88:91]\n"
    "v_lshlrev_b32 v147, 16, v147\n"
    "v_cvt_pk_bf16_f32 v144, v144, v144\n"
    "v_mfma_f32_16x16x32_bf16 v[92:95], v[4:7], v[28:31], v[92:95]\n"
    "v_lshlrev_b32 v144, 16, v144\n"
    "v_rcp_f32 v144, v144\n"
    "v_mfma_f32_16x16x32_bf16 v[92:95], v[36:39], v[240:243], v[92:95]\n"
    "v_mul_f32 v144, %[sc], v144\n"
    "s_mov_b64 exec, s[80:81]\n"
    "ds_write2_b32 v57, v147, v144 offset1:128\n"
    "s_mov_b64 exec, -1\n"
    "s_waitcnt lgkmcnt(4)\n"
    "v_mfma_f32_16x16x32_bf16 v[96:99], v[8:11], v[16:19], v[96:99]\n"
    "v_mfma_f32_16x16x32_bf16 v[96:99], v[40:43], v[228:231], v[96:99]\n"
    "s_waitcnt lgkmcnt(0)\n"
    "s_barrier\n"
    "v_mfma_f32_16x16x32_bf16 v[100:103], v[8:11], v[20:23], v[100:103]\n"
    "ds_read2_b32 v[144:145], v60 offset1:16\n"
    "ds_read2_b32 v[146:147], v61 offset1:16\n"
    "v_mfma_f32_16x16x32_bf16 v[100:103], v[40:43], v[232:235], v[100:103]\n"
    "ds_read_b128 v[152:155], v58 offset:0\n"
    "ds_read_b128 v[156:159], v58 offset:512\n"
    "v_mfma_f32_16x16x32_bf16 v[104:107], v[8:11], v[24:27], v[104:107]\n"
    "ds_read_b128 v[160:163], v58 offset:64\n"
    "ds_read_b128 v[164:167], v58 offset:576\n"
    "v_mfma_f32_16x16x32_bf16 v[104:107], v[40:43], v[236:239], v[104:107]\n"
    "ds_read_b128 v[168:171], v59 offset:0\n"
    "ds_read_b128 v[172:175], v59 offset:512\n"
    "v_mfma_f32_16x16x32_bf16 v[108:111], v[8:11], v[28:31], v[108:111]\n"
    "ds_read_b128 v[176:179], v59 offset:64\n"
    "ds_read_b128 v[180:183], v59 offset:576\n"
    "v_mfma_f32_16x16x32_bf16 v[108:111], v[40:43], v[240:243], v[108:111]\n"
    "v_mfma_f32_16x16x32_bf16 v[112:115], v[12:15], v[16:19], v[112:115]\n"
    "v_mfma_f32_16x16x32_bf16 v[112:115], v[44:47], v[228:231], v[112:115]\n"
    "v_mfma_f32_16x16x32_bf16 v[116:119], v[12:15], v[20:23], v[116:119]\n"
    "v_mfma_f32_16x16x32_bf16 v[116:119], v[44:47], v[232:235], v[116:119]\n"
    "v_mfma_f32_16x16x32_bf16 v[120:123], v[12:15], v[24:27], v[120:123]\n"
    "v_mfma_f32_16x16x32_bf16 v[120:123], v[44:47], v[236:239], v[120:123]\n"
    "v_mfma_f32_16x16x32_bf16 v[124:127], v[12:15], v[28:31], v[124:127]\n"
    "v_mfma_f32_16x16x32_bf16 v[124:127], v[44:47], v[240:243], v[124:127]\n"
    "s_nop 0\n"
    "s_waitcnt lgkmcnt(4)\n"
    "v_pk_fma_f32 v[64:65], v[152:153], v[144:145], v[64:65] op_sel_hi:[1,0,1]\n"
    "v_pk_fma_f32 v[66:67], v[154:155], v[144:145], v[66:67] op_sel_hi:[1,0,1]\n"
    "v_pk_fma_f32 v[80:81], v[160:161], v[144:145], v[80:81] op_sel_hi:[1,0,1]\n"
    "v_pk_fma_f32 v[82:83], v[162:163], v[144:145], v[82:83] op_sel_hi:[1,0,1]\n"
    "v_pk_fma_f32 v[68:69], v[152:153], v[144:145], v[68:69] op_sel:[0,1,0] op_sel_hi:[1,1,1]\n"
    "v_pk_fma_f32 v[70:71], v[154:155], v[144:145], v[70:71] op_sel:[0,1,0] op_sel_hi:[1,1,1]\n"
    "v_pk_fma_f32 v[84:85], v[160:161], v[144:145], v[84:85] op_sel:[0,1,0] op_sel_hi:[1,1,1]\n"
    "v_pk_fma_f32 v[86:87], v[162:163], v[144:145], v[86:87] op_sel:[0,1,0] op_sel_hi:[1,1,1]\n"
    "v_pk_mul_f32 v[64:65], v[64:65], v[156:157] \n"
    "v_pk_mul_f32 v[66:67], v[66:67], v[158:159] \n"
    "v_pk_mul_f32 v[80:81], v[80:81], v[164:165] \n"
    "v_pk_mul_f32 v[82:83], v[82:83], v[166:167] \n"
    "v_pk_mul_f32 v[68:69], v[68:69], v[156:157] \n"
    "v_pk_mul_f32 v[70:71], v[70:71], v[158:159] \n"
    "v_pk_mul_f32 v[84:85], v[84:85], v[164:165] \n"
    "v_pk_mul_f32 v[86:87], v[86:87], v[166:167] \n"
    "v_cvt_pk_bf16_f32 v184, v64, v65\n"
    "v_cvt_pk_bf16_f32 v185, v66, v67\n"
    "v_cvt_pk_bf16_f32 v186, v80, v81\n"
    "v_cvt_pk_bf16_f32 v187, v82, v83\n"
    "v_cvt_pk_bf16_f32 v188, v68, v69\n"
    "v_cvt_pk_bf16_f32 v189, v70, v71\n"
    "v_cvt_pk_bf16_f32 v190, v84, v85\n"
    "v_cvt_pk_bf16_f32 v191, v86, v87\n"
    "buffer_store_dwordx4 v[184:187], v224, %[rs], %[tb0] offen offset:0 sc1\n"
    "buffer_store_dwordx4 v[188:191], v225, %[rs], %[tb0] offen offset:0 sc1\n"
    "v_pk_fma_f32 v[72:73], v[152:153], v[146:147], v[72:73] op_sel_hi:[1,0,1]\n"
    "v_pk_fma_f32 v[74:75], v[154:155], v[146:147], v[74:75] op_sel_hi:[1,0,1]\n"
    "v_pk_fma_f32 v[88:89], v[160:161], v[146:147], v[88:89] op_sel_hi:[1,0,1]\n"
    "v_pk_fma_f32 v[90:91], v[162:163], v[146:147], v[90:91] op_sel_hi:[1,0,1]\n"
    "v_pk_fma_f32 v[76:77], v[152:153], v[146:147], v[76:77] op_sel:[0,1,0] op_sel_hi:[1,1,1]\n"
    "v_pk_fma_f32 v[78:79], v[154:155], v[146:147], v[78:79] op_sel:[0,1,0] op_sel_hi:[1,1,1]\n"
    "v_pk_fma_f32 v[92:93], v[160:161], v[146:147], v[92:93] op_sel:[0,1,0] op_sel_hi:[1,1,1]\n"
    "v_pk_fma_f32 v[94:95], v[162:163], v[146:147], v[94:95] op_sel:[0,1,0] op_sel_hi:[1,1,1]\n"
    "v_pk_mul_f32 v[72:73], v[72:73], v[156:157] \n"
    "v_pk_mul_f32 v[74:75], v[74:75], v[158:159] \n"
    "v_pk_mul_f32 v[88:89], v[88:89], v[164:165] \n"
    "v_pk_mul_f32 v[90:91], v[90:91], v[166:167] \n"
    "v_pk_mul_f32 v[76:77], v[76:77], v[156:157] \n"
    "v_pk_mul_f32 v[78:79], v[78:79], v[158:159] \n"
    "v_pk_mul_f32 v[92:93], v[92:93], v[164:165] \n"
    "v_pk_mul_f32 v[94:95], v[94:95], v[166:167] \n"
    "v_cvt_pk_bf16_f32 v192, v72, v73\n"
    "v_cvt_pk_bf16_f32 v193, v74, v75\n"
    "v_cvt_pk_bf16_f32 v194, v88, v89\n"
    "v_cvt_pk_bf16_f32 v195, v90, v91\n"
    "v_cvt_pk_bf16_f32 v196, v76, v77\n"
    "v_cvt_pk_bf16_f32 v197, v78, v79\n"
    "v_cvt_pk_bf16_f32 v198, v92, v93\n"
    "v_cvt_pk_bf16_f32 v199, v94, v95\n"
    "buffer_store_dwordx4 v[192:195], v226, %[rs], %[tb0] offen offset:0 sc1\n"
    "buffer_store_dwordx4 v[196:199], v227, %[rs], %[tb0] offen offset:0 sc1\n"
    "s_waitcnt lgkmcnt(0)\n"
    "v_pk_fma_f32 v[96:97], v[168:169], v[144:145], v[96:97] op_sel_hi:[1,0,1]\n"
    "v_pk_fma_f32 v[98:99], v[170:171], v[144:145], v[98:99] op_sel_hi:[1,0,1]\n"
    "v_pk_fma_f32 v[112:113], v[176:177], v[144:145], v[112:113] op_sel_hi:[1,0,1]\n"
    "v_pk_fma_f32 v[114:115], v[178:179], v[144:145], v[114:115] op_sel_hi:[1,0,1]\n"
    "v_pk_fma_f32 v[100:101], v[168:169], v[144:145], v[100:101] op_sel:[0,1,0] op_sel_hi:[1,1,1]\n"
    "v_pk_fma_f32 v[102:103], v[170:171], v[144:145], v[102:103] op_sel:[0,1,0] op_sel_hi:[1,1,1]\n"
    "v_pk_fma_f32 v[116:117], v[176:177], v[144:145], v[116:117] op_sel:[0,1,0] op_sel_hi:[1,1,1]\n"
    "v_pk_fma_f32 v[118:119], v[178:179], v[144:145], v[118:119] op_sel:[0,1,0] op_sel_hi:[1,1,1]\n"
    "v_pk_mul_f32 v[96:97], v[96:97], v[172:173] \n"
    "v_pk_mul_f32 v[98:99], v[98:99], v[174:175] \n"
    "v_pk_mul_f32 v[112:113], v[112:113], v[180:181] \n"
    "v_pk_mul_f32 v[114:115], v[114:115], v[182:183] \n"
    "v_pk_mul_f32 v[100:101], v[100:101], v[172:173] \n"
    "v_pk_mul_f32 v[102:103], v[102:103], v[174:175] \n"
    "v_pk_mul_f32 v[116:117], v[116:117], v[180:181] \n"
    "v_pk_mul_f32 v[118:119], v[118:119], v[182:183] \n"
    "v_cvt_pk_bf16_f32 v200, v96, v97\n"
    "v_cvt_pk_bf16_f32 v201, v98, v99\n"
    "v_cvt_pk_bf16_f32 v202, v112, v113\n"
    "v_cvt_pk_bf16_f32 v203, v114, v115\n"
    "v_cvt_pk_bf16_f32 v204, v100, v101\n"
    "v_cvt_pk_bf16_f32 v205, v102, v103\n"
    "v_cvt_pk_bf16_f32 v206, v116, v117\n"
    "v_cvt_pk_bf16_f32 v207, v118, v119\n"
    "buffer_store_dwordx4 v[200:203], v224, %[rs], %[tb1] offen offset:0 sc1\n"
    "buffer_store_dwordx4 v[204:207], v225, %[rs], %[tb1] offen offset:0 sc1\n"
    "v_pk_fma_f32 v[104:105], v[168:169], v[146:147], v[104:105] op_sel_hi:[1,0,1]\n"
    "v_pk_fma_f32 v[106:107], v[170:171], v[146:147], v[106:107] op_sel_hi:[1,0,1]\n"
    "v_pk_fma_f32 v[120:121], v[176:177], v[146:147], v[120:121] op_sel_hi:[1,0,1]\n"
    "v_pk_fma_f32 v[122:123], v[178:179], v[146:147], v[122:123] op_sel_hi:[1,0,1]\n"
    "v_pk_fma_f32 v[108:109], v[168:169], v[146:147], v[108:109] op_sel:[0,1,0] op_sel_hi:[1,1,1]\n"
    "v_pk_fma_f32 v[110:111], v[170:171], v[146:147], v[110:111] op_sel:[0,1,0] op_sel_hi:[1,1,1]\n"
    "v_pk_fma_f32 v[124:125], v[176:177], v[146:147], v[124:125] op_sel:[0,1,0] op_sel_hi:[1,1,1]\n"
    "v_pk_fma_f32 v[126:127], v[178:179], v[146:147], v[126:127] op_sel:[0,1,0] op_sel_hi:[1,1,1]\n"
    "v_pk_mul_f32 v[104:105], v[104:105], v[172:173] \n"
    "v_pk_mul_f32 v[106:107], v[106:107], v[174:175] \n"
    "v_pk_mul_f32 v[120:121], v[120:121], v[180:181] \n"
    "v_pk_mul_f32 v[122:123], v[122:123], v[182:183] \n"
    "v_pk_mul_f32 v[108:109], v[108:109], v[172:173] \n"
    "v_pk_mul_f32 v[110:111], v[110:111], v[174:175] \n"
    "v_pk_mul_f32 v[124:125], v[124:125], v[180:181] \n"
    "v_pk_mul_f32 v[126:127], v[126:127], v[182:183] \n"
    "v_cvt_pk_bf16_f32 v208, v104, v105\n"
    "v_cvt_pk_bf16_f32 v209, v106, v107\n"
    "v_cvt_pk_bf16_f32 v210, v120, v121\n"
    "v_cvt_pk_bf16_f32 v211, v122, v123\n"
    "v_cvt_pk_bf16_f32 v212, v108, v109\n"
    "v_cvt_pk_bf16_f32 v213, v110, v111\n"
    "v_cvt_pk_bf16_f32 v214, v124, v125\n"
    "v_cvt_pk_bf16_f32 v215, v126, v127\n"
    "buffer_store_dwordx4 v[208:211], v226, %[rs], %[tb1] offen offset:0 sc1\n"
    "buffer_store_dwordx4 v[212:215], v227, %[rs], %[tb1] offen offset:0 sc1\n"
    P1_OPS);
}

DEV bf16x16 phalf(f32x16 s, int i) {
  f32x8 r = i ? __builtin_shufflevector(s, s, 8, 9, 10, 11, 12, 13, 14, 15) : __builtin_shufflevector(s, s, 0, 1, 2, 3, 4, 5, 6, 7);
  return __builtin_bit_cast(bf16x16, r);
}
DEV unsigned wd_base(int cb, int w) { return WS_WD + (cb * 54 + 27 * (w >> 1) + 14 * (w & 1)) * 5120; }
DEV void p2_addr(int lane, int odd, u32x4& k, u32x2& o) {
  const int h = lane >> 5, rho = lane & 31, sw = swap23(rho);
  const unsigned av = QT + (sw < 16 ? h * 256 + sw * 16 : sw < 20 ? 768 + h * 64 + sw * 16 : (sw & 7) == 4 ? QT : QT + 16), ob = (rho >> 4) * 20480 + (rho & 15) * 16;
  k = u32x4{h * 512u + rho * 16, 1024u + (rho & 15) * 16 + (rho >> 4) * 8, av, av + (sw < 16 ? 512 : sw < 20 ? 128 : 0)};
  o = u32x2{ob + h * 256 + odd * 768, ob + 512 + odd * 8};
}
struct Stash { f32x32 a, b, c; f32x16 d; f32x8 e; };
DEV void p2_compute(char* ws, int head, int qb, int cb, int wv, int lane, const P1Out& p, Stash& st) {
  const int idx = 0xDDDD;
  u32x4 ka;
  u32x2 oa;
  p2_addr(lane, head & 1, ka, oa);
  u32x8 v = {(unsigned)idx, L_FLG, L_FLG + wv * 4, lane * 16u, (C_FQ + head * 8 + qb) * 256, ka[0], ka[1], ka[2]};
  u32x4 w = {ka[3], oa[0], oa[1], ka[1] + (lane >> 5) * QT};
  const unsigned tb = WS_O + (qb * 16 + wv * 4) * 20480 + (head >> 1) * 1280;
  unsigned wd = wd_base(cb, wv) + 15 * 1024;
  f32x16 o[2];
  bf16x8 q0[2];
  u32x2 qt;
  int need, dl;
  asm volatile(
    "v_mov_b32 v64, 0\n"
    "v_mov_b32 v65, 0\n"
    "v_mov_b32 v66, 0\n"
    "v_mov_b32 v67, 0\n"
    "v_mov_b32 v68, 0\n"
    "v_mov_b32 v69, 0\n"
    "v_mov_b32 v70, 0\n"
    "v_mov_b32 v71, 0\n"
    "v_mov_b32 v72, 0\n"
    "v_mov_b32 v73, 0\n"
    "v_mov_b32 v74, 0\n"
    "v_mov_b32 v75, 0\n"
    "v_mov_b32 v76, 0\n"
    "v_mov_b32 v77, 0\n"
    "v_mov_b32 v78, 0\n"
    "v_mov_b32 v79, 0\n"
    "v_mov_b32 v80, 0\n"
    "v_mov_b32 v81, 0\n"
    "v_mov_b32 v82, 0\n"
    "v_mov_b32 v83, 0\n"
    "v_mov_b32 v84, 0\n"
    "v_mov_b32 v85, 0\n"
    "v_mov_b32 v86, 0\n"
    "v_mov_b32 v87, 0\n"
    "v_mov_b32 v88, 0\n"
    "v_mov_b32 v89, 0\n"
    "v_mov_b32 v90, 0\n"
    "v_mov_b32 v91, 0\n"
    "v_mov_b32 v92, 0\n"
    "v_mov_b32 v93, 0\n"
    "v_mov_b32 v94, 0\n"
    "v_mov_b32 v95, 0\n"
    "s_waitcnt vmcnt(0)\n"
    "s_mov_b64 exec, %[sm]\n"
    "s_cbranch_execz 13f\n"
    "global_atomic_add v252, v253, %[fl] sc1\n"
    "13:\n"
    "s_mov_b64 exec, -1\n"
    "v_mov_b32 v9, 0\n"
    "s_mov_b32 %[dl], 200000\n"
    "s_mov_b64 exec, 1\n"
    "global_atomic_add v8, v124, v9, %[fl] sc0 sc1\n"
    "s_sleep 4\n"
    "1:\n"
    "global_atomic_add v24, v124, v9, %[fl] sc0 sc1\n"
    "s_sleep 4\n"
    "s_waitcnt vmcnt(1)\n"
    "v_cmp_gt_u32 vcc, 8, v8\n"
    "s_cbranch_vccz 2f\n"
    "global_atomic_add v8, v124, v9, %[fl] sc0 sc1\n"
    "s_sleep 4\n"
    "s_waitcnt vmcnt(1)\n"
    "v_cmp_gt_u32 vcc, 8, v24\n"
    "s_cbranch_vccz 2f\n"
    "s_sub_u32 %[dl], %[dl], 1\n"
    "s_cbranch_scc0 1b\n"
    "2:\n"
    "s_mov_b64 exec, -1\n"
    "global_load_dwordx2 v[118:119], v131, %[qp] sc1\n"
    "global_load_dwordx4 v[108:111], v123, %[qp] sc1\n"
    "global_load_dwordx4 v[112:115], v123, %[qp] offset:1280 sc1\n"
    "s_mov_b32 %[need], 2\n"
    "3:\n"
    "ds_read_b32 v0, v121\n"
    "s_waitcnt lgkmcnt(0)\n"
    "v_cmp_eq_u32 vcc, 0, v0\n"
    "s_cbranch_vccnz 3b\n"
    "ds_read_b64 v[116:117], v126 offset:0\n"
    "ds_read_b128 v[96:99], v125 offset:0\n"
    ".p2align 5\n"
    "s_waitcnt vmcnt(0) lgkmcnt(1)\n"
    "v_mfma_f32_32x32x4_2b_bf16 v[0:31], v[116:117], v[118:119], 0\n"
    "ds_read_b64 v[116:117], v126 offset:2688\n"
    "ds_read_b128 v[100:103], v127 offset:0\n"
    "v_mov_b32 v131, 0\n"
    "s_nop 14\n"
    "s_waitcnt lgkmcnt(2)\n"
    "v_mfma_f32_32x32x16_bf16 v[0:15], v[96:99], v[108:111], v[0:15]\n"
    "v_mfma_f32_32x32x16_bf16 v[16:31], v[96:99], v[112:115], v[16:31]\n"
    "ds_read_b128 v[96:99], v125 offset:2688\n"
    "ds_read_b128 v[104:107], v128 offset:0\n"
    "s_nop 7\n"
    "s_waitcnt lgkmcnt(2)\n"
    "v_exp_f32 v0, v0\n"
    "v_exp_f32 v1, v1\n"
    "v_exp_f32 v2, v2\n"
    "v_exp_f32 v3, v3\n"
    "v_exp_f32 v4, v4\n"
    "v_exp_f32 v5, v5\n"
    "v_mfma_f32_32x32x4_2b_bf16 v[32:63], v[116:117], v[118:119], 0\n"
    "4:\n"
    "ds_read_b64 v[116:117], v126 offset:5376\n"
    "v_exp_f32 v6, v6\n"
    "v_exp_f32 v7, v7\n"
    "v_exp_f32 v16, v16\n"
    "v_exp_f32 v17, v17\n"
    "v_exp_f32 v18, v18\n"
    "v_exp_f32 v19, v19\n"
    "v_exp_f32 v20, v20\n"
    "v_exp_f32 v21, v21\n"
    "v_smfmac_f32_32x32x32_bf16 v[64:79], v[100:103], v[0:7], v120\n"
    "s_waitcnt lgkmcnt(1)\n"
    "v_exp_f32 v22, v22\n"
    "v_exp_f32 v23, v23\n"
    "v_exp_f32 v8, v8\n"
    "v_exp_f32 v9, v9\n"
    "v_smfmac_f32_32x32x32_bf16 v[80:95], v[100:103], v[16:23], v120\n"
    "ds_read_b128 v[100:103], v127 offset:2688\n"
    "v_exp_f32 v10, v10\n"
    "v_exp_f32 v11, v11\n"
    "v_exp_f32 v12, v12\n"
    "v_exp_f32 v13, v13\n"
    "v_mfma_f32_32x32x16_bf16 v[32:47], v[96:99], v[108:111], v[32:47]\n"
    "v_exp_f32 v14, v14\n"
    "v_exp_f32 v15, v15\n"
    "v_exp_f32 v24, v24\n"
    "v_exp_f32 v25, v25\n"
    "v_mfma_f32_32x32x16_bf16 v[48:63], v[96:99], v[112:115], v[48:63]\n"
    "ds_read_b128 v[96:99], v125 offset:5376\n"
    "v_exp_f32 v26, v26\n"
    "v_exp_f32 v27, v27\n"
    "v_exp_f32 v28, v28\n"
    "v_exp_f32 v29, v29\n"
    "v_smfmac_f32_32x32x32_bf16 v[64:79], v[104:107], v[8:15], v120\n"
    "s_waitcnt lgkmcnt(1)\n"
    "v_exp_f32 v30, v30\n"
    "v_exp_f32 v31, v31\n"
    "v_exp_f32 v32, v32\n"
    "v_exp_f32 v33, v33\n"
    "v_smfmac_f32_32x32x32_bf16 v[80:95], v[104:107], v[24:31], v120\n"
    "ds_read_b128 v[104:107], v128 offset:2688\n"
    "v_exp_f32 v34, v34\n"
    "v_exp_f32 v35, v35\n"
    "v_exp_f32 v36, v36\n"
    "v_exp_f32 v37, v37\n"
    "v_mfma_f32_32x32x4_2b_bf16 v[0:31], v[116:117], v[118:119], 0\n"
    "ds_read_b64 v[116:117], v126 offset:8064\n"
    "v_exp_f32 v38, v38\n"
    "v_exp_f32 v39, v39\n"
    "v_exp_f32 v48, v48\n"
    "v_exp_f32 v49, v49\n"
    "v_exp_f32 v50, v50\n"
    "v_exp_f32 v51, v51\n"
    "v_exp_f32 v52, v52\n"
    "v_exp_f32 v53, v53\n"
    "v_smfmac_f32_32x32x32_bf16 v[64:79], v[100:103], v[32:39], v120\n"
    "s_waitcnt lgkmcnt(1)\n"
    "v_exp_f32 v54, v54\n"
    "v_exp_f32 v55, v55\n"
    "v_exp_f32 v40, v40\n"
    "v_exp_f32 v41, v41\n"
    "v_smfmac_f32_32x32x32_bf16 v[80:95], v[100:103], v[48:55], v120\n"
    "ds_read_b128 v[100:103], v127 offset:5376\n"
    "v_exp_f32 v42, v42\n"
    "v_exp_f32 v43, v43\n"
    "v_exp_f32 v44, v44\n"
    "v_exp_f32 v45, v45\n"
    "v_mfma_f32_32x32x16_bf16 v[0:15], v[96:99], v[108:111], v[0:15]\n"
    "s_movk_i32 %[dl], 21504\n"
    "v_exp_f32 v46, v46\n"
    "v_exp_f32 v47, v47\n"
    "v_exp_f32 v56, v56\n"
    "v_exp_f32 v57, v57\n"
    "v_mfma_f32_32x32x16_bf16 v[16:31], v[96:99], v[112:115], v[16:31]\n"
    "ds_read_b128 v[96:99], v125 offset:8064\n"
    "v_exp_f32 v58, v58\n"
    "v_exp_f32 v59, v59\n"
    "v_exp_f32 v60, v60\n"
    "v_exp_f32 v61, v61\n"
    "v_smfmac_f32_32x32x32_bf16 v[64:79], v[104:107], v[40:47], v120\n"
    "s_waitcnt lgkmcnt(1)\n"
    "v_exp_f32 v62, v62\n"
    "v_exp_f32 v63, v63\n"
    "v_exp_f32 v0, v0\n"
    "v_exp_f32 v1, v1\n"
    "v_smfmac_f32_32x32x32_bf16 v[80:95], v[104:107], v[56:63], v120\n"
    "ds_read_b128 v[104:107], v128 offset:5376\n"
    "v_exp_f32 v2, v2\n"
    "v_exp_f32 v3, v3\n"
    "v_exp_f32 v4, v4\n"
    "v_exp_f32 v5, v5\n"
    "v_mfma_f32_32x32x4_2b_bf16 v[32:63], v[116:117], v[118:119], 0\n"
    "ds_read_b64 v[116:117], v126 offset:10752\n"
    "v_exp_f32 v6, v6\n"
    "v_exp_f32 v7, v7\n"
    "v_exp_f32 v16, v16\n"
    "v_exp_f32 v17, v17\n"
    "v_exp_f32 v18, v18\n"
    "v_exp_f32 v19, v19\n"
    "v_exp_f32 v20, v20\n"
    "v_exp_f32 v21, v21\n"
    "v_smfmac_f32_32x32x32_bf16 v[64:79], v[100:103], v[0:7], v120\n"
    "s_waitcnt lgkmcnt(1)\n"
    "v_exp_f32 v22, v22\n"
    "v_exp_f32 v23, v23\n"
    "v_exp_f32 v8, v8\n"
    "v_exp_f32 v9, v9\n"
    "v_smfmac_f32_32x32x32_bf16 v[80:95], v[100:103], v[16:23], v120\n"
    "ds_read_b128 v[100:103], v127 offset:8064\n"
    "v_exp_f32 v10, v10\n"
    "v_exp_f32 v11, v11\n"
    "v_exp_f32 v12, v12\n"
    "v_exp_f32 v13, v13\n"
    "v_mfma_f32_32x32x16_bf16 v[32:47], v[96:99], v[108:111], v[32:47]\n"
    "s_cmp_eq_u32 %[need], 7\n"
    "v_exp_f32 v14, v14\n"
    "v_exp_f32 v15, v15\n"
    "v_exp_f32 v24, v24\n"
    "v_exp_f32 v25, v25\n"
    "v_mfma_f32_32x32x16_bf16 v[48:63], v[96:99], v[112:115], v[48:63]\n"
    "ds_read_b128 v[96:99], v125 offset:10752\n"
    "v_exp_f32 v26, v26\n"
    "v_exp_f32 v27, v27\n"
    "v_exp_f32 v28, v28\n"
    "v_exp_f32 v29, v29\n"
    "v_smfmac_f32_32x32x32_bf16 v[64:79], v[104:107], v[8:15], v120\n"
    "s_waitcnt lgkmcnt(1)\n"
    "v_exp_f32 v30, v30\n"
    "v_exp_f32 v31, v31\n"
    "v_exp_f32 v32, v32\n"
    "v_exp_f32 v33, v33\n"
    "v_smfmac_f32_32x32x32_bf16 v[80:95], v[104:107], v[24:31], v120\n"
    "ds_read_b128 v[104:107], v128 offset:8064\n"
    "v_exp_f32 v34, v34\n"
    "v_exp_f32 v35, v35\n"
    "v_exp_f32 v36, v36\n"
    "v_exp_f32 v37, v37\n"
    "v_mfma_f32_32x32x4_2b_bf16 v[0:31], v[116:117], v[118:119], 0\n"
    "ds_read_b64 v[116:117], v126 offset:13440\n"
    "v_exp_f32 v38, v38\n"
    "v_exp_f32 v39, v39\n"
    "v_exp_f32 v48, v48\n"
    "v_exp_f32 v49, v49\n"
    "v_exp_f32 v50, v50\n"
    "v_exp_f32 v51, v51\n"
    "v_exp_f32 v52, v52\n"
    "v_exp_f32 v53, v53\n"
    "v_smfmac_f32_32x32x32_bf16 v[64:79], v[100:103], v[32:39], v120\n"
    "s_waitcnt lgkmcnt(1)\n"
    "v_exp_f32 v54, v54\n"
    "v_exp_f32 v55, v55\n"
    "v_exp_f32 v40, v40\n"
    "v_exp_f32 v41, v41\n"
    "v_smfmac_f32_32x32x32_bf16 v[80:95], v[100:103], v[48:55], v120\n"
    "ds_read_b128 v[100:103], v127 offset:10752\n"
    "v_exp_f32 v42, v42\n"
    "v_exp_f32 v43, v43\n"
    "v_exp_f32 v44, v44\n"
    "v_exp_f32 v45, v45\n"
    "v_mfma_f32_32x32x16_bf16 v[0:15], v[96:99], v[108:111], v[0:15]\n"
    "s_cmov_b32 %[dl], -107520\n"
    "v_exp_f32 v46, v46\n"
    "v_exp_f32 v47, v47\n"
    "v_exp_f32 v56, v56\n"
    "v_exp_f32 v57, v57\n"
    "v_mfma_f32_32x32x16_bf16 v[16:31], v[96:99], v[112:115], v[16:31]\n"
    "ds_read_b128 v[96:99], v125 offset:13440\n"
    "v_exp_f32 v58, v58\n"
    "v_exp_f32 v59, v59\n"
    "v_exp_f32 v60, v60\n"
    "v_exp_f32 v61, v61\n"
    "v_smfmac_f32_32x32x32_bf16 v[64:79], v[104:107], v[40:47], v120\n"
    "s_waitcnt lgkmcnt(1)\n"
    "v_exp_f32 v62, v62\n"
    "v_exp_f32 v63, v63\n"
    "v_exp_f32 v0, v0\n"
    "v_exp_f32 v1, v1\n"
    "v_smfmac_f32_32x32x32_bf16 v[80:95], v[104:107], v[56:63], v120\n"
    "ds_read_b128 v[104:107], v128 offset:10752\n"
    "v_exp_f32 v2, v2\n"
    "v_exp_f32 v3, v3\n"
    "v_exp_f32 v4, v4\n"
    "v_exp_f32 v5, v5\n"
    "v_mfma_f32_32x32x4_2b_bf16 v[32:63], v[116:117], v[118:119], 0\n"
    "ds_read_b64 v[116:117], v126 offset:16128\n"
    "v_exp_f32 v6, v6\n"
    "v_exp_f32 v7, v7\n"
    "v_exp_f32 v16, v16\n"
    "v_exp_f32 v17, v17\n"
    "v_exp_f32 v18, v18\n"
    "v_exp_f32 v19, v19\n"
    "v_exp_f32 v20, v20\n"
    "v_exp_f32 v21, v21\n"
    "v_smfmac_f32_32x32x32_bf16 v[64:79], v[100:103], v[0:7], v120\n"
    "s_waitcnt lgkmcnt(1)\n"
    "v_exp_f32 v22, v22\n"
    "v_exp_f32 v23, v23\n"
    "v_exp_f32 v8, v8\n"
    "v_exp_f32 v9, v9\n"
    "v_smfmac_f32_32x32x32_bf16 v[80:95], v[100:103], v[16:23], v120\n"
    "ds_read_b128 v[100:103], v127 offset:13440\n"
    "v_exp_f32 v10, v10\n"
    "v_exp_f32 v11, v11\n"
    "v_exp_f32 v12, v12\n"
    "v_exp_f32 v13, v13\n"
    "v_mfma_f32_32x32x16_bf16 v[32:47], v[96:99], v[108:111], v[32:47]\n"
    "ds_read_b32 v124, v121\n"
    "v_exp_f32 v14, v14\n"
    "v_exp_f32 v15, v15\n"
    "v_exp_f32 v24, v24\n"
    "v_exp_f32 v25, v25\n"
    "v_mfma_f32_32x32x16_bf16 v[48:63], v[96:99], v[112:115], v[48:63]\n"
    "ds_read_b128 v[96:99], v125 offset:16128\n"
    "v_exp_f32 v26, v26\n"
    "v_exp_f32 v27, v27\n"
    "v_exp_f32 v28, v28\n"
    "v_exp_f32 v29, v29\n"
    "v_smfmac_f32_32x32x32_bf16 v[64:79], v[104:107], v[8:15], v120\n"
    "s_waitcnt lgkmcnt(2)\n"
    "v_exp_f32 v30, v30\n"
    "v_exp_f32 v31, v31\n"
    "v_exp_f32 v32, v32\n"
    "v_exp_f32 v33, v33\n"
    "v_smfmac_f32_32x32x32_bf16 v[80:95], v[104:107], v[24:31], v120\n"
    "ds_read_b128 v[104:107], v128 offset:13440\n"
    "v_exp_f32 v34, v34\n"
    "v_exp_f32 v35, v35\n"
    "v_exp_f32 v36, v36\n"
    "v_exp_f32 v37, v37\n"
    "v_mfma_f32_32x32x4_2b_bf16 v[0:31], v[116:117], v[118:119], 0\n"
    "ds_read_b64 v[116:117], v126 offset:18816\n"
    "v_exp_f32 v38, v38\n"
    "v_exp_f32 v39, v39\n"
    "v_exp_f32 v48, v48\n"
    "v_exp_f32 v49, v49\n"
    "v_exp_f32 v50, v50\n"
    "v_exp_f32 v51, v51\n"
    "v_exp_f32 v52, v52\n"
    "v_exp_f32 v53, v53\n"
    "v_smfmac_f32_32x32x32_bf16 v[64:79], v[100:103], v[32:39], v120\n"
    "s_waitcnt lgkmcnt(1)\n"
    "v_cmp_gt_u32 vcc, %[need], v124\n"
    "v_exp_f32 v54, v54\n"
    "v_exp_f32 v55, v55\n"
    "v_exp_f32 v40, v40\n"
    "v_exp_f32 v41, v41\n"
    "v_smfmac_f32_32x32x32_bf16 v[80:95], v[100:103], v[48:55], v120\n"
    "ds_read_b128 v[100:103], v127 offset:16128\n"
    "v_exp_f32 v42, v42\n"
    "v_exp_f32 v43, v43\n"
    "v_exp_f32 v44, v44\n"
    "v_exp_f32 v45, v45\n"
    "v_mfma_f32_32x32x16_bf16 v[0:15], v[96:99], v[108:111], v[0:15]\n"
    "s_cbranch_vccnz 8f\n"
    "7:\n"
    "v_exp_f32 v46, v46\n"
    "v_exp_f32 v47, v47\n"
    "v_exp_f32 v56, v56\n"
    "v_exp_f32 v57, v57\n"
    "v_mfma_f32_32x32x16_bf16 v[16:31], v[96:99], v[112:115], v[16:31]\n"
    "ds_read_b128 v[96:99], v125 offset:18816\n"
    "v_exp_f32 v58, v58\n"
    "v_exp_f32 v59, v59\n"
    "v_exp_f32 v60, v60\n"
    "v_exp_f32 v61, v61\n"
    "v_smfmac_f32_32x32x32_bf16 v[64:79], v[104:107], v[40:47], v120\n"
    "s_waitcnt lgkmcnt(1)\n"
    "v_add_u32 v126, %[dl], v126\n"
    "v_exp_f32 v62, v62\n"
    "v_exp_f32 v63, v63\n"
    "v_exp_f32 v0, v0\n"
    "v_exp_f32 v1, v1\n"
    "v_smfmac_f32_32x32x32_bf16 v[80:95], v[104:107], v[56:63], v120\n"
    "ds_read_b128 v[104:107], v128 offset:16128\n"
    "v_exp_f32 v2, v2\n"
    "v_exp_f32 v3, v3\n"
    "v_exp_f32 v4, v4\n"
    "v_exp_f32 v5, v5\n"
    "v_mfma_f32_32x32x4_2b_bf16 v[32:63], v[116:117], v[118:119], 0\n"
    "ds_read_b64 v[116:117], v126 offset:0\n"
    "v_exp_f32 v6, v6\n"
    "v_exp_f32 v7, v7\n"
    "v_exp_f32 v16, v16\n"
    "v_exp_f32 v17, v17\n"
    "v_exp_f32 v18, v18\n"
    "v_exp_f32 v19, v19\n"
    "v_exp_f32 v20, v20\n"
    "v_exp_f32 v21, v21\n"
    "v_smfmac_f32_32x32x32_bf16 v[64:79], v[100:103], v[0:7], v120\n"
    "s_waitcnt lgkmcnt(1)\n"
    "v_add_u32 v125, %[dl], v125\n"
    "v_exp_f32 v22, v22\n"
    "v_exp_f32 v23, v23\n"
    "v_exp_f32 v8, v8\n"
    "v_exp_f32 v9, v9\n"
    "v_smfmac_f32_32x32x32_bf16 v[80:95], v[100:103], v[16:23], v120\n"
    "ds_read_b128 v[100:103], v127 offset:18816\n"
    "v_exp_f32 v10, v10\n"
    "v_exp_f32 v11, v11\n"
    "v_exp_f32 v12, v12\n"
    "v_exp_f32 v13, v13\n"
    "v_mfma_f32_32x32x16_bf16 v[32:47], v[96:99], v[108:111], v[32:47]\n"
    "s_add_u32 %[need], %[need], 1\n"
    "v_add_u32 v127, %[dl], v127\n"
    "v_exp_f32 v14, v14\n"
    "v_exp_f32 v15, v15\n"
    "v_exp_f32 v24, v24\n"
    "v_exp_f32 v25, v25\n"
    "v_mfma_f32_32x32x16_bf16 v[48:63], v[96:99], v[112:115], v[48:63]\n"
    "ds_read_b128 v[96:99], v125 offset:0\n"
    "v_exp_f32 v26, v26\n"
    "v_exp_f32 v27, v27\n"
    "v_exp_f32 v28, v28\n"
    "v_exp_f32 v29, v29\n"
    "v_smfmac_f32_32x32x32_bf16 v[64:79], v[104:107], v[8:15], v120\n"
    "s_waitcnt lgkmcnt(1)\n"
    "v_add_u32 v131, 1, v131\n"
    "v_exp_f32 v30, v30\n"
    "v_exp_f32 v31, v31\n"
    "v_exp_f32 v32, v32\n"
    "v_exp_f32 v33, v33\n"
    "v_smfmac_f32_32x32x32_bf16 v[80:95], v[104:107], v[24:31], v120\n"
    "ds_read_b128 v[104:107], v128 offset:18816\n"
    "v_exp_f32 v34, v34\n"
    "v_exp_f32 v35, v35\n"
    "v_exp_f32 v36, v36\n"
    "v_exp_f32 v37, v37\n"
    "v_mfma_f32_32x32x4_2b_bf16 v[0:31], v[116:117], v[118:119], 0\n"
    "ds_read_b64 v[116:117], v126 offset:2688\n"
    "v_exp_f32 v38, v38\n"
    "v_exp_f32 v39, v39\n"
    "v_exp_f32 v48, v48\n"
    "v_exp_f32 v49, v49\n"
    "v_exp_f32 v50, v50\n"
    "v_exp_f32 v51, v51\n"
    "v_exp_f32 v52, v52\n"
    "v_exp_f32 v53, v53\n"
    "v_smfmac_f32_32x32x32_bf16 v[64:79], v[100:103], v[32:39], v120\n"
    "s_waitcnt lgkmcnt(1)\n"
    "ds_write_b32 v122, v131 offset:16\n"
    "v_exp_f32 v54, v54\n"
    "v_exp_f32 v55, v55\n"
    "v_exp_f32 v40, v40\n"
    "v_exp_f32 v41, v41\n"
    "v_smfmac_f32_32x32x32_bf16 v[80:95], v[100:103], v[48:55], v120\n"
    "ds_read_b128 v[100:103], v127 offset:0\n"
    "v_exp_f32 v42, v42\n"
    "v_exp_f32 v43, v43\n"
    "v_exp_f32 v44, v44\n"
    "v_exp_f32 v45, v45\n"
    "v_mfma_f32_32x32x16_bf16 v[0:15], v[96:99], v[108:111], v[0:15]\n"
    "s_cmp_lt_u32 %[need], 8\n"
    "v_add_u32 v128, %[dl], v128\n"
    "v_exp_f32 v46, v46\n"
    "v_exp_f32 v47, v47\n"
    "v_exp_f32 v56, v56\n"
    "v_exp_f32 v57, v57\n"
    "v_mfma_f32_32x32x16_bf16 v[16:31], v[96:99], v[112:115], v[16:31]\n"
    "ds_read_b128 v[96:99], v125 offset:2688\n"
    "v_exp_f32 v58, v58\n"
    "v_exp_f32 v59, v59\n"
    "v_exp_f32 v60, v60\n"
    "v_exp_f32 v61, v61\n"
    "v_smfmac_f32_32x32x32_bf16 v[64:79], v[104:107], v[40:47], v120\n"
    "s_waitcnt lgkmcnt(1)\n"
    "v_exp_f32 v62, v62\n"
    "v_exp_f32 v63, v63\n"
    "v_exp_f32 v0, v0\n"
    "v_exp_f32 v1, v1\n"
    "v_smfmac_f32_32x32x32_bf16 v[80:95], v[104:107], v[56:63], v120\n"
    "ds_read_b128 v[104:107], v128 offset:0\n"
    "v_exp_f32 v2, v2\n"
    "v_exp_f32 v3, v3\n"
    "v_exp_f32 v4, v4\n"
    "v_exp_f32 v5, v5\n"
    "v_mfma_f32_32x32x4_2b_bf16 v[32:63], v[116:117], v[118:119], 0\n"
    "s_cbranch_scc1 4b\n"
    "ds_read_b64 v[116:117], v126 offset:5376\n"
    "ds_read_b32 v124, v121 offset:4\n"
    "v_exp_f32 v6, v6\n"
    "v_exp_f32 v7, v7\n"
    "v_exp_f32 v16, v16\n"
    "v_exp_f32 v17, v17\n"
    "v_exp_f32 v18, v18\n"
    "v_exp_f32 v19, v19\n"
    "v_exp_f32 v20, v20\n"
    "v_exp_f32 v21, v21\n"
    "v_smfmac_f32_32x32x32_bf16 v[64:79], v[100:103], v[0:7], v120\n"
    "s_waitcnt lgkmcnt(2)\n"
    "v_exp_f32 v22, v22\n"
    "v_exp_f32 v23, v23\n"
    "v_exp_f32 v8, v8\n"
    "v_exp_f32 v9, v9\n"
    "v_smfmac_f32_32x32x32_bf16 v[80:95], v[100:103], v[16:23], v120\n"
    "ds_read_b128 v[100:103], v127 offset:2688\n"
    "v_exp_f32 v10, v10\n"
    "v_exp_f32 v11, v11\n"
    "v_exp_f32 v12, v12\n"
    "v_exp_f32 v13, v13\n"
    "v_mfma_f32_32x32x16_bf16 v[32:47], v[96:99], v[108:111], v[32:47]\n"
    "s_waitcnt lgkmcnt(1)\n"
    "v_cmp_gt_u32 vcc, 2, v124\n"
    "v_exp_f32 v14, v14\n"
    "v_exp_f32 v15, v15\n"
    "v_exp_f32 v24, v24\n"
    "v_exp_f32 v25, v25\n"
    "v_mfma_f32_32x32x16_bf16 v[48:63], v[96:99], v[112:115], v[48:63]\n"
    "ds_read_b128 v[96:99], v125 offset:5376\n"
    "v_exp_f32 v26, v26\n"
    "v_exp_f32 v27, v27\n"
    "v_exp_f32 v28, v28\n"
    "v_exp_f32 v29, v29\n"
    "v_smfmac_f32_32x32x32_bf16 v[64:79], v[104:107], v[8:15], v120\n"
    "s_waitcnt lgkmcnt(1)\n"
    "s_cbranch_vccnz 28f\n"
    "27:\n"
    "v_exp_f32 v30, v30\n"
    "v_exp_f32 v31, v31\n"
    "v_exp_f32 v32, v32\n"
    "v_exp_f32 v33, v33\n"
    "v_smfmac_f32_32x32x32_bf16 v[80:95], v[104:107], v[24:31], v120\n"
    "ds_read_b128 v[104:107], v128 offset:2688\n"
    "v_exp_f32 v34, v34\n"
    "v_exp_f32 v35, v35\n"
    "v_exp_f32 v36, v36\n"
    "v_exp_f32 v37, v37\n"
    "v_mfma_f32_32x32x4_2b_bf16 v[0:31], v[116:117], v[118:119], 0\n"
    "ds_read_b64 v[116:117], v126 offset:8064\n"
    "v_exp_f32 v38, v38\n"
    "v_exp_f32 v39, v39\n"
    "v_exp_f32 v48, v48\n"
    "v_exp_f32 v49, v49\n"
    "v_exp_f32 v50, v50\n"
    "v_exp_f32 v51, v51\n"
    "v_exp_f32 v52, v52\n"
    "v_exp_f32 v53, v53\n"
    "v_smfmac_f32_32x32x32_bf16 v[64:79], v[100:103], v[32:39], v120\n"
    "s_waitcnt lgkmcnt(1)\n"
    "buffer_load_dwordx4 v[132:135], v123, %[rs], %[wd] offen offset:0 sc1\n"
    "v_exp_f32 v54, v54\n"
    "v_exp_f32 v55, v55\n"
    "v_exp_f32 v40, v40\n"
    "v_exp_f32 v41, v41\n"
    "v_smfmac_f32_32x32x32_bf16 v[80:95], v[100:103], v[48:55], v120\n"
    "ds_read_b128 v[100:103], v127 offset:5376\n"
    "v_exp_f32 v42, v42\n"
    "v_exp_f32 v43, v43\n"
    "v_exp_f32 v44, v44\n"
    "v_exp_f32 v45, v45\n"
    "v_mfma_f32_32x32x16_bf16 v[0:15], v[96:99], v[108:111], v[0:15]\n"
    "buffer_load_dwordx4 v[136:139], v123, %[rs], %[wd] offen offset:1024 sc1\n"
    "v_exp_f32 v46, v46\n"
    "v_exp_f32 v47, v47\n"
    "v_exp_f32 v56, v56\n"
    "v_exp_f32 v57, v57\n"
    "v_mfma_f32_32x32x16_bf16 v[16:31], v[96:99], v[112:115], v[16:31]\n"
    "ds_read_b128 v[96:99], v125 offset:8064\n"
    "v_exp_f32 v58, v58\n"
    "v_exp_f32 v59, v59\n"
    "v_exp_f32 v60, v60\n"
    "v_exp_f32 v61, v61\n"
    "v_smfmac_f32_32x32x32_bf16 v[64:79], v[104:107], v[40:47], v120\n"
    "s_waitcnt lgkmcnt(1)\n"
    "buffer_load_dwordx4 v[140:143], v123, %[rs], %[wd] offen offset:2048 sc1\n"
    "v_exp_f32 v62, v62\n"
    "v_exp_f32 v63, v63\n"
    "v_exp_f32 v0, v0\n"
    "v_exp_f32 v1, v1\n"
    "v_smfmac_f32_32x32x32_bf16 v[80:95], v[104:107], v[56:63], v120\n"
    "ds_read_b128 v[104:107], v128 offset:5376\n"
    "v_exp_f32 v2, v2\n"
    "v_exp_f32 v3, v3\n"
    "v_exp_f32 v4, v4\n"
    "v_exp_f32 v5, v5\n"
    "v_mfma_f32_32x32x4_2b_bf16 v[32:63], v[116:117], v[118:119], 0\n"
    "ds_read_b64 v[116:117], v126 offset:10752\n"
    "v_exp_f32 v6, v6\n"
    "v_exp_f32 v7, v7\n"
    "v_exp_f32 v16, v16\n"
    "v_exp_f32 v17, v17\n"
    "v_exp_f32 v18, v18\n"
    "v_exp_f32 v19, v19\n"
    "v_exp_f32 v20, v20\n"
    "v_exp_f32 v21, v21\n"
    "v_smfmac_f32_32x32x32_bf16 v[64:79], v[100:103], v[0:7], v120\n"
    "s_waitcnt lgkmcnt(1)\n"
    "buffer_load_dwordx4 v[144:147], v123, %[rs], %[wd] offen offset:3072 sc1\n"
    "v_exp_f32 v22, v22\n"
    "v_exp_f32 v23, v23\n"
    "v_exp_f32 v8, v8\n"
    "v_exp_f32 v9, v9\n"
    "v_smfmac_f32_32x32x32_bf16 v[80:95], v[100:103], v[16:23], v120\n"
    "ds_read_b128 v[100:103], v127 offset:8064\n"
    "v_exp_f32 v10, v10\n"
    "v_exp_f32 v11, v11\n"
    "v_exp_f32 v12, v12\n"
    "v_exp_f32 v13, v13\n"
    "v_mfma_f32_32x32x16_bf16 v[32:47], v[96:99], v[108:111], v[32:47]\n"
    "s_add_u32 %[wd], %[wd], 4096\n"
    "buffer_load_dwordx4 v[148:151], v123, %[rs], %[wd] offen offset:0 sc1\n"
    "v_exp_f32 v14, v14\n"
    "v_exp_f32 v15, v15\n"
    "v_exp_f32 v24, v24\n"
    "v_exp_f32 v25, v25\n"
    "v_mfma_f32_32x32x16_bf16 v[48:63], v[96:99], v[112:115], v[48:63]\n"
    "ds_read_b128 v[96:99], v125 offset:10752\n"
    "v_exp_f32 v26, v26\n"
    "v_exp_f32 v27, v27\n"
    "v_exp_f32 v28, v28\n"
    "v_exp_f32 v29, v29\n"
    "v_smfmac_f32_32x32x32_bf16 v[64:79], v[104:107], v[8:15], v120\n"
    "s_waitcnt lgkmcnt(1)\n"
    "buffer_load_dwordx4 v[152:155], v123, %[rs], %[wd] offen offset:1024 sc1\n"
    "v_exp_f32 v30, v30\n"
    "v_exp_f32 v31, v31\n"
    "v_exp_f32 v32, v32\n"
    "v_exp_f32 v33, v33\n"
    "v_smfmac_f32_32x32x32_bf16 v[80:95], v[104:107], v[24:31], v120\n"
    "ds_read_b128 v[104:107], v128 offset:8064\n"
    "v_exp_f32 v34, v34\n"
    "v_exp_f32 v35, v35\n"
    "v_exp_f32 v36, v36\n"
    "v_exp_f32 v37, v37\n"
    "v_mfma_f32_32x32x4_2b_bf16 v[0:31], v[116:117], v[118:119], 0\n"
    "ds_read_b64 v[116:117], v126 offset:13440\n"
    "v_exp_f32 v38, v38\n"
    "v_exp_f32 v39, v39\n"
    "v_exp_f32 v48, v48\n"
    "v_exp_f32 v49, v49\n"
    "v_exp_f32 v50, v50\n"
    "v_exp_f32 v51, v51\n"
    "v_exp_f32 v52, v52\n"
    "v_exp_f32 v53, v53\n"
    "v_smfmac_f32_32x32x32_bf16 v[64:79], v[100:103], v[32:39], v120\n"
    "s_waitcnt lgkmcnt(1)\n"
    "buffer_load_dwordx4 v[156:159], v123, %[rs], %[wd] offen offset:2048 sc1\n"
    "v_exp_f32 v54, v54\n"
    "v_exp_f32 v55, v55\n"
    "v_exp_f32 v40, v40\n"
    "v_exp_f32 v41, v41\n"
    "v_smfmac_f32_32x32x32_bf16 v[80:95], v[100:103], v[48:55], v120\n"
    "ds_read_b128 v[100:103], v127 offset:10752\n"
    "v_exp_f32 v42, v42\n"
    "v_exp_f32 v43, v43\n"
    "v_exp_f32 v44, v44\n"
    "v_exp_f32 v45, v45\n"
    "v_mfma_f32_32x32x16_bf16 v[0:15], v[96:99], v[108:111], v[0:15]\n"
    "buffer_load_dwordx4 v[160:163], v123, %[rs], %[wd] offen offset:3072 sc1\n"
    "v_exp_f32 v46, v46\n"
    "v_exp_f32 v47, v47\n"
    "v_exp_f32 v56, v56\n"
    "v_exp_f32 v57, v57\n"
    "v_mfma_f32_32x32x16_bf16 v[16:31], v[96:99], v[112:115], v[16:31]\n"
    "ds_read_b128 v[96:99], v125 offset:13440\n"
    "v_exp_f32 v58, v58\n"
    "v_exp_f32 v59, v59\n"
    "v_exp_f32 v60, v60\n"
    "v_exp_f32 v61, v61\n"
    "v_smfmac_f32_32x32x32_bf16 v[64:79], v[104:107], v[40:47], v120\n"
    "s_waitcnt lgkmcnt(1)\n"
    "s_add_u32 %[wd], %[wd], 4096\n"
    "buffer_load_dwordx4 v[164:167], v123, %[rs], %[wd] offen offset:0 sc1\n"
    "v_exp_f32 v62, v62\n"
    "v_exp_f32 v63, v63\n"
    "v_exp_f32 v0, v0\n"
    "v_exp_f32 v1, v1\n"
    "v_smfmac_f32_32x32x32_bf16 v[80:95], v[104:107], v[56:63], v120\n"
    "ds_read_b128 v[104:107], v128 offset:10752\n"
    "v_exp_f32 v2, v2\n"
    "v_exp_f32 v3, v3\n"
    "v_exp_f32 v4, v4\n"
    "v_exp_f32 v5, v5\n"
    "v_mfma_f32_32x32x4_2b_bf16 v[32:63], v[116:117], v[118:119], 0\n"
    "ds_read_b64 v[116:117], v126 offset:16128\n"
    "v_exp_f32 v6, v6\n"
    "v_exp_f32 v7, v7\n"
    "v_exp_f32 v16, v16\n"
    "v_exp_f32 v17, v17\n"
    "v_exp_f32 v18, v18\n"
    "v_exp_f32 v19, v19\n"
    "v_exp_f32 v20, v20\n"
    "v_exp_f32 v21, v21\n"
    "v_smfmac_f32_32x32x32_bf16 v[64:79], v[100:103], v[0:7], v120\n"
    "s_waitcnt lgkmcnt(1)\n"
    "buffer_load_dwordx4 v[168:171], v123, %[rs], %[wd] offen offset:1024 sc1\n"
    "v_exp_f32 v22, v22\n"
    "v_exp_f32 v23, v23\n"
    "v_exp_f32 v8, v8\n"
    "v_exp_f32 v9, v9\n"
    "v_smfmac_f32_32x32x32_bf16 v[80:95], v[100:103], v[16:23], v120\n"
    "ds_read_b128 v[100:103], v127 offset:13440\n"
    "v_exp_f32 v10, v10\n"
    "v_exp_f32 v11, v11\n"
    "v_exp_f32 v12, v12\n"
    "v_exp_f32 v13, v13\n"
    "v_mfma_f32_32x32x16_bf16 v[32:47], v[96:99], v[108:111], v[32:47]\n"
    "ds_read_b32 v124, v121\n"
    "buffer_load_dwordx4 v[172:175], v123, %[rs], %[wd] offen offset:2048 sc1\n"
    "v_exp_f32 v14, v14\n"
    "v_exp_f32 v15, v15\n"
    "v_exp_f32 v24, v24\n"
    "v_exp_f32 v25, v25\n"
    "v_mfma_f32_32x32x16_bf16 v[48:63], v[96:99], v[112:115], v[48:63]\n"
    "ds_read_b128 v[96:99], v125 offset:16128\n"
    "v_exp_f32 v26, v26\n"
    "v_exp_f32 v27, v27\n"
    "v_exp_f32 v28, v28\n"
    "v_exp_f32 v29, v29\n"
    "v_smfmac_f32_32x32x32_bf16 v[64:79], v[104:107], v[8:15], v120\n"
    "s_waitcnt lgkmcnt(2)\n"
    "buffer_load_dwordx4 v[176:179], v123, %[rs], %[wd] offen offset:3072 sc1\n"
    "v_exp_f32 v30, v30\n"
    "v_exp_f32 v31, v31\n"
    "v_exp_f32 v32, v32\n"
    "v_exp_f32 v33, v33\n"
    "v_smfmac_f32_32x32x32_bf16 v[80:95], v[104:107], v[24:31], v120\n"
    "ds_read_b128 v[104:107], v128 offset:13440\n"
    "v_exp_f32 v34, v34\n"
    "v_exp_f32 v35, v35\n"
    "v_exp_f32 v36, v36\n"
    "v_exp_f32 v37, v37\n"
    "v_mfma_f32_32x32x4_2b_bf16 v[0:31], v[116:117], v[118:119], 0\n"
    "ds_read_b64 v[116:117], v126 offset:18816\n"
    "v_exp_f32 v38, v38\n"
    "v_exp_f32 v39, v39\n"
    "v_exp_f32 v48, v48\n"
    "v_exp_f32 v49, v49\n"
    "v_exp_f32 v50, v50\n"
    "v_exp_f32 v51, v51\n"
    "v_exp_f32 v52, v52\n"
    "v_exp_f32 v53, v53\n"
    "v_smfmac_f32_32x32x32_bf16 v[64:79], v[100:103], v[32:39], v120\n"
    "s_waitcnt lgkmcnt(1)\n"
    "v_cmp_gt_u32 vcc, %[need], v124\n"
    "s_add_u32 %[wd], %[wd], 4096\n"
    "buffer_load_dwordx4 v[180:183], v123, %[rs], %[wd] offen offset:0 sc1\n"
    "v_exp_f32 v54, v54\n"
    "v_exp_f32 v55, v55\n"
    "v_exp_f32 v40, v40\n"
    "v_exp_f32 v41, v41\n"
    "v_smfmac_f32_32x32x32_bf16 v[80:95], v[100:103], v[48:55], v120\n"
    "ds_read_b128 v[100:103], v127 offset:16128\n"
    "v_exp_f32 v42, v42\n"
    "v_exp_f32 v43, v43\n"
    "v_exp_f32 v44, v44\n"
    "v_exp_f32 v45, v45\n"
    "v_mfma_f32_32x32x16_bf16 v[0:15], v[96:99], v[108:111], v[0:15]\n"
    "s_cbranch_vccnz 18f\n"
    "17:\n"
    "buffer_load_dwordx4 v[184:187], v123, %[rs], %[wd] offen offset:1024 sc1\n"
    "v_exp_f32 v46, v46\n"
    "v_exp_f32 v47, v47\n"
    "v_exp_f32 v56, v56\n"
    "v_exp_f32 v57, v57\n"
    "v_mfma_f32_32x32x16_bf16 v[16:31], v[96:99], v[112:115], v[16:31]\n"
    "ds_read_b128 v[96:99], v125 offset:18816\n"
    "v_exp_f32 v58, v58\n"
    "v_exp_f32 v59, v59\n"
    "v_exp_f32 v60, v60\n"
    "v_exp_f32 v61, v61\n"
    "v_smfmac_f32_32x32x32_bf16 v[64:79], v[104:107], v[40:47], v120\n"
    "s_waitcnt lgkmcnt(1)\n"
    "buffer_load_dwordx4 v[188:191], v123, %[rs], %[wd] offen offset:2048 sc1\n"
    "v_exp_f32 v62, v62\n"
    "v_exp_f32 v63, v63\n"
    "v_exp_f32 v0, v0\n"
    "v_exp_f32 v1, v1\n"
    "v_smfmac_f32_32x32x32_bf16 v[80:95], v[104:107], v[56:63], v120\n"
    "ds_read_b128 v[104:107], v128 offset:16128\n"
    "v_exp_f32 v2, v2\n"
    "v_exp_f32 v3, v3\n"
    "v_exp_f32 v4, v4\n"
    "v_exp_f32 v5, v5\n"
    "v_mfma_f32_32x32x4_2b_bf16 v[32:63], v[116:117], v[118:119], 0\n"
    "ds_read_b64 v[116:117], v126 offset:21504\n"
    "v_exp_f32 v6, v6\n"
    "v_exp_f32 v7, v7\n"
    "v_exp_f32 v16, v16\n"
    "v_exp_f32 v17, v17\n"
    "v_exp_f32 v18, v18\n"
    "v_exp_f32 v19, v19\n"
    "v_exp_f32 v20, v20\n"
    "v_exp_f32 v21, v21\n"
    "v_smfmac_f32_32x32x32_bf16 v[64:79], v[100:103], v[0:7], v120\n"
    "s_waitcnt lgkmcnt(1)\n"
    "buffer_load_dwordx4 v[192:195], v123, %[rs], %[wd] offen offset:3072 sc1\n"
    "v_exp_f32 v22, v22\n"
    "v_exp_f32 v23, v23\n"
    "v_exp_f32 v8, v8\n"
    "v_exp_f32 v9, v9\n"
    "v_smfmac_f32_32x32x32_bf16 v[80:95], v[100:103], v[16:23], v120\n"
    "ds_read_b128 v[100:103], v127 offset:18816\n"
    "v_exp_f32 v10, v10\n"
    "v_exp_f32 v11, v11\n"
    "v_exp_f32 v12, v12\n"
    "v_exp_f32 v13, v13\n"
    "v_mfma_f32_32x32x16_bf16 v[32:47], v[96:99], v[108:111], v[32:47]\n"
    "s_add_u32 %[wd], %[wd], 4096\n"
    "buffer_load_dwordx4 v[196:199], v123, %[rs], %[wd] offen offset:0 sc1\n"
    "v_exp_f32 v14, v14\n"
    "v_exp_f32 v15, v15\n"
    "v_exp_f32 v24, v24\n"
    "v_exp_f32 v25, v25\n"
    "v_mfma_f32_32x32x16_bf16 v[48:63], v[96:99], v[112:115], v[48:63]\n"
    "ds_read_b128 v[96:99], v125 offset:21504\n"
    "v_exp_f32 v26, v26\n"
    "v_exp_f32 v27, v27\n"
    "v_exp_f32 v28, v28\n"
    "v_exp_f32 v29, v29\n"
    "v_smfmac_f32_32x32x32_bf16 v[64:79], v[104:107], v[8:15], v120\n"
    "s_waitcnt lgkmcnt(1)\n"
    "buffer_load_dwordx4 v[200:203], v123, %[rs], %[wd] offen offset:1024 sc1\n"
    "v_exp_f32 v30, v30\n"
    "v_exp_f32 v31, v31\n"
    "v_exp_f32 v32, v32\n"
    "v_exp_f32 v33, v33\n"
    "v_smfmac_f32_32x32x32_bf16 v[80:95], v[104:107], v[24:31], v120\n"
    "ds_read_b128 v[104:107], v128 offset:18816\n"
    "v_exp_f32 v34, v34\n"
    "v_exp_f32 v35, v35\n"
    "v_exp_f32 v36, v36\n"
    "v_exp_f32 v37, v37\n"
    "v_mfma_f32_32x32x4_2b_bf16 v[0:31], v[116:117], v[118:119], 0\n"
    "ds_read_b64 v[116:117], v126 offset:24192\n"
    "v_exp_f32 v38, v38\n"
    "v_exp_f32 v39, v39\n"
    "v_exp_f32 v48, v48\n"
    "v_exp_f32 v49, v49\n"
    "v_exp_f32 v50, v50\n"
    "v_exp_f32 v51, v51\n"
    "v_exp_f32 v52, v52\n"
    "v_exp_f32 v53, v53\n"
    "v_smfmac_f32_32x32x32_bf16 v[64:79], v[100:103], v[32:39], v120\n"
    "s_waitcnt lgkmcnt(1)\n"
    "buffer_load_dwordx4 v[204:207], v123, %[rs], %[wd] offen offset:2048 sc1\n"
    "v_exp_f32 v54, v54\n"
    "v_exp_f32 v55, v55\n"
    "v_exp_f32 v40, v40\n"
    "v_exp_f32 v41, v41\n"
    "v_smfmac_f32_32x32x32_bf16 v[80:95], v[100:103], v[48:55], v120\n"
    "ds_read_b128 v[100:103], v127 offset:21504\n"
    "v_exp_f32 v42, v42\n"
    "v_exp_f32 v43, v43\n"
    "v_exp_f32 v44, v44\n"
    "v_exp_f32 v45, v45\n"
    "v_mfma_f32_32x32x16_bf16 v[0:15], v[96:99], v[108:111], v[0:15]\n"
    "buffer_load_dwordx4 v[208:211], v123, %[rs], %[wd] offen offset:3072 sc1\n"
    "v_exp_f32 v46, v46\n"
    "v_exp_f32 v47, v47\n"
    "v_exp_f32 v56, v56\n"
    "v_exp_f32 v57, v57\n"
    "v_mfma_f32_32x32x16_bf16 v[16:31], v[96:99], v[112:115], v[16:31]\n"
    "ds_read_b128 v[96:99], v125 offset:24192\n"
    "v_exp_f32 v58, v58\n"
    "v_exp_f32 v59, v59\n"
    "v_exp_f32 v60, v60\n"
    "v_exp_f32 v61, v61\n"
    "v_smfmac_f32_32x32x32_bf16 v[64:79], v[104:107], v[40:47], v120\n"
    "s_waitcnt lgkmcnt(1)\n"
    "s_add_u32 %[wd], %[wd], 4096\n"
    "buffer_load_dwordx4 v[212:215], v123, %[rs], %[wd] offen offset:0 sc1\n"
    "v_exp_f32 v62, v62\n"
    "v_exp_f32 v63, v63\n"
    "v_exp_f32 v0, v0\n"
    "v_exp_f32 v1, v1\n"
    "v_smfmac_f32_32x32x32_bf16 v[80:95], v[104:107], v[56:63], v120\n"
    "ds_read_b128 v[104:107], v128 offset:21504\n"
    "v_exp_f32 v2, v2\n"
    "v_exp_f32 v3, v3\n"
    "v_exp_f32 v4, v4\n"
    "v_exp_f32 v5, v5\n"
    "v_mfma_f32_32x32x4_2b_bf16 v[32:63], v[116:117], v[118:119], 0\n"
    "ds_read_b64 v[116:117], v126 offset:26880\n"
    "v_exp_f32 v6, v6\n"
    "v_exp_f32 v7, v7\n"
    "v_exp_f32 v16, v16\n"
    "v_exp_f32 v17, v17\n"
    "v_exp_f32 v18, v18\n"
    "v_exp_f32 v19, v19\n"
    "v_exp_f32 v20, v20\n"
    "v_exp_f32 v21, v21\n"
    "v_smfmac_f32_32x32x32_bf16 v[64:79], v[100:103], v[0:7], v120\n"
    "s_waitcnt lgkmcnt(1)\n"
    "buffer_load_dwordx4 v[216:219], v123, %[rs], %[wd] offen offset:1024 sc1\n"
    "v_exp_f32 v22, v22\n"
    "v_exp_f32 v23, v23\n"
    "v_exp_f32 v8, v8\n"
    "v_exp_f32 v9, v9\n"
    "v_smfmac_f32_32x32x32_bf16 v[80:95], v[100:103], v[16:23], v120\n"
    "ds_read_b128 v[100:103], v127 offset:24192\n"
    "v_exp_f32 v10, v10\n"
    "v_exp_f32 v11, v11\n"
    "v_exp_f32 v12, v12\n"
    "v_exp_f32 v13, v13\n"
    "v_mfma_f32_32x32x16_bf16 v[32:47], v[96:99], v[108:111], v[32:47]\n"
    "buffer_load_dwordx4 v[220:223], v123, %[rs], %[wd] offen offset:2048 sc1\n"
    "v_exp_f32 v14, v14\n"
    "v_exp_f32 v15, v15\n"
    "v_exp_f32 v24, v24\n"
    "v_exp_f32 v25, v25\n"
    "v_mfma_f32_32x32x16_bf16 v[48:63], v[96:99], v[112:115], v[48:63]\n"
    "ds_read_b128 v[96:99], v125 offset:26880\n"
    "v_exp_f32 v26, v26\n"
    "v_exp_f32 v27, v27\n"
    "v_exp_f32 v28, v28\n"
    "v_exp_f32 v29, v29\n"
    "v_smfmac_f32_32x32x32_bf16 v[64:79], v[104:107], v[8:15], v120\n"
    "s_waitcnt lgkmcnt(1)\n"
    "buffer_load_dwordx4 v[224:227], v123, %[rs], %[wd] offen offset:3072 sc1\n"
    "v_exp_f32 v30, v30\n"
    "v_exp_f32 v31, v31\n"
    "v_exp_f32 v32, v32\n"
    "v_exp_f32 v33, v33\n"
    "v_smfmac_f32_32x32x32_bf16 v[80:95], v[104:107], v[24:31], v120\n"
    "ds_read_b128 v[104:107], v128 offset:24192\n"
    "v_exp_f32 v34, v34\n"
    "v_exp_f32 v35, v35\n"
    "v_exp_f32 v36, v36\n"
    "v_exp_f32 v37, v37\n"
    "v_mfma_f32_32x32x4_2b_bf16 v[0:31], v[116:117], v[118:119], 0\n"
    "ds_read_b64 v[116:117], v126 offset:29568\n"
    "v_exp_f32 v38, v38\n"
    "v_exp_f32 v39, v39\n"
    "v_exp_f32 v48, v48\n"
    "v_exp_f32 v49, v49\n"
    "v_exp_f32 v50, v50\n"
    "v_exp_f32 v51, v51\n"
    "v_exp_f32 v52, v52\n"
    "v_exp_f32 v53, v53\n"
    "v_smfmac_f32_32x32x32_bf16 v[64:79], v[100:103], v[32:39], v120\n"
    "s_waitcnt lgkmcnt(1)\n"
    "s_add_u32 %[wd], %[wd], 4096\n"
    "buffer_load_dwordx4 v[228:231], v123, %[rs], %[wd] offen offset:0 sc1\n"
    "v_exp_f32 v54, v54\n"
    "v_exp_f32 v55, v55\n"
    "v_exp_f32 v40, v40\n"
    "v_exp_f32 v41, v41\n"
    "v_smfmac_f32_32x32x32_bf16 v[80:95], v[100:103], v[48:55], v120\n"
    "ds_read_b128 v[100:103], v127 offset:26880\n"
    "v_exp_f32 v42, v42\n"
    "v_exp_f32 v43, v43\n"
    "v_exp_f32 v44, v44\n"
    "v_exp_f32 v45, v45\n"
    "v_mfma_f32_32x32x16_bf16 v[0:15], v[96:99], v[108:111], v[0:15]\n"
    "buffer_load_dwordx4 v[232:235], v123, %[rs], %[wd] offen offset:1024 sc1\n"
    "v_exp_f32 v46, v46\n"
    "v_exp_f32 v47, v47\n"
    "v_exp_f32 v56, v56\n"
    "v_exp_f32 v57, v57\n"
    "v_mfma_f32_32x32x16_bf16 v[16:31], v[96:99], v[112:115], v[16:31]\n"
    "ds_read_b128 v[96:99], v125 offset:29568\n"
    "v_exp_f32 v58, v58\n"
    "v_exp_f32 v59, v59\n"
    "v_exp_f32 v60, v60\n"
    "v_exp_f32 v61, v61\n"
    "v_smfmac_f32_32x32x32_bf16 v[64:79], v[104:107], v[40:47], v120\n"
    "s_waitcnt lgkmcnt(1)\n"
    "buffer_load_dwordx4 v[236:239], v123, %[rs], %[wd] offen offset:2048 sc1\n"
    "v_exp_f32 v62, v62\n"
    "v_exp_f32 v63, v63\n"
    "v_exp_f32 v0, v0\n"
    "v_exp_f32 v1, v1\n"
    "v_smfmac_f32_32x32x32_bf16 v[80:95], v[104:107], v[56:63], v120\n"
    "ds_read_b128 v[104:107], v128 offset:26880\n"
    "v_exp_f32 v2, v2\n"
    "v_exp_f32 v3, v3\n"
    "v_exp_f32 v4, v4\n"
    "v_exp_f32 v5, v5\n"
    "v_mfma_f32_32x32x4_2b_bf16 v[32:63], v[116:117], v[118:119], 0\n"
    "ds_read_b64 v[116:117], v126 offset:32256\n"
    "v_exp_f32 v6, v6\n"
    "v_exp_f32 v7, v7\n"
    "v_exp_f32 v16, v16\n"
    "v_exp_f32 v17, v17\n"
    "v_exp_f32 v18, v18\n"
    "v_exp_f32 v19, v19\n"
    "v_exp_f32 v20, v20\n"
    "v_exp_f32 v21, v21\n"
    "v_smfmac_f32_32x32x32_bf16 v[64:79], v[100:103], v[0:7], v120\n"
    "s_waitcnt lgkmcnt(1)\n"
    "buffer_load_dwordx4 v[240:243], v123, %[rs], %[wd] offen offset:3072 sc1\n"
    "v_exp_f32 v22, v22\n"
    "v_exp_f32 v23, v23\n"
    "v_exp_f32 v8, v8\n"
    "v_exp_f32 v9, v9\n"
    "v_smfmac_f32_32x32x32_bf16 v[80:95], v[100:103], v[16:23], v120\n"
    "ds_read_b128 v[100:103], v127 offset:29568\n"
    "v_exp_f32 v10, v10\n"
    "v_exp_f32 v11, v11\n"
    "v_exp_f32 v12, v12\n"
    "v_exp_f32 v13, v13\n"
    "v_mfma_f32_32x32x16_bf16 v[32:47], v[96:99], v[108:111], v[32:47]\n"
    "s_add_u32 %[wd], %[wd], 4096\n"
    "buffer_load_dwordx4 v[244:247], v123, %[rs], %[wd] offen offset:0 sc1\n"
    "v_exp_f32 v14, v14\n"
    "v_exp_f32 v15, v15\n"
    "v_exp_f32 v24, v24\n"
    "v_exp_f32 v25, v25\n"
    "v_mfma_f32_32x32x16_bf16 v[48:63], v[96:99], v[112:115], v[48:63]\n"
    "ds_read_b128 v[96:99], v125 offset:32256\n"
    "v_exp_f32 v26, v26\n"
    "v_exp_f32 v27, v27\n"
    "v_exp_f32 v28, v28\n"
    "v_exp_f32 v29, v29\n"
    "v_smfmac_f32_32x32x32_bf16 v[64:79], v[104:107], v[8:15], v120\n"
    "s_waitcnt lgkmcnt(1)\n"
    "buffer_load_dwordx4 v[248:251], v123, %[rs], %[wd] offen offset:1024 sc1\n"
    "v_exp_f32 v30, v30\n"
    "v_exp_f32 v31, v31\n"
    "v_exp_f32 v32, v32\n"
    "v_exp_f32 v33, v33\n"
    "v_smfmac_f32_32x32x32_bf16 v[80:95], v[104:107], v[24:31], v120\n"
    "ds_read_b128 v[104:107], v128 offset:29568\n"
    "v_exp_f32 v34, v34\n"
    "v_exp_f32 v35, v35\n"
    "v_exp_f32 v36, v36\n"
    "v_exp_f32 v37, v37\n"
    "v_mfma_f32_32x32x4_2b_bf16 v[0:31], v[116:117], v[118:119], 0\n"
    "ds_read_b64 v[116:117], v126 offset:34944\n"
    "v_exp_f32 v38, v38\n"
    "v_exp_f32 v39, v39\n"
    "v_exp_f32 v48, v48\n"
    "v_exp_f32 v49, v49\n"
    "v_exp_f32 v50, v50\n"
    "v_exp_f32 v51, v51\n"
    "v_exp_f32 v52, v52\n"
    "v_exp_f32 v53, v53\n"
    "v_smfmac_f32_32x32x32_bf16 v[64:79], v[100:103], v[32:39], v120\n"
    "s_waitcnt lgkmcnt(1)\n"
    "v_exp_f32 v54, v54\n"
    "v_exp_f32 v55, v55\n"
    "v_exp_f32 v40, v40\n"
    "v_exp_f32 v41, v41\n"
    "v_smfmac_f32_32x32x32_bf16 v[80:95], v[100:103], v[48:55], v120\n"
    "ds_read_b128 v[100:103], v127 offset:32256\n"
    "v_exp_f32 v42, v42\n"
    "v_exp_f32 v43, v43\n"
    "v_exp_f32 v44, v44\n"
    "v_exp_f32 v45, v45\n"
    "v_mfma_f32_32x32x16_bf16 v[0:15], v[96:99], v[108:111], v[0:15]\n"
    "v_exp_f32 v46, v46\n"
    "v_exp_f32 v47, v47\n"
    "v_exp_f32 v56, v56\n"
    "v_exp_f32 v57, v57\n"
    "v_mfma_f32_32x32x16_bf16 v[16:31], v[96:99], v[112:115], v[16:31]\n"
    "ds_read_b128 v[96:99], v125 offset:34944\n"
    "v_exp_f32 v58, v58\n"
    "v_exp_f32 v59, v59\n"
    "v_exp_f32 v60, v60\n"
    "v_exp_f32 v61, v61\n"
    "v_smfmac_f32_32x32x32_bf16 v[64:79], v[104:107], v[40:47], v120\n"
    "s_waitcnt lgkmcnt(1)\n"
    "v_exp_f32 v62, v62\n"
    "v_exp_f32 v63, v63\n"
    "v_exp_f32 v0, v0\n"
    "v_exp_f32 v1, v1\n"
    "v_smfmac_f32_32x32x32_bf16 v[80:95], v[104:107], v[56:63], v120\n"
    "ds_read_b128 v[104:107], v128 offset:32256\n"
    "v_exp_f32 v2, v2\n"
    "v_exp_f32 v3, v3\n"
    "v_exp_f32 v4, v4\n"
    "v_exp_f32 v5, v5\n"
    "v_mfma_f32_32x32x4_2b_bf16 v[32:63], v[116:117], v[118:119], 0\n"
    "ds_read_b64 v[116:117], v126 offset:37632\n"
    "v_exp_f32 v6, v6\n"
    "v_exp_f32 v7, v7\n"
    "v_exp_f32 v16, v16\n"
    "v_exp_f32 v17, v17\n"
    "v_exp_f32 v18, v18\n"
    "v_exp_f32 v19, v19\n"
    "v_exp_f32 v20, v20\n"
    "v_exp_f32 v21, v21\n"
    "v_smfmac_f32_32x32x32_bf16 v[64:79], v[100:103], v[0:7], v120\n"
    "s_waitcnt lgkmcnt(1)\n"
    "v_exp_f32 v22, v22\n"
    "v_exp_f32 v23, v23\n"
    "v_exp_f32 v8, v8\n"
    "v_exp_f32 v9, v9\n"
    "v_smfmac_f32_32x32x32_bf16 v[80:95], v[100:103], v[16:23], v120\n"
    "ds_read_b128 v[100:103], v127 offset:34944\n"
    "v_exp_f32 v10, v10\n"
    "v_exp_f32 v11, v11\n"
    "v_exp_f32 v12, v12\n"
    "v_exp_f32 v13, v13\n"
    "v_mfma_f32_32x32x16_bf16 v[32:47], v[96:99], v[108:111], v[32:47]\n"
    "v_exp_f32 v14, v14\n"
    "v_exp_f32 v15, v15\n"
    "v_exp_f32 v24, v24\n"
    "v_exp_f32 v25, v25\n"
    "v_mfma_f32_32x32x16_bf16 v[48:63], v[96:99], v[112:115], v[48:63]\n"
    "ds_read_b128 v[96:99], v125 offset:37632\n"
    "v_exp_f32 v26, v26\n"
    "v_exp_f32 v27, v27\n"
    "v_exp_f32 v28, v28\n"
    "v_exp_f32 v29, v29\n"
    "v_smfmac_f32_32x32x32_bf16 v[64:79], v[104:107], v[8:15], v120\n"
    "s_waitcnt lgkmcnt(1)\n"
    "v_exp_f32 v30, v30\n"
    "v_exp_f32 v31, v31\n"
    "v_exp_f32 v32, v32\n"
    "v_exp_f32 v33, v33\n"
    "v_smfmac_f32_32x32x32_bf16 v[80:95], v[104:107], v[24:31], v120\n"
    "ds_read_b128 v[104:107], v128 offset:34944\n"
    "v_exp_f32 v34, v34\n"
    "v_exp_f32 v35, v35\n"
    "v_exp_f32 v36, v36\n"
    "v_exp_f32 v37, v37\n"
    "v_mfma_f32_32x32x4_2b_bf16 v[0:31], v[116:117], v[118:119], 0\n"
    "ds_read_b64 v[116:117], v126 offset:40320\n"
    "v_exp_f32 v38, v38\n"
    "v_exp_f32 v39, v39\n"
    "v_exp_f32 v48, v48\n"
    "v_exp_f32 v49, v49\n"
    "v_exp_f32 v50, v50\n"
    "v_exp_f32 v51, v51\n"
    "v_exp_f32 v52, v52\n"
    "v_exp_f32 v53, v53\n"
    "v_smfmac_f32_32x32x32_bf16 v[64:79], v[100:103], v[32:39], v120\n"
    "s_waitcnt lgkmcnt(1)\n"
    "v_exp_f32 v54, v54\n"
    "v_exp_f32 v55, v55\n"
    "v_exp_f32 v40, v40\n"
    "v_exp_f32 v41, v41\n"
    "v_smfmac_f32_32x32x32_bf16 v[80:95], v[100:103], v[48:55], v120\n"
    "ds_read_b128 v[100:103], v127 offset:37632\n"
    "v_exp_f32 v42, v42\n"
    "v_exp_f32 v43, v43\n"
    "v_exp_f32 v44, v44\n"
    "v_exp_f32 v45, v45\n"
    "v_mfma_f32_32x32x16_bf16 v[0:15], v[96:99], v[108:111], v[0:15]\n"
    "v_exp_f32 v46, v46\n"
    "v_exp_f32 v47, v47\n"
    "v_exp_f32 v56, v56\n"
    "v_exp_f32 v57, v57\n"
    "v_mfma_f32_32x32x16_bf16 v[16:31], v[96:99], v[112:115], v[16:31]\n"
    "ds_read_b128 v[96:99], v125 offset:40320\n"
    "v_exp_f32 v58, v58\n"
    "v_exp_f32 v59, v59\n"
    "v_exp_f32 v60, v60\n"
    "v_exp_f32 v61, v61\n"
    "v_smfmac_f32_32x32x32_bf16 v[64:79], v[104:107], v[40:47], v120\n"
    "s_waitcnt lgkmcnt(1)\n"
    "v_exp_f32 v62, v62\n"
    "v_exp_f32 v63, v63\n"
    "v_exp_f32 v0, v0\n"
    "v_exp_f32 v1, v1\n"
    "v_smfmac_f32_32x32x32_bf16 v[80:95], v[104:107], v[56:63], v120\n"
    "ds_read_b128 v[104:107], v128 offset:37632\n"
    "v_exp_f32 v2, v2\n"
    "v_exp_f32 v3, v3\n"
    "v_exp_f32 v4, v4\n"
    "v_exp_f32 v5, v5\n"
    "v_mfma_f32_32x32x4_2b_bf16 v[32:63], v[116:117], v[118:119], 0\n"
    "v_exp_f32 v6, v6\n"
    "v_exp_f32 v7, v7\n"
    "v_exp_f32 v16, v16\n"
    "v_exp_f32 v17, v17\n"
    "v_exp_f32 v18, v18\n"
    "v_exp_f32 v19, v19\n"
    "v_exp_f32 v20, v20\n"
    "v_exp_f32 v21, v21\n"
    "v_smfmac_f32_32x32x32_bf16 v[64:79], v[100:103], v[0:7], v120\n"
    "s_waitcnt lgkmcnt(0)\n"
    "v_exp_f32 v22, v22\n"
    "v_exp_f32 v23, v23\n"
    "v_exp_f32 v8, v8\n"
    "v_exp_f32 v9, v9\n"
    "v_smfmac_f32_32x32x32_bf16 v[80:95], v[100:103], v[16:23], v120\n"
    "ds_read_b128 v[100:103], v127 offset:40320\n"
    "v_exp_f32 v10, v10\n"
    "v_exp_f32 v11, v11\n"
    "v_exp_f32 v12, v12\n"
    "v_exp_f32 v13, v13\n"
    "v_mfma_f32_32x32x16_bf16 v[32:47], v[96:99], v[108:111], v[32:47]\n"
    "v_exp_f32 v14, v14\n"
    "v_exp_f32 v15, v15\n"
    "v_exp_f32 v24, v24\n"
    "v_exp_f32 v25, v25\n"
    "v_mfma_f32_32x32x16_bf16 v[48:63], v[96:99], v[112:115], v[48:63]\n"
    "v_exp_f32 v26, v26\n"
    "v_exp_f32 v27, v27\n"
    "v_exp_f32 v28, v28\n"
    "v_exp_f32 v29, v29\n"
    "v_smfmac_f32_32x32x32_bf16 v[64:79], v[104:107], v[8:15], v120\n"
    "s_waitcnt lgkmcnt(0)\n"
    "v_exp_f32 v30, v30\n"
    "v_exp_f32 v31, v31\n"
    "v_exp_f32 v32, v32\n"
    "v_exp_f32 v33, v33\n"
    "v_smfmac_f32_32x32x32_bf16 v[80:95], v[104:107], v[24:31], v120\n"
    "ds_read_b128 v[104:107], v128 offset:40320\n"
    "v_exp_f32 v34, v34\n"
    "v_exp_f32 v35, v35\n"
    "v_exp_f32 v36, v36\n"
    "v_exp_f32 v37, v37\n"
    "v_exp_f32 v38, v38\n"
    "v_exp_f32 v39, v39\n"
    "v_exp_f32 v40, v40\n"
    "v_exp_f32 v41, v41\n"
    "v_smfmac_f32_32x32x32_bf16 v[64:79], v[100:103], v[32:39], v120\n"
    "s_waitcnt lgkmcnt(0)\n"
    "v_exp_f32 v42, v42\n"
    "v_exp_f32 v43, v43\n"
    "v_exp_f32 v44, v44\n"
    "v_exp_f32 v45, v45\n"
    "v_exp_f32 v46, v46\n"
    "v_exp_f32 v47, v47\n"
    "v_exp_f32 v48, v48\n"
    "v_exp_f32 v49, v49\n"
    "v_smfmac_f32_32x32x32_bf16 v[64:79], v[104:107], v[40:47], v120\n"
    "v_exp_f32 v50, v50\n"
    "v_exp_f32 v51, v51\n"
    "v_exp_f32 v52, v52\n"
    "v_exp_f32 v53, v53\n"
    "v_exp_f32 v54, v54\n"
    "v_exp_f32 v55, v55\n"
    "v_exp_f32 v56, v56\n"
    "v_exp_f32 v57, v57\n"
    "v_smfmac_f32_32x32x32_bf16 v[80:95], v[100:103], v[48:55], v120\n"
    "v_exp_f32 v58, v58\n"
    "v_exp_f32 v59, v59\n"
    "v_exp_f32 v60, v60\n"
    "v_exp_f32 v61, v61\n"
    "v_exp_f32 v62, v62\n"
    "v_exp_f32 v63, v63\n"
    "s_add_u32 %[dl], %[tb], 40960\n"
    "s_nop 0\n"
    "v_smfmac_f32_32x32x32_bf16 v[80:95], v[104:107], v[56:63], v120\n"
    "v_rcp_f32 v12, v76\n"
    "s_nop 0\n"
    "v_pk_mul_f32 v[0:1], v[64:65], v[12:13] op_sel_hi:[1,0]\n"
    "v_pk_mul_f32 v[2:3], v[66:67], v[12:13] op_sel_hi:[1,0]\n"
    "v_pk_mul_f32 v[4:5], v[68:69], v[12:13] op_sel_hi:[1,0]\n"
    "v_pk_mul_f32 v[6:7], v[70:71], v[12:13] op_sel_hi:[1,0]\n"
    "v_cvt_pk_bf16_f32 v16, v0, v1\n"
    "v_cvt_pk_bf16_f32 v17, v2, v3\n"
    "v_cvt_pk_bf16_f32 v18, v4, v5\n"
    "v_cvt_pk_bf16_f32 v19, v6, v7\n"
    "buffer_store_dwordx4 v[16:19], v129, %[rs], %[tb] offen sc1\n"
    "s_mov_b32 exec_hi, 0\n"
    "v_pk_mul_f32 v[8:9], v[72:73], v[12:13] op_sel_hi:[1,0]\n"
    "v_pk_mul_f32 v[10:11], v[74:75], v[12:13] op_sel_hi:[1,0]\n"
    "v_cvt_pk_bf16_f32 v20, v8, v9\n"
    "v_cvt_pk_bf16_f32 v21, v10, v11\n"
    "buffer_store_dwordx2 v[20:21], v130, %[rs], %[tb] offen sc1\n"
    "s_mov_b32 exec_hi, -1\n"
    "v_rcp_f32 v12, v92\n"
    "s_nop 0\n"
    "v_pk_mul_f32 v[0:1], v[80:81], v[12:13] op_sel_hi:[1,0]\n"
    "v_pk_mul_f32 v[2:3], v[82:83], v[12:13] op_sel_hi:[1,0]\n"
    "v_pk_mul_f32 v[4:5], v[84:85], v[12:13] op_sel_hi:[1,0]\n"
    "v_pk_mul_f32 v[6:7], v[86:87], v[12:13] op_sel_hi:[1,0]\n"
    "v_cvt_pk_bf16_f32 v24, v0, v1\n"
    "v_cvt_pk_bf16_f32 v25, v2, v3\n"
    "v_cvt_pk_bf16_f32 v26, v4, v5\n"
    "v_cvt_pk_bf16_f32 v27, v6, v7\n"
    "buffer_store_dwordx4 v[24:27], v129, %[rs], %[dl] offen sc1\n"
    "s_mov_b32 exec_hi, 0\n"
    "v_pk_mul_f32 v[8:9], v[88:89], v[12:13] op_sel_hi:[1,0]\n"
    "v_pk_mul_f32 v[10:11], v[90:91], v[12:13] op_sel_hi:[1,0]\n"
    "v_cvt_pk_bf16_f32 v28, v8, v9\n"
    "v_cvt_pk_bf16_f32 v29, v10, v11\n"
    "buffer_store_dwordx2 v[28:29], v130, %[rs], %[dl] offen sc1\n"
    "s_mov_b32 exec_hi, -1\n"
    "s_waitcnt vmcnt(4)\n"
    "s_branch 9f\n"
    "8:\n"
    "16:\n"
    "ds_read_b32 v124, v121 offset:0\n"
    "s_waitcnt lgkmcnt(0)\n"
    "v_cmp_gt_u32 vcc, %[need], v124\n"
    "s_cbranch_vccnz 16b\n"
    "s_branch 7b\n"
    "18:\n"
    "16:\n"
    "ds_read_b32 v124, v121 offset:0\n"
    "s_waitcnt lgkmcnt(0)\n"
    "v_cmp_gt_u32 vcc, %[need], v124\n"
    "s_cbranch_vccnz 16b\n"
    "s_branch 17b\n"
    "28:\n"
    "16:\n"
    "ds_read_b32 v124, v121 offset:4\n"
    "s_waitcnt lgkmcnt(0)\n"
    "v_cmp_gt_u32 vcc, 2, v124\n"
    "s_cbranch_vccnz 16b\n"
    "s_branch 27b\n"
    "9:\n"
    : "={v[64:79]}"(o[0]), "={v[80:95]}"(o[1]), "={v[108:111]}"(q0[0]), "={v[112:115]}"(q0[1]), "={v[118:119]}"(qt),
      "={v[132:163]}"(st.a), "={v[164:195]}"(st.b), "={v[196:227]}"(st.c), "={v[228:243]}"(st.d), "={v[244:251]}"(st.e),
      "+{v[120:127]}"(v), "+{v[128:131]}"(w), [wd] "+s"(wd), [need] "=&s"(need), [dl] "=&s"(dl)
    : [fl] "s"(ws + WS_FL), [qp] "s"(ws + WS_Q + (head * 64 + qb * 8 + wv * 2) * QT), "{v[252:253]}"(p.s), [sm] "s"(p.sm), [rs] "s"(rawrsrc(ws, WS_END)), [tb] "s"(tb)
    : "vcc", "scc", "memory", "v0", "v1", "v2", "v3", "v4", "v5", "v6", "v7", "v8", "v9", "v10", "v11", "v12", "v13", "v14", "v15", "v16", "v17", "v18", "v19", "v20", "v21", "v22", "v23", "v24", "v25", "v26", "v27", "v28", "v29", "v30", "v31", "v32", "v33", "v34", "v35", "v36", "v37", "v38", "v39", "v40", "v41", "v42", "v43", "v44", "v45", "v46", "v47", "v48", "v49", "v50", "v51", "v52", "v53", "v54", "v55", "v56", "v57", "v58", "v59", "v60", "v61", "v62", "v63", "v96", "v97", "v98", "v99", "v100", "v101", "v102", "v103", "v104", "v105", "v106", "v107", "v116", "v117");
#pragma unroll
  for (int u = 0; u < 2; ++u) {
    float chk = 0.f;
#pragma unroll
    for (int i = 0; i < 12; ++i) chk = fmaf(o[u][i], 0.0f, chk);
    const float l = o[u][12];
    const bool ok = (l >= 5.421010862e-20f) && (l <= 1.8446744e19f) && (chk == 0.f);
    if (__builtin_expect(__builtin_amdgcn_ballot_w64(!ok) != 0, 0)) {
      const rsrc_t wr = mkrsrc(ws, WS_END);
      lane = lane_id();
      p2_addr(lane, head & 1, ka, oa);
      const s16x4 qu = __builtin_bit_cast(s16x4, (lane >> 5) == u ? qt : u32x2{0, 0});
      float m = -3.0e38f;
      f32x16 c = {};
      unsigned g = WS_KV + head * 64 * KVT;
#pragma unroll 1
      for (int t = 0; t < 64; ++t, g += KVT) {
        const bf16x8 k0 = __builtin_bit_cast(bf16x8, __builtin_amdgcn_raw_buffer_load_b128(wr, ka[0], g, 16));
        const s16x4 kt = __builtin_bit_cast(s16x4, __builtin_amdgcn_raw_buffer_load_b64(wr, ka[1], g, 16));
        const bf16x8 w0 = __builtin_bit_cast(bf16x8, __builtin_amdgcn_raw_buffer_load_b128(wr, ka[2], g, 16));
        const bf16x8 w1 = __builtin_bit_cast(bf16x8, __builtin_amdgcn_raw_buffer_load_b128(wr, ka[3], g, 16));
        f32x16 s = __builtin_amdgcn_mfma_f32_32x32x16_bf16(k0, q0[u], f32x16{}, 0, 0, 0);
        s = __builtin_amdgcn_mfma_f32_32x32x8bf16_1k(kt, qu, s, 0, 0, 0);
        float mn = m;
#pragma unroll
        for (int i = 0; i < 16; ++i) mn = fmaxf(mn, s[i]);
        mn = fmaxf(mn, __shfl_xor(mn, 32));
        const float alpha = __builtin_amdgcn_exp2f(m - mn);
        m = mn;
#pragma unroll
        for (int i = 0; i < 16; ++i) { s[i] = __builtin_amdgcn_exp2f(s[i] - mn); c[i] *= alpha; }
        c = __builtin_amdgcn_smfmac_f32_32x32x32_bf16(w0, phalf(s, 0), c, idx, 0, 0);
        c = __builtin_amdgcn_smfmac_f32_32x32x32_bf16(w1, phalf(s, 1), c, idx, 0, 0);
      }
      const float il = __builtin_amdgcn_rcpf(c[12]);
      WAITV(0);
      st16(wr, oa[0], tb + u * 40960, u32x4{pack2(c[0] * il, c[1] * il), pack2(c[2] * il, c[3] * il), pack2(c[4] * il, c[5] * il), pack2(c[6] * il, c[7] * il)});
      if (lane < 32) st8(wr, oa[1], tb + u * 40960, u32x2{pack2(c[8] * il, c[9] * il), pack2(c[10] * il, c[11] * il)});
    }
  }
}
DEV void p2_kvload(char* ws, int head, int qb, u32x8 v, int lane) {
  unsigned kv = WS_KV + head * 64 * KVT, i, nr, dst, n, t;
  asm volatile(
    "s_mov_b32 %[i], 0\n"
    "s_mov_b32 %[dst], 0\n"
    "v_mov_b32 v33, 0\n"
    "s_mov_b32 %[n], 200000\n"
    "s_mov_b64 exec, 0x1\n"
    "global_atomic_add v40, %[fo], v33, %[fl] sc0 sc1\n"
    "s_sleep 2\n"
    "global_atomic_add v41, %[fo], v33, %[fl] sc0 sc1\n"
    "s_sleep 2\n"
    "11:\n"
    "global_atomic_add v42, %[fo], v33, %[fl] sc0 sc1\n"
    "s_sleep 2\n"
    "s_waitcnt vmcnt(2)\n"
    "v_cmp_gt_u32 vcc, 16, v40\n"
    "s_bitcmp0_b32 vcc_lo, 0\n"
    "s_cbranch_scc1 12f\n"
    "global_atomic_add v40, %[fo], v33, %[fl] sc0 sc1\n"
    "s_sleep 2\n"
    "s_waitcnt vmcnt(2)\n"
    "v_cmp_gt_u32 vcc, 16, v41\n"
    "s_bitcmp0_b32 vcc_lo, 0\n"
    "s_cbranch_scc1 12f\n"
    "global_atomic_add v41, %[fo], v33, %[fl] sc0 sc1\n"
    "s_sleep 2\n"
    "s_waitcnt vmcnt(2)\n"
    "v_cmp_gt_u32 vcc, 16, v42\n"
    "s_bitcmp0_b32 vcc_lo, 0\n"
    "s_cbranch_scc1 12f\n"
    "s_sub_u32 %[n], %[n], 1\n"
    "s_cbranch_scc0 11b\n"
    "12:\n"
    "s_mov_b64 exec, -1\n"
    "s_or_b32 %[nr], vcc_lo, 0xfe\n"
    "1:\n"
    "v_mov_b32 v32, %[i]\n"
    "s_cmp_lg_u32 %[i], 2\n"
    "s_cbranch_scc1 2f\n"
    "ds_write_b32 v26, v26 offset:8\n"
    "2:\n"
    "s_or_b32 %[t], %[nr], 0xc0\n"
    "s_bitcmp0_b32 %[t], %[i]\n"
    "s_cbranch_scc1 6f\n"
    "s_waitcnt vmcnt(0)\n"
    "ds_write_b32 v26, v32\n"
    "s_bitcmp0_b32 %[nr], %[i]\n"
    "s_cbranch_scc1 4f\n"
    "s_mov_b64 exec, 0xff\n"
    "3:\n"
    "global_atomic_add v36, %[fo], v33, %[fl] sc0 sc1\n"
    "s_waitcnt vmcnt(0)\n"
    "v_cmp_gt_u32 vcc, 16, v36\n"
    "s_sub_u32 %[n], %[n], 1\n"
    "s_cbranch_scc1 7f\n"
    "s_bitcmp1_b32 vcc_lo, %[i]\n"
    "s_cbranch_scc1 3b\n"
    "7:\n"
    "s_mov_b32 %[nr], vcc_lo\n"
    "s_mov_b64 exec, -1\n"
    "4:\n"
    "s_cmp_lt_u32 %[i], 6\n"
    "s_cbranch_scc1 6f\n"
    "s_sub_u32 %[t], %[i], 5\n"
    "s_branch 8f\n"
    "5:\n"
    "s_sleep 16\n"
    "8:\n"
    "ds_read_b128 v[36:39], v26 offset:16\n"
    "s_waitcnt lgkmcnt(0)\n"
    "v_min3_u32 v36, v36, v37, v38\n"
    "v_min_u32 v36, v36, v39\n"
    "v_cmp_gt_u32 vcc, %[t], v36\n"
    "s_cbranch_vccnz 5b\n"
    "6:\n"
    "s_add_u32 m0, %[dst], 0\n"
    "s_nop 0\n"
    "buffer_load_dwordx4 v24, %[rs], %[kv] offen offset:0 sc1 lds\n"
    "buffer_load_dwordx4 v24, %[rs], %[kv] offen offset:1024 sc1 lds\n"
    "buffer_load_dwordx4 v24, %[rs], %[kv] offen offset:2048 sc1 lds\n"
    "buffer_load_dwordx4 v24, %[rs], %[kv] offen offset:3072 sc1 lds\n"
    "s_add_u32 m0, %[dst], 4096\n"
    "s_nop 0\n"
    "buffer_load_dwordx4 v25, %[rs], %[kv] offen offset:0 sc1 lds\n"
    "buffer_load_dwordx4 v25, %[rs], %[kv] offen offset:1024 sc1 lds\n"
    "buffer_load_dwordx4 v25, %[rs], %[kv] offen offset:2048 sc1 lds\n"
    "buffer_load_dwordx4 v25, %[rs], %[kv] offen offset:3072 sc1 lds\n"
    "s_add_u32 m0, %[dst], 8192\n"
    "s_add_u32 %[kv], %[kv], 8192\n"
    "buffer_load_dwordx4 v24, %[rs], %[kv] offen offset:0 sc1 lds\n"
    "buffer_load_dwordx4 v24, %[rs], %[kv] offen offset:1024 sc1 lds\n"
    "buffer_load_dwordx4 v24, %[rs], %[kv] offen offset:2048 sc1 lds\n"
    "buffer_load_dwordx4 v24, %[rs], %[kv] offen offset:3072 sc1 lds\n"
    "s_add_u32 m0, %[dst], 12288\n"
    "s_nop 0\n"
    "buffer_load_dwordx4 v25, %[rs], %[kv] offen offset:0 sc1 lds\n"
    "buffer_load_dwordx4 v25, %[rs], %[kv] offen offset:1024 sc1 lds\n"
    "buffer_load_dwordx4 v25, %[rs], %[kv] offen offset:2048 sc1 lds\n"
    "buffer_load_dwordx4 v25, %[rs], %[kv] offen offset:3072 sc1 lds\n"
    "s_add_u32 m0, %[dst], 16384\n"
    "s_add_u32 %[kv], %[kv], 8192\n"
    "buffer_load_dwordx4 v24, %[rs], %[kv] offen offset:0 sc1 lds\n"
    "buffer_load_dwordx4 v24, %[rs], %[kv] offen offset:1024 sc1 lds\n"
    "buffer_load_dwordx4 v24, %[rs], %[kv] offen offset:2048 sc1 lds\n"
    "buffer_load_dwordx4 v24, %[rs], %[kv] offen offset:3072 sc1 lds\n"
    "s_add_u32 m0, %[dst], 20480\n"
    "s_nop 0\n"
    "buffer_load_dwordx4 v25, %[rs], %[kv] offen offset:0 sc1 lds\n"
    "s_add_u32 %[kv], %[kv], 5120\n"
    "s_add_u32 %[dst], %[dst], 21504\n"
    "s_cmp_eq_u32 %[i], 5\n"
    "s_cmov_b32 %[dst], 0\n"
    "s_add_u32 %[i], %[i], 1\n"
    "s_waitcnt vmcnt(21)\n"
    "ds_write_b32 v26, v32\n"
    "s_cmp_lt_u32 %[i], 8\n"
    "s_cbranch_scc1 1b\n"
    "v_mov_b32 v32, 9\n"
    "s_waitcnt vmcnt(0)\n"
    "ds_write_b32 v26, v32\n"
    : [kv] "+s"(kv), [i] "=&s"(i), [nr] "=&s"(nr), [dst] "=&s"(dst), [n] "=&s"(n), [t] "=&s"(t)
    : [rs] "s"(rawrsrc(ws, WS_END)), [fl] "s"(ws + WS_FL), "{v[24:31]}"(v), [fo] "{v34}"((C_F1 + (qb & 1) * 256 + head * 8 + (lane & 7)) * 256)
    : "vcc", "scc", "memory", "v32", "v33", "v36", "v37", "v38", "v39", "v40", "v41", "v42");
}
DEV int fb_t0(int fb) { return 108 * (fb >> 3) + ((fb & 4) ? 56 + 13 * (fb & 3) : 14 * (fb & 3)); }
#define WAIT_KV() while (lds_flag(L_FLG + 8) == 0) __builtin_amdgcn_s_sleep(4)
template <int N>
DEV void retile_rows(const char* src, char* ws, unsigned dst, unsigned tl, unsigned cnt, int lane) {
  const rsrc_t wr = mkrsrc(ws, WS_END);
  const int q = (lane & 15) >> 2, pp = lane & 3, g = lane >> 4;
  unsigned vo[5];
#pragma unroll
  for (int i = 0; i < 5; ++i) { int c = lane + 64 * i, r = (c * 205) >> 11; vo[i] = r * 1280 + (c - 10 * r) * 16; }
  const unsigned a = tl + (N == 2 ? 8 * (q >> 1) + 2 * g + (q & 1) : 8 * g + q) * 160, d = N == 2 ? 2560 : 640;
  unsigned v16 = lane * 16, one = 1, ca = cnt * 256, a0 = a + pp * 16, a1 = a + (pp & 1) * 16 + (pp >> 1) * 8;
  asm volatile("" : "+v"(v16), "+v"(one), "+v"(ca), "+v"(a0), "+v"(a1), "+v"(vo[0]), "+v"(vo[1]), "+v"(vo[2]), "+v"(vo[3]), "+v"(vo[4]));
  asm volatile("" : "+s"(dst), "+s"(src));
  WAIT_KV();
#pragma unroll
  for (int i = 0; i < N * 5; ++i) dma1(vo[i % 5], src + i / 5 * 32 * 1280, tl + i * 1024);
  WAITV(0);
#pragma unroll
  for (int i = 0; i < N * 5; ++i) {
    const int T = i % 5;
    const unsigned c = i / 5 * 5120 + (T < 4 ? a0 + (T >> 1) * 64 + (T & 1) * 8 : a1 + 128);
    st16(wr, v16 + (i & 3) * 1024, dst + (i >> 2) * 4096, __builtin_bit_cast(u32x4, cat(lds_tr(c), lds_tr(c + d))));
  }
  asm volatile("s_waitcnt vmcnt(0)\n\ts_mov_b64 exec, 1\n\tglobal_atomic_add %0, %1, %2 sc1\n\ts_mov_b64 exec, -1" ::"v"(ca), "v"(one), "s"(ws + WS_FL) : "memory");
}
DEV void retile_gu(const char* wg, const char* wu, char* ws, int u, int lane) {
  const int kq = (u * 1214) >> 16, quad = u - 54 * kq;
  const rsrc_t wr = mkrsrc(ws, WS_END);
  const int i = lane & 15, g = lane >> 4, rho = 4 * ((i & 7) >> 1) + 2 * (i >> 3) + (i & 1);
  unsigned dst[4], str[4];
  int fbs[4];
#pragma unroll
  for (int t = 0; t < 4; ++t) {
    const int T = quad * 4 + t, hi = T >= 108, v = T - 108 * hi, fb = 8 * hi + (v < 56 ? (v * 147) >> 11 : 4 + (((v - 56) * 158) >> 11)), t0 = fb_t0(fb);
    fbs[t] = fb; str[t] = ((fb & 4) ? 13 : 14) * 2048; dst[t] = WS_WGU + t0 * 20480 + (T - t0) * 2048 + ((kq * 5) >> 1) * str[t] + (kq & 1) * 1024;
  }
  u32x4 sel[4];
#pragma unroll
  for (int t = 0; t < 4; ++t) { const unsigned o = i == 4 * t ? 0x3f803f80u : 0u; sel[t] = u32x4{o, o, o, o}; }
  f32x4 cs = {};
  wg += kq * 160 * (F_ * 2) + quad * 64;
  wu += kq * 160 * (F_ * 2) + quad * 64;
  unsigned a = L_STG + ((i & 3) >> 1) * 2048 + (8 * g + (i >> 2)) * 64 + (i & 1) * 8, vg = (lane >> 2) * (F_ * 2) + (lane & 3) * 16;
  unsigned va = g * 256 + rho * 16, vc = g * 256 + rho * 4, one = 1, ca = (C_WG + (g == 0 ? fbs[0] : g == 1 ? fbs[1] : g == 2 ? fbs[2] : fbs[3])) * 256, sc = WS_CS + quad * 1024 + kq * 64;
  asm volatile("" : "+v"(a), "+v"(vg), "+v"(va), "+v"(vc), "+v"(one), "+v"(ca), "+v"(cs), "+v"(sel[0]), "+v"(sel[1]), "+v"(sel[2]), "+v"(sel[3]));
  asm volatile("" : "+s"(dst[0]), "+s"(dst[1]), "+s"(dst[2]), "+s"(dst[3]), "+s"(str[0]), "+s"(str[1]), "+s"(str[2]), "+s"(str[3]), "+s"(sc), "+s"(wg), "+s"(wu));
  WAIT_KV();
#pragma unroll
  for (int n = 0; n < 20; ++n) dma1(vg, ((n & 2) ? wu : wg) + ((n >> 2) * 32 + (n & 1) * 16) * (F_ * 2), L_STG + n * 1024);
  WAITV(0);
  bf16x8 f[20];
#pragma unroll
  for (int n = 0; n < 20; ++n) {
    const unsigned c = a + (n >> 2) * 4096 + (n & 3) * 16;
    f[n] = cat(lds_tr(c), lds_tr(c + 256));
    st16(wr, va, dst[n & 3], __builtin_bit_cast(u32x4, f[n]));
    dst[n & 3] += ((kq + (n >> 2)) & 1) ? str[n & 3] - 1024 : 1024;
  }
  asm volatile("" : "+v"(cs) :: "memory");
#pragma unroll
  for (int n = 0; n < 20; ++n) cs = __builtin_amdgcn_mfma_f32_16x16x32_bf16(__builtin_bit_cast(bf16x8, sel[n & 3]), f[n], cs, 0, 0, 0);
  st4(wr, vc, sc, __builtin_bit_cast(unsigned, cs[0]));
  asm volatile("s_waitcnt vmcnt(0)\n\ts_mov_b32 exec_lo, 0x10001\n\ts_mov_b32 exec_hi, 0x10001\n\tglobal_atomic_add %0, %1, %2 sc1\n\ts_mov_b64 exec, -1" ::"v"(ca), "v"(one), "s"(ws + WS_FL) : "memory");
}

DEV unsigned l_box(int i) { return (i & 1) * 15360 + (i >> 1) * (6 * L_OCT); }
DEV unsigned a_base(int tb, int fb, int w) { return WS_A + ((tb * 2 + (w >> 1)) * 216 + fb_t0(fb) + (w & 1) * ((fb & 4) ? 6 : 7)) * 1024; }
DEV void p4_compute(char* ws, int tb, int fb, int wv, int lane) {
  const int wm = wv >> 1, wn = wv & 1, r = lane & 15, g = lane >> 4, q = fb >> 2, nt = (fb & 4) ? 13 : 14, tw = wn * (nt - 7);
  u32x16 v;
#pragma unroll
  for (int m = 0; m < 4; ++m) {
    v[m] = l_box(2 * wm + ((m ^ q) >> 1)) + ((m ^ q) & 1) * 2048 + lane * 16;
    v[8 + 2 * m] = v[9 + 2 * m] = L_MISC + (wm * 64 + ((m ^ q) << 4) + r) * 8;
  }
  v[4] = L_WR + tw * 2048 + lane * 16;
  v[5] = lane * 16;
  v[6] = L_MISC + 1024 + tw * 64 + g * 16;
  v[7] = (C_F4 + lane * 128 + (tb * 2 + wm) * 4 + q) * 256;
  unsigned ab = a_base(tb, fb, wv);
  int cnt, dh, dw;
  asm volatile(
    "ds_read_b128 v[232:235], v14 offset:0\n"
    "ds_read_b128 v[236:239], v14 offset:64\n"
    "ds_read_b128 v[240:243], v14 offset:128\n"
    "ds_read_b128 v[244:247], v14 offset:192\n"
    "ds_read_b128 v[248:251], v14 offset:256\n"
    "ds_read_b128 v[252:255], v14 offset:320\n"
    "ds_read_b128 v[24:27], v14 offset:384\n"
    "v_mov_b32 v32, 0\n"
    "v_mov_b32 v33, 0\n"
    "v_mov_b32 v34, 0\n"
    "v_mov_b32 v35, 0\n"
    "v_mov_b32 v36, 0\n"
    "v_mov_b32 v37, 0\n"
    "v_mov_b32 v38, 0\n"
    "v_mov_b32 v39, 0\n"
    "v_mov_b32 v40, 0\n"
    "v_mov_b32 v41, 0\n"
    "v_mov_b32 v42, 0\n"
    "v_mov_b32 v43, 0\n"
    "v_mov_b32 v44, 0\n"
    "v_mov_b32 v45, 0\n"
    "v_mov_b32 v46, 0\n"
    "v_mov_b32 v47, 0\n"
    "v_mov_b32 v48, 0\n"
    "v_mov_b32 v49, 0\n"
    "v_mov_b32 v50, 0\n"
    "v_mov_b32 v51, 0\n"
    "v_mov_b32 v52, 0\n"
    "v_mov_b32 v53, 0\n"
    "v_mov_b32 v54, 0\n"
    "v_mov_b32 v55, 0\n"
    "v_mov_b32 v56, 0\n"
    "v_mov_b32 v57, 0\n"
    "v_mov_b32 v58, 0\n"
    "v_mov_b32 v59, 0\n"
    "v_mov_b32 v60, 0\n"
    "v_mov_b32 v61, 0\n"
    "v_mov_b32 v62, 0\n"
    "v_mov_b32 v63, 0\n"
    "v_mov_b32 v64, 0\n"
    "v_mov_b32 v65, 0\n"
    "v_mov_b32 v66, 0\n"
    "v_mov_b32 v67, 0\n"
    "v_mov_b32 v68, 0\n"
    "v_mov_b32 v69, 0\n"
    "v_mov_b32 v70, 0\n"
    "v_mov_b32 v71, 0\n"
    "v_mov_b32 v72, 0\n"
    "v_mov_b32 v73, 0\n"
    "v_mov_b32 v74, 0\n"
    "v_mov_b32 v75, 0\n"
    "v_mov_b32 v76, 0\n"
    "v_mov_b32 v77, 0\n"
    "v_mov_b32 v78, 0\n"
    "v_mov_b32 v79, 0\n"
    "v_mov_b32 v80, 0\n"
    "v_mov_b32 v81, 0\n"
    "v_mov_b32 v82, 0\n"
    "v_mov_b32 v83, 0\n"
    "v_mov_b32 v84, 0\n"
    "v_mov_b32 v85, 0\n"
    "v_mov_b32 v86, 0\n"
    "v_mov_b32 v87, 0\n"
    "v_mov_b32 v88, 0\n"
    "v_mov_b32 v89, 0\n"
    "v_mov_b32 v90, 0\n"
    "v_mov_b32 v91, 0\n"
    "v_mov_b32 v92, 0\n"
    "v_mov_b32 v93, 0\n"
    "v_mov_b32 v94, 0\n"
    "v_mov_b32 v95, 0\n"
    "v_mov_b32 v96, 0\n"
    "v_mov_b32 v97, 0\n"
    "v_mov_b32 v98, 0\n"
    "v_mov_b32 v99, 0\n"
    "v_mov_b32 v100, 0\n"
    "v_mov_b32 v101, 0\n"
    "v_mov_b32 v102, 0\n"
    "v_mov_b32 v103, 0\n"
    "v_mov_b32 v104, 0\n"
    "v_mov_b32 v105, 0\n"
    "v_mov_b32 v106, 0\n"
    "v_mov_b32 v107, 0\n"
    "v_mov_b32 v108, 0\n"
    "v_mov_b32 v109, 0\n"
    "v_mov_b32 v110, 0\n"
    "v_mov_b32 v111, 0\n"
    "v_mov_b32 v112, 0\n"
    "v_mov_b32 v113, 0\n"
    "v_mov_b32 v114, 0\n"
    "v_mov_b32 v115, 0\n"
    "v_mov_b32 v116, 0\n"
    "v_mov_b32 v117, 0\n"
    "v_mov_b32 v118, 0\n"
    "v_mov_b32 v119, 0\n"
    "v_mov_b32 v120, 0\n"
    "v_mov_b32 v121, 0\n"
    "v_mov_b32 v122, 0\n"
    "v_mov_b32 v123, 0\n"
    "v_mov_b32 v124, 0\n"
    "v_mov_b32 v125, 0\n"
    "v_mov_b32 v126, 0\n"
    "v_mov_b32 v127, 0\n"
    "v_mov_b32 v128, 0\n"
    "v_mov_b32 v129, 0\n"
    "v_mov_b32 v130, 0\n"
    "v_mov_b32 v131, 0\n"
    "v_mov_b32 v132, 0\n"
    "v_mov_b32 v133, 0\n"
    "v_mov_b32 v134, 0\n"
    "v_mov_b32 v135, 0\n"
    "v_mov_b32 v136, 0\n"
    "v_mov_b32 v137, 0\n"
    "v_mov_b32 v138, 0\n"
    "v_mov_b32 v139, 0\n"
    "v_mov_b32 v140, 0\n"
    "v_mov_b32 v141, 0\n"
    "v_mov_b32 v142, 0\n"
    "v_mov_b32 v143, 0\n"
    "v_mov_b32 v0, 0\n"
    "v_mov_b32 v1, 0\n"
    "v_mov_b32 v2, 0\n"
    "v_mov_b32 v3, 0\n"
    "v_mov_b32 v4, 0\n"
    "v_mov_b32 v5, 0\n"
    "v_mov_b32 v6, 0\n"
    "v_mov_b32 v7, 0\n"
    "s_mov_b32 %[cnt], 0\n"
    "s_barrier\n"
    "ds_read_b128 v[208:211], v12 offset:0\n"
    "ds_read_b128 v[212:215], v12 offset:1024\n"
    "ds_read_b128 v[144:147], v8 offset:0\n"
    "ds_read_b128 v[148:151], v8 offset:1024\n"
    "ds_read_b128 v[152:155], v9 offset:0\n"
    "ds_read_b128 v[156:159], v9 offset:1024\n"
    "ds_read_b128 v[160:163], v10 offset:0\n"
    "ds_read_b128 v[164:167], v10 offset:1024\n"
    "ds_read_b128 v[168:171], v11 offset:0\n"
    "ds_read_b128 v[172:175], v11 offset:1024\n"
    "ds_read_b128 v[216:219], v12 offset:2048\n"
    "ds_read_b128 v[220:223], v12 offset:3072\n"
    "ds_read_b128 v[224:227], v12 offset:4096\n"
    "ds_read_b128 v[228:231], v12 offset:5120\n"
    "s_branch 2f\n"
    ".p2align 5\n"
    "1:\n"
    "s_waitcnt lgkmcnt(0)\n"
    "s_barrier\n"
    "v_mfma_f32_16x16x32_bf16 v[96:99], v[216:219], v[176:179], v[96:99]\n"
    "ds_read_b128 v[208:211], v12 offset:0\n"
    "v_mfma_f32_16x16x32_bf16 v[96:99], v[220:223], v[180:183], v[96:99]\n"
    "ds_read_b128 v[212:215], v12 offset:1024\n"
    "v_mfma_f32_16x16x32_bf16 v[100:103], v[216:219], v[184:187], v[100:103]\n"
    "v_mfma_f32_16x16x32_bf16 v[100:103], v[220:223], v[188:191], v[100:103]\n"
    "ds_read_b128 v[144:147], v8 offset:0\n"
    "v_mfma_f32_16x16x32_bf16 v[104:107], v[216:219], v[192:195], v[104:107]\n"
    "ds_read_b128 v[148:151], v8 offset:1024\n"
    "v_mfma_f32_16x16x32_bf16 v[104:107], v[220:223], v[196:199], v[104:107]\n"
    "v_mfma_f32_16x16x32_bf16 v[108:111], v[216:219], v[200:203], v[108:111]\n"
    "ds_read_b128 v[152:155], v9 offset:0\n"
    "v_mfma_f32_16x16x32_bf16 v[108:111], v[220:223], v[204:207], v[108:111]\n"
    "ds_read_b128 v[156:159], v9 offset:1024\n"
    "v_mfma_f32_16x16x32_bf16 v[112:115], v[224:227], v[176:179], v[112:115]\n"
    "v_mfma_f32_16x16x32_bf16 v[112:115], v[228:231], v[180:183], v[112:115]\n"
    "ds_read_b128 v[160:163], v10 offset:0\n"
    "v_mfma_f32_16x16x32_bf16 v[116:119], v[224:227], v[184:187], v[116:119]\n"
    "ds_read_b128 v[164:167], v10 offset:1024\n"
    "v_mfma_f32_16x16x32_bf16 v[116:119], v[228:231], v[188:191], v[116:119]\n"
    "v_mfma_f32_16x16x32_bf16 v[120:123], v[224:227], v[192:195], v[120:123]\n"
    "ds_read_b128 v[168:171], v11 offset:0\n"
    "v_mfma_f32_16x16x32_bf16 v[120:123], v[228:231], v[196:199], v[120:123]\n"
    "ds_read_b128 v[172:175], v11 offset:1024\n"
    "v_mfma_f32_16x16x32_bf16 v[124:127], v[224:227], v[200:203], v[124:127]\n"
    "v_mfma_f32_16x16x32_bf16 v[124:127], v[228:231], v[204:207], v[124:127]\n"
    "ds_read_b128 v[216:219], v12 offset:2048\n"
    "v_mfma_f32_16x16x32_bf16 v[128:131], v[0:3], v[176:179], v[128:131]\n"
    "ds_read_b128 v[220:223], v12 offset:3072\n"
    "v_mfma_f32_16x16x32_bf16 v[128:131], v[4:7], v[180:183], v[128:131]\n"
    "v_mfma_f32_16x16x32_bf16 v[132:135], v[0:3], v[184:187], v[132:135]\n"
    "ds_read_b128 v[224:227], v12 offset:4096\n"
    "v_mfma_f32_16x16x32_bf16 v[132:135], v[4:7], v[188:191], v[132:135]\n"
    "ds_read_b128 v[228:231], v12 offset:5120\n"
    "v_mfma_f32_16x16x32_bf16 v[136:139], v[0:3], v[192:195], v[136:139]\n"
    "v_mfma_f32_16x16x32_bf16 v[136:139], v[4:7], v[196:199], v[136:139]\n"
    "v_mfma_f32_16x16x32_bf16 v[140:143], v[0:3], v[200:203], v[140:143]\n"
    "v_mfma_f32_16x16x32_bf16 v[140:143], v[4:7], v[204:207], v[140:143]\n"
    "2:\n"
    "s_waitcnt lgkmcnt(8)\n"
    "v_mfma_f32_16x16x32_bf16 v[32:35], v[208:211], v[144:147], v[32:35]\n"
    "s_add_u32 %[cnt], %[cnt], 1\n"
    "v_mfma_f32_16x16x32_bf16 v[32:35], v[212:215], v[148:151], v[32:35]\n"
    "s_mov_b64 exec, %[em]\n"
    "ds_read_b128 v[0:3], v12 offset:12288\n"
    "s_mov_b64 exec, -1\n"
    "v_mfma_f32_16x16x32_bf16 v[36:39], v[208:211], v[152:155], v[36:39]\n"
    "s_movk_i32 %[dh], 4096\n"
    "v_mfma_f32_16x16x32_bf16 v[36:39], v[212:215], v[156:159], v[36:39]\n"
    "s_mov_b64 exec, %[em]\n"
    "ds_read_b128 v[4:7], v12 offset:13312\n"
    "s_mov_b64 exec, -1\n"
    "s_waitcnt lgkmcnt(6)\n"
    "v_mfma_f32_16x16x32_bf16 v[40:43], v[208:211], v[160:163], v[40:43]\n"
    "s_bitcmp1_b32 0x249, %[cnt]\n"
    "v_mfma_f32_16x16x32_bf16 v[40:43], v[212:215], v[164:167], v[40:43]\n"
    "v_mfma_f32_16x16x32_bf16 v[44:47], v[208:211], v[168:171], v[44:47]\n"
    "s_cmov_b32 %[dh], -8192\n"
    "v_mfma_f32_16x16x32_bf16 v[44:47], v[212:215], v[172:175], v[44:47]\n"
    "ds_read_b128 v[208:211], v12 offset:6144\n"
    "s_waitcnt lgkmcnt(5)\n"
    "v_mfma_f32_16x16x32_bf16 v[48:51], v[216:219], v[144:147], v[48:51]\n"
    "s_mul_i32 %[dw], %[dh], 7\n"
    "v_mfma_f32_16x16x32_bf16 v[48:51], v[220:223], v[148:151], v[48:51]\n"
    "ds_read_b128 v[212:215], v12 offset:7168\n"
    "v_mfma_f32_16x16x32_bf16 v[52:55], v[216:219], v[152:155], v[52:55]\n"
    "s_cmp_lt_u32 %[cnt], 8\n"
    "v_mfma_f32_16x16x32_bf16 v[52:55], v[220:223], v[156:159], v[52:55]\n"
    "v_add_u32 v8, %[dh], v8\n"
    "v_mfma_f32_16x16x32_bf16 v[56:59], v[216:219], v[160:163], v[56:59]\n"
    "v_add_u32 v9, %[dh], v9\n"
    "v_mfma_f32_16x16x32_bf16 v[56:59], v[220:223], v[164:167], v[56:59]\n"
    "v_add_u32 v10, %[dh], v10\n"
    "v_mfma_f32_16x16x32_bf16 v[60:63], v[216:219], v[168:171], v[60:63]\n"
    "v_mfma_f32_16x16x32_bf16 v[60:63], v[220:223], v[172:175], v[60:63]\n"
    "ds_read_b128 v[216:219], v12 offset:8192\n"
    "s_waitcnt lgkmcnt(5)\n"
    "v_mfma_f32_16x16x32_bf16 v[64:67], v[224:227], v[144:147], v[64:67]\n"
    "v_mfma_f32_16x16x32_bf16 v[64:67], v[228:231], v[148:151], v[64:67]\n"
    "ds_read_b128 v[220:223], v12 offset:9216\n"
    "v_mfma_f32_16x16x32_bf16 v[68:71], v[224:227], v[152:155], v[68:71]\n"
    "v_mfma_f32_16x16x32_bf16 v[68:71], v[228:231], v[156:159], v[68:71]\n"
    "v_add_u32 v11, %[dh], v11\n"
    "v_mfma_f32_16x16x32_bf16 v[72:75], v[224:227], v[160:163], v[72:75]\n"
    "v_mfma_f32_16x16x32_bf16 v[72:75], v[228:231], v[164:167], v[72:75]\n"
    "v_mfma_f32_16x16x32_bf16 v[76:79], v[224:227], v[168:171], v[76:79]\n"
    "v_mfma_f32_16x16x32_bf16 v[76:79], v[228:231], v[172:175], v[76:79]\n"
    "ds_read_b128 v[224:227], v12 offset:10240\n"
    "s_waitcnt lgkmcnt(3)\n"
    "v_mfma_f32_16x16x32_bf16 v[80:83], v[208:211], v[144:147], v[80:83]\n"
    "ds_read_b128 v[228:231], v12 offset:11264\n"
    "v_mfma_f32_16x16x32_bf16 v[80:83], v[212:215], v[148:151], v[80:83]\n"
    "v_mfma_f32_16x16x32_bf16 v[84:87], v[208:211], v[152:155], v[84:87]\n"
    "v_add_u32 v12, %[dw], v12\n"
    "v_mfma_f32_16x16x32_bf16 v[84:87], v[212:215], v[156:159], v[84:87]\n"
    "v_mfma_f32_16x16x32_bf16 v[88:91], v[208:211], v[160:163], v[88:91]\n"
    "v_mfma_f32_16x16x32_bf16 v[88:91], v[212:215], v[164:167], v[88:91]\n"
    "v_mfma_f32_16x16x32_bf16 v[92:95], v[208:211], v[168:171], v[92:95]\n"
    "v_mfma_f32_16x16x32_bf16 v[92:95], v[212:215], v[172:175], v[92:95]\n"
    "s_waitcnt lgkmcnt(0)\n"
    "s_barrier\n"
    "v_mfma_f32_16x16x32_bf16 v[96:99], v[216:219], v[144:147], v[96:99]\n"
    "ds_read_b128 v[208:211], v12 offset:0\n"
    "v_mfma_f32_16x16x32_bf16 v[96:99], v[220:223], v[148:151], v[96:99]\n"
    "ds_read_b128 v[212:215], v12 offset:1024\n"
    "v_mfma_f32_16x16x32_bf16 v[100:103], v[216:219], v[152:155], v[100:103]\n"
    "v_mfma_f32_16x16x32_bf16 v[100:103], v[220:223], v[156:159], v[100:103]\n"
    "ds_read_b128 v[176:179], v8 offset:0\n"
    "v_mfma_f32_16x16x32_bf16 v[104:107], v[216:219], v[160:163], v[104:107]\n"
    "ds_read_b128 v[180:183], v8 offset:1024\n"
    "v_mfma_f32_16x16x32_bf16 v[104:107], v[220:223], v[164:167], v[104:107]\n"
    "v_mfma_f32_16x16x32_bf16 v[108:111], v[216:219], v[168:171], v[108:111]\n"
    "ds_read_b128 v[184:187], v9 offset:0\n"
    "v_mfma_f32_16x16x32_bf16 v[108:111], v[220:223], v[172:175], v[108:111]\n"
    "ds_read_b128 v[188:191], v9 offset:1024\n"
    "v_mfma_f32_16x16x32_bf16 v[112:115], v[224:227], v[144:147], v[112:115]\n"
    "v_mfma_f32_16x16x32_bf16 v[112:115], v[228:231], v[148:151], v[112:115]\n"
    "ds_read_b128 v[192:195], v10 offset:0\n"
    "v_mfma_f32_16x16x32_bf16 v[116:119], v[224:227], v[152:155], v[116:119]\n"
    "ds_read_b128 v[196:199], v10 offset:1024\n"
    "v_mfma_f32_16x16x32_bf16 v[116:119], v[228:231], v[156:159], v[116:119]\n"
    "v_mfma_f32_16x16x32_bf16 v[120:123], v[224:227], v[160:163], v[120:123]\n"
    "ds_read_b128 v[200:203], v11 offset:0\n"
    "v_mfma_f32_16x16x32_bf16 v[120:123], v[228:231], v[164:167], v[120:123]\n"
    "ds_read_b128 v[204:207], v11 offset:1024\n"
    "v_mfma_f32_16x16x32_bf16 v[124:127], v[224:227], v[168:171], v[124:127]\n"
    "v_mfma_f32_16x16x32_bf16 v[124:127], v[228:231], v[172:175], v[124:127]\n"
    "ds_read_b128 v[216:219], v12 offset:2048\n"
    "v_mfma_f32_16x16x32_bf16 v[128:131], v[0:3], v[144:147], v[128:131]\n"
    "ds_read_b128 v[220:223], v12 offset:3072\n"
    "v_mfma_f32_16x16x32_bf16 v[128:131], v[4:7], v[148:151], v[128:131]\n"
    "v_mfma_f32_16x16x32_bf16 v[132:135], v[0:3], v[152:155], v[132:135]\n"
    "ds_read_b128 v[224:227], v12 offset:4096\n"
    "v_mfma_f32_16x16x32_bf16 v[132:135], v[4:7], v[156:159], v[132:135]\n"
    "ds_read_b128 v[228:231], v12 offset:5120\n"
    "v_mfma_f32_16x16x32_bf16 v[136:139], v[0:3], v[160:163], v[136:139]\n"
    "v_mfma_f32_16x16x32_bf16 v[136:139], v[4:7], v[164:167], v[136:139]\n"
    "v_mfma_f32_16x16x32_bf16 v[140:143], v[0:3], v[168:171], v[140:143]\n"
    "v_mfma_f32_16x16x32_bf16 v[140:143], v[4:7], v[172:175], v[140:143]\n"
    "s_waitcnt lgkmcnt(8)\n"
    "v_mfma_f32_16x16x32_bf16 v[32:35], v[208:211], v[176:179], v[32:35]\n"
    "s_add_u32 %[cnt], %[cnt], 1\n"
    "v_mfma_f32_16x16x32_bf16 v[32:35], v[212:215], v[180:183], v[32:35]\n"
    "s_mov_b64 exec, %[em]\n"
    "ds_read_b128 v[0:3], v12 offset:12288\n"
    "s_mov_b64 exec, -1\n"
    "v_mfma_f32_16x16x32_bf16 v[36:39], v[208:211], v[184:187], v[36:39]\n"
    "s_movk_i32 %[dh], 4096\n"
    "v_mfma_f32_16x16x32_bf16 v[36:39], v[212:215], v[188:191], v[36:39]\n"
    "s_mov_b64 exec, %[em]\n"
    "ds_read_b128 v[4:7], v12 offset:13312\n"
    "s_mov_b64 exec, -1\n"
    "s_waitcnt lgkmcnt(6)\n"
    "v_mfma_f32_16x16x32_bf16 v[40:43], v[208:211], v[192:195], v[40:43]\n"
    "s_bitcmp1_b32 0x249, %[cnt]\n"
    "v_mfma_f32_16x16x32_bf16 v[40:43], v[212:215], v[196:199], v[40:43]\n"
    "v_mfma_f32_16x16x32_bf16 v[44:47], v[208:211], v[200:203], v[44:47]\n"
    "s_cmov_b32 %[dh], -8192\n"
    "v_mfma_f32_16x16x32_bf16 v[44:47], v[212:215], v[204:207], v[44:47]\n"
    "ds_read_b128 v[208:211], v12 offset:6144\n"
    "s_waitcnt lgkmcnt(5)\n"
    "v_mfma_f32_16x16x32_bf16 v[48:51], v[216:219], v[176:179], v[48:51]\n"
    "s_mul_i32 %[dw], %[dh], 7\n"
    "v_mfma_f32_16x16x32_bf16 v[48:51], v[220:223], v[180:183], v[48:51]\n"
    "ds_read_b128 v[212:215], v12 offset:7168\n"
    "v_mfma_f32_16x16x32_bf16 v[52:55], v[216:219], v[184:187], v[52:55]\n"
    "s_cmp_lt_u32 %[cnt], 8\n"
    "v_mfma_f32_16x16x32_bf16 v[52:55], v[220:223], v[188:191], v[52:55]\n"
    "v_add_u32 v8, %[dh], v8\n"
    "v_mfma_f32_16x16x32_bf16 v[56:59], v[216:219], v[192:195], v[56:59]\n"
    "v_add_u32 v9, %[dh], v9\n"
    "v_mfma_f32_16x16x32_bf16 v[56:59], v[220:223], v[196:199], v[56:59]\n"
    "v_add_u32 v10, %[dh], v10\n"
    "v_mfma_f32_16x16x32_bf16 v[60:63], v[216:219], v[200:203], v[60:63]\n"
    "v_mfma_f32_16x16x32_bf16 v[60:63], v[220:223], v[204:207], v[60:63]\n"
    "ds_read_b128 v[216:219], v12 offset:8192\n"
    "s_waitcnt lgkmcnt(5)\n"
    "v_mfma_f32_16x16x32_bf16 v[64:67], v[224:227], v[176:179], v[64:67]\n"
    "v_mfma_f32_16x16x32_bf16 v[64:67], v[228:231], v[180:183], v[64:67]\n"
    "ds_read_b128 v[220:223], v12 offset:9216\n"
    "v_mfma_f32_16x16x32_bf16 v[68:71], v[224:227], v[184:187], v[68:71]\n"
    "v_mfma_f32_16x16x32_bf16 v[68:71], v[228:231], v[188:191], v[68:71]\n"
    "v_add_u32 v11, %[dh], v11\n"
    "v_mfma_f32_16x16x32_bf16 v[72:75], v[224:227], v[192:195], v[72:75]\n"
    "v_mfma_f32_16x16x32_bf16 v[72:75], v[228:231], v[196:199], v[72:75]\n"
    "v_mfma_f32_16x16x32_bf16 v[76:79], v[224:227], v[200:203], v[76:79]\n"
    "v_mfma_f32_16x16x32_bf16 v[76:79], v[228:231], v[204:207], v[76:79]\n"
    "ds_read_b128 v[224:227], v12 offset:10240\n"
    "s_waitcnt lgkmcnt(3)\n"
    "v_mfma_f32_16x16x32_bf16 v[80:83], v[208:211], v[176:179], v[80:83]\n"
    "ds_read_b128 v[228:231], v12 offset:11264\n"
    "v_mfma_f32_16x16x32_bf16 v[80:83], v[212:215], v[180:183], v[80:83]\n"
    "v_mfma_f32_16x16x32_bf16 v[84:87], v[208:211], v[184:187], v[84:87]\n"
    "v_add_u32 v12, %[dw], v12\n"
    "v_mfma_f32_16x16x32_bf16 v[84:87], v[212:215], v[188:191], v[84:87]\n"
    "v_mfma_f32_16x16x32_bf16 v[88:91], v[208:211], v[192:195], v[88:91]\n"
    "v_mfma_f32_16x16x32_bf16 v[88:91], v[212:215], v[196:199], v[88:91]\n"
    "v_mfma_f32_16x16x32_bf16 v[92:95], v[208:211], v[200:203], v[92:95]\n"
    "v_mfma_f32_16x16x32_bf16 v[92:95], v[212:215], v[204:207], v[92:95]\n"
    "s_cbranch_scc1 1b\n"
    "s_waitcnt lgkmcnt(0)\n"
    "s_barrier\n"
    "v_mfma_f32_16x16x32_bf16 v[96:99], v[216:219], v[176:179], v[96:99]\n"
    "ds_read_b64 v[16:17], v16 offset:0\n"
    "ds_read_b64 v[18:19], v18 offset:0\n"
    "v_mfma_f32_16x16x32_bf16 v[96:99], v[220:223], v[180:183], v[96:99]\n"
    "ds_read_b64 v[20:21], v20 offset:0\n"
    "ds_read_b64 v[22:23], v22 offset:0\n"
    "v_mfma_f32_16x16x32_bf16 v[100:103], v[216:219], v[184:187], v[100:103]\n"
    "ds_read_b128 v[208:211], v12 offset:0\n"
    "v_mfma_f32_16x16x32_bf16 v[100:103], v[220:223], v[188:191], v[100:103]\n"
    "ds_read_b128 v[212:215], v12 offset:1024\n"
    "v_mfma_f32_16x16x32_bf16 v[104:107], v[216:219], v[192:195], v[104:107]\n"
    "ds_read_b128 v[144:147], v8 offset:0\n"
    "v_mfma_f32_16x16x32_bf16 v[104:107], v[220:223], v[196:199], v[104:107]\n"
    "ds_read_b128 v[148:151], v8 offset:1024\n"
    "v_mfma_f32_16x16x32_bf16 v[108:111], v[216:219], v[200:203], v[108:111]\n"
    "ds_read_b128 v[152:155], v9 offset:0\n"
    "s_waitcnt lgkmcnt(5)\n"
    "v_fmac_f32 v32, v232, v16\n"
    "v_fmac_f32 v33, v233, v16\n"
    "v_mfma_f32_16x16x32_bf16 v[108:111], v[220:223], v[204:207], v[108:111]\n"
    "ds_read_b128 v[156:159], v9 offset:1024\n"
    "v_fmac_f32 v34, v234, v16\n"
    "v_fmac_f32 v35, v235, v16\n"
    "v_mfma_f32_16x16x32_bf16 v[112:115], v[224:227], v[176:179], v[112:115]\n"
    "v_fmac_f32 v36, v232, v18\n"
    "v_fmac_f32 v37, v233, v18\n"
    "v_mfma_f32_16x16x32_bf16 v[112:115], v[228:231], v[180:183], v[112:115]\n"
    "ds_read_b128 v[160:163], v10 offset:0\n"
    "v_fmac_f32 v38, v234, v18\n"
    "v_fmac_f32 v39, v235, v18\n"
    "v_mfma_f32_16x16x32_bf16 v[116:119], v[224:227], v[184:187], v[116:119]\n"
    "ds_read_b128 v[164:167], v10 offset:1024\n"
    "v_fmac_f32 v40, v232, v20\n"
    "v_fmac_f32 v41, v233, v20\n"
    "v_mfma_f32_16x16x32_bf16 v[116:119], v[228:231], v[188:191], v[116:119]\n"
    "v_fmac_f32 v42, v234, v20\n"
    "v_fmac_f32 v43, v235, v20\n"
    "v_mfma_f32_16x16x32_bf16 v[120:123], v[224:227], v[192:195], v[120:123]\n"
    "ds_read_b128 v[168:171], v11 offset:0\n"
    "v_fmac_f32 v44, v232, v22\n"
    "v_fmac_f32 v45, v233, v22\n"
    "v_mfma_f32_16x16x32_bf16 v[120:123], v[228:231], v[196:199], v[120:123]\n"
    "ds_read_b128 v[172:175], v11 offset:1024\n"
    "v_fmac_f32 v46, v234, v22\n"
    "v_fmac_f32 v47, v235, v22\n"
    "v_mfma_f32_16x16x32_bf16 v[124:127], v[224:227], v[200:203], v[124:127]\n"
    "v_fmac_f32 v48, v236, v16\n"
    "v_fmac_f32 v49, v237, v16\n"
    "v_mfma_f32_16x16x32_bf16 v[124:127], v[228:231], v[204:207], v[124:127]\n"
    "ds_read_b128 v[216:219], v12 offset:2048\n"
    "v_fmac_f32 v50, v238, v16\n"
    "v_fmac_f32 v51, v239, v16\n"
    "v_mfma_f32_16x16x32_bf16 v[128:131], v[0:3], v[176:179], v[128:131]\n"
    "ds_read_b128 v[220:223], v12 offset:3072\n"
    "v_fmac_f32 v52, v236, v18\n"
    "v_fmac_f32 v53, v237, v18\n"
    "v_mfma_f32_16x16x32_bf16 v[128:131], v[4:7], v[180:183], v[128:131]\n"
    "v_fmac_f32 v54, v238, v18\n"
    "v_fmac_f32 v55, v239, v18\n"
    "v_mfma_f32_16x16x32_bf16 v[132:135], v[0:3], v[184:187], v[132:135]\n"
    "ds_read_b128 v[224:227], v12 offset:4096\n"
    "v_fmac_f32 v56, v236, v20\n"
    "v_fmac_f32 v57, v237, v20\n"
    "v_mfma_f32_16x16x32_bf16 v[132:135], v[4:7], v[188:191], v[132:135]\n"
    "ds_read_b128 v[228:231], v12 offset:5120\n"
    "v_fmac_f32 v58, v238, v20\n"
    "v_fmac_f32 v59, v239, v20\n"
    "v_mfma_f32_16x16x32_bf16 v[136:139], v[0:3], v[192:195], v[136:139]\n"
    "v_fmac_f32 v60, v236, v22\n"
    "v_fmac_f32 v61, v237, v22\n"
    "v_mfma_f32_16x16x32_bf16 v[136:139], v[4:7], v[196:199], v[136:139]\n"
    "v_fmac_f32 v62, v238, v22\n"
    "v_fmac_f32 v63, v239, v22\n"
    "v_mfma_f32_16x16x32_bf16 v[140:143], v[0:3], v[200:203], v[140:143]\n"
    "v_fmac_f32 v64, v240, v16\n"
    "v_fmac_f32 v65, v241, v16\n"
    "v_mfma_f32_16x16x32_bf16 v[140:143], v[4:7], v[204:207], v[140:143]\n"
    "v_fmac_f32 v66, v242, v16\n"
    "v_fmac_f32 v67, v243, v16\n"
    "s_waitcnt lgkmcnt(10)\n"
    "v_mfma_f32_16x16x32_bf16 v[32:35], v[208:211], v[144:147], v[32:35]\n"
    "ds_read_b128 v[0:3], v12 offset:6144\n"
    "v_fmac_f32 v68, v240, v18\n"
    "v_fmac_f32 v69, v241, v18\n"
    "v_mfma_f32_16x16x32_bf16 v[32:35], v[212:215], v[148:151], v[32:35]\n"
    "ds_read_b128 v[4:7], v12 offset:7168\n"
    "v_fmac_f32 v70, v242, v18\n"
    "v_fmac_f32 v71, v243, v18\n"
    "s_waitcnt lgkmcnt(10)\n"
    "v_mfma_f32_16x16x32_bf16 v[36:39], v[208:211], v[152:155], v[36:39]\n"
    "v_fmac_f32 v72, v240, v20\n"
    "v_fmac_f32 v73, v241, v20\n"
    "v_mfma_f32_16x16x32_bf16 v[36:39], v[212:215], v[156:159], v[36:39]\n"
    "v_fmac_f32 v74, v242, v20\n"
    "v_fmac_f32 v75, v243, v20\n"
    "s_waitcnt lgkmcnt(8)\n"
    "v_mfma_f32_16x16x32_bf16 v[40:43], v[208:211], v[160:163], v[40:43]\n"
    "v_fmac_f32 v76, v240, v22\n"
    "v_fmac_f32 v77, v241, v22\n"
    "v_mfma_f32_16x16x32_bf16 v[40:43], v[212:215], v[164:167], v[40:43]\n"
    "v_fmac_f32 v78, v242, v22\n"
    "v_fmac_f32 v79, v243, v22\n"
    "s_waitcnt lgkmcnt(6)\n"
    "v_mfma_f32_16x16x32_bf16 v[44:47], v[208:211], v[168:171], v[44:47]\n"
    "v_fmac_f32 v80, v244, v16\n"
    "v_fmac_f32 v81, v245, v16\n"
    "v_mfma_f32_16x16x32_bf16 v[44:47], v[212:215], v[172:175], v[44:47]\n"
    "ds_read_b128 v[208:211], v12 offset:8192\n"
    "v_fmac_f32 v82, v246, v16\n"
    "v_fmac_f32 v83, v247, v16\n"
    "s_waitcnt lgkmcnt(5)\n"
    "v_mfma_f32_16x16x32_bf16 v[48:51], v[216:219], v[144:147], v[48:51]\n"
    "v_fmac_f32 v84, v244, v18\n"
    "v_fmac_f32 v85, v245, v18\n"
    "v_mfma_f32_16x16x32_bf16 v[48:51], v[220:223], v[148:151], v[48:51]\n"
    "ds_read_b128 v[212:215], v12 offset:9216\n"
    "v_fmac_f32 v86, v246, v18\n"
    "v_fmac_f32 v87, v247, v18\n"
    "v_mfma_f32_16x16x32_bf16 v[52:55], v[216:219], v[152:155], v[52:55]\n"
    "v_fmac_f32 v88, v244, v20\n"
    "v_fmac_f32 v89, v245, v20\n"
    "v_mfma_f32_16x16x32_bf16 v[52:55], v[220:223], v[156:159], v[52:55]\n"
    "v_fmac_f32 v90, v246, v20\n"
    "v_fmac_f32 v91, v247, v20\n"
    "v_mfma_f32_16x16x32_bf16 v[56:59], v[216:219], v[160:163], v[56:59]\n"
    "v_fmac_f32 v92, v244, v22\n"
    "v_fmac_f32 v93, v245, v22\n"
    "v_mfma_f32_16x16x32_bf16 v[56:59], v[220:223], v[164:167], v[56:59]\n"
    "v_fmac_f32 v94, v246, v22\n"
    "v_fmac_f32 v95, v247, v22\n"
    "v_mfma_f32_16x16x32_bf16 v[60:63], v[216:219], v[168:171], v[60:63]\n"
    "v_fmac_f32 v96, v248, v16\n"
    "v_fmac_f32 v97, v249, v16\n"
    "v_mfma_f32_16x16x32_bf16 v[60:63], v[220:223], v[172:175], v[60:63]\n"
    "v_fmac_f32 v98, v250, v16\n"
    "v_fmac_f32 v99, v251, v16\n"
    "s_waitcnt lgkmcnt(4)\n"
    "v_mfma_f32_16x16x32_bf16 v[64:67], v[224:227], v[144:147], v[64:67]\n"
    "v_add_u32 v14, -57344, v12\n"
    "v_add_u32 v11, -8192, v11\n"
    "v_mfma_f32_16x16x32_bf16 v[64:67], v[228:231], v[148:151], v[64:67]\n"
    "v_add_u32 v10, -8192, v10\n"
    "v_add_u32 v9, -8192, v9\n"
    "v_mfma_f32_16x16x32_bf16 v[68:71], v[224:227], v[152:155], v[68:71]\n"
    "v_add_u32 v8, -8192, v8\n"
    "v_fmac_f32 v100, v248, v18\n"
    "v_mfma_f32_16x16x32_bf16 v[68:71], v[228:231], v[156:159], v[68:71]\n"
    "v_fmac_f32 v101, v249, v18\n"
    "v_fmac_f32 v102, v250, v18\n"
    "v_mfma_f32_16x16x32_bf16 v[72:75], v[224:227], v[160:163], v[72:75]\n"
    "v_fmac_f32 v103, v251, v18\n"
    "v_fmac_f32 v104, v248, v20\n"
    "v_mfma_f32_16x16x32_bf16 v[72:75], v[228:231], v[164:167], v[72:75]\n"
    "v_fmac_f32 v105, v249, v20\n"
    "v_fmac_f32 v106, v250, v20\n"
    "v_mfma_f32_16x16x32_bf16 v[76:79], v[224:227], v[168:171], v[76:79]\n"
    "v_fmac_f32 v107, v251, v20\n"
    "v_fmac_f32 v108, v248, v22\n"
    "v_mfma_f32_16x16x32_bf16 v[76:79], v[228:231], v[172:175], v[76:79]\n"
    "v_fmac_f32 v109, v249, v22\n"
    "v_fmac_f32 v110, v250, v22\n"
    "s_barrier\n"
    "s_waitcnt lgkmcnt(2)\n"
    "v_mfma_f32_16x16x32_bf16 v[80:83], v[0:3], v[144:147], v[80:83]\n"
    "ds_read_b128 v[216:219], v14 offset:0\n"
    "v_fmac_f32 v111, v251, v22\n"
    "v_fmac_f32 v112, v252, v16\n"
    "v_mfma_f32_16x16x32_bf16 v[80:83], v[4:7], v[148:151], v[80:83]\n"
    "ds_read_b128 v[220:223], v14 offset:1024\n"
    "v_fmac_f32 v113, v253, v16\n"
    "v_fmac_f32 v114, v254, v16\n"
    "v_mfma_f32_16x16x32_bf16 v[84:87], v[0:3], v[152:155], v[84:87]\n"
    "ds_read_b128 v[176:179], v8 offset:0\n"
    "v_fmac_f32 v115, v255, v16\n"
    "v_fmac_f32 v116, v252, v18\n"
    "v_mfma_f32_16x16x32_bf16 v[84:87], v[4:7], v[156:159], v[84:87]\n"
    "ds_read_b128 v[180:183], v8 offset:1024\n"
    "v_fmac_f32 v117, v253, v18\n"
    "v_fmac_f32 v118, v254, v18\n"
    "v_mfma_f32_16x16x32_bf16 v[88:91], v[0:3], v[160:163], v[88:91]\n"
    "ds_read_b128 v[184:187], v9 offset:0\n"
    "v_fmac_f32 v119, v255, v18\n"
    "v_fmac_f32 v120, v252, v20\n"
    "v_mfma_f32_16x16x32_bf16 v[88:91], v[4:7], v[164:167], v[88:91]\n"
    "ds_read_b128 v[188:191], v9 offset:1024\n"
    "v_fmac_f32 v121, v253, v20\n"
    "v_fmac_f32 v122, v254, v20\n"
    "v_mfma_f32_16x16x32_bf16 v[92:95], v[0:3], v[168:171], v[92:95]\n"
    "ds_read_b128 v[192:195], v10 offset:0\n"
    "v_fmac_f32 v123, v255, v20\n"
    "v_fmac_f32 v124, v252, v22\n"
    "v_mfma_f32_16x16x32_bf16 v[92:95], v[4:7], v[172:175], v[92:95]\n"
    "ds_read_b128 v[196:199], v10 offset:1024\n"
    "v_fmac_f32 v125, v253, v22\n"
    "v_fmac_f32 v126, v254, v22\n"
    "s_waitcnt lgkmcnt(8)\n"
    "v_mfma_f32_16x16x32_bf16 v[96:99], v[208:211], v[144:147], v[96:99]\n"
    "ds_read_b128 v[200:203], v11 offset:0\n"
    "v_fmac_f32 v127, v255, v22\n"
    "v_fmac_f32 v128, v24, v16\n"
    "v_mfma_f32_16x16x32_bf16 v[96:99], v[212:215], v[148:151], v[96:99]\n"
    "ds_read_b128 v[204:207], v11 offset:1024\n"
    "v_fmac_f32 v129, v25, v16\n"
    "v_fmac_f32 v130, v26, v16\n"
    "v_mfma_f32_16x16x32_bf16 v[100:103], v[208:211], v[152:155], v[100:103]\n"
    "ds_read_b128 v[224:227], v12 offset:10240\n"
    "v_fmac_f32 v131, v27, v16\n"
    "v_fmac_f32 v132, v24, v18\n"
    "v_mfma_f32_16x16x32_bf16 v[100:103], v[212:215], v[156:159], v[100:103]\n"
    "ds_read_b128 v[228:231], v12 offset:11264\n"
    "v_fmac_f32 v133, v25, v18\n"
    "v_fmac_f32 v134, v26, v18\n"
    "v_mfma_f32_16x16x32_bf16 v[104:107], v[208:211], v[160:163], v[104:107]\n"
    "ds_read_b128 v[0:3], v14 offset:2048\n"
    "v_fmac_f32 v135, v27, v18\n"
    "v_fmac_f32 v136, v24, v20\n"
    "v_mfma_f32_16x16x32_bf16 v[104:107], v[212:215], v[164:167], v[104:107]\n"
    "ds_read_b128 v[4:7], v14 offset:3072\n"
    "v_fmac_f32 v137, v25, v20\n"
    "v_fmac_f32 v138, v26, v20\n"
    "v_mfma_f32_16x16x32_bf16 v[108:111], v[208:211], v[168:171], v[108:111]\n"
    "v_fmac_f32 v139, v27, v20\n"
    "v_fmac_f32 v140, v24, v22\n"
    "v_mfma_f32_16x16x32_bf16 v[108:111], v[212:215], v[172:175], v[108:111]\n"
    "ds_read_b128 v[208:211], v12 offset:12288\n"
    "v_fmac_f32 v141, v25, v22\n"
    "v_fmac_f32 v142, v26, v22\n"
    "s_waitcnt lgkmcnt(11)\n"
    "v_mfma_f32_16x16x32_bf16 v[32:35], v[216:219], v[176:179], v[32:35]\n"
    "ds_read_b128 v[212:215], v12 offset:13312\n"
    "v_fmac_f32 v143, v27, v22\n"
    "v_mfma_f32_16x16x32_bf16 v[32:35], v[220:223], v[180:183], v[32:35]\n"
    "s_waitcnt lgkmcnt(10)\n"
    "v_mfma_f32_16x16x32_bf16 v[36:39], v[216:219], v[184:187], v[36:39]\n"
    "v_mfma_f32_16x16x32_bf16 v[36:39], v[220:223], v[188:191], v[36:39]\n"
    "s_waitcnt lgkmcnt(8)\n"
    "v_mfma_f32_16x16x32_bf16 v[40:43], v[216:219], v[192:195], v[40:43]\n"
    "v_mfma_f32_16x16x32_bf16 v[40:43], v[220:223], v[196:199], v[40:43]\n"
    "s_waitcnt lgkmcnt(6)\n"
    "v_mfma_f32_16x16x32_bf16 v[44:47], v[216:219], v[200:203], v[44:47]\n"
    "v_max_f32 v32, 0, v32\n"
    "v_max_f32 v33, 0, v33\n"
    "v_mfma_f32_16x16x32_bf16 v[44:47], v[220:223], v[204:207], v[44:47]\n"
    "ds_read_b128 v[216:219], v14 offset:4096\n"
    "v_mul_f32 v34, v17, v34\n"
    "v_mul_f32 v35, v17, v35\n"
    "s_waitcnt lgkmcnt(5)\n"
    "v_mfma_f32_16x16x32_bf16 v[112:115], v[224:227], v[144:147], v[112:115]\n"
    "ds_read_b128 v[220:223], v14 offset:5120\n"
    "v_mul_f32 v32, v32, v34\n"
    "v_mul_f32 v33, v33, v35\n"
    "v_mfma_f32_16x16x32_bf16 v[112:115], v[228:231], v[148:151], v[112:115]\n"
    "v_cvt_pk_bf16_f32 v32, v32, v33\n"
    "v_max_f32 v36, 0, v36\n"
    "v_mfma_f32_16x16x32_bf16 v[116:119], v[224:227], v[152:155], v[116:119]\n"
    "v_max_f32 v37, 0, v37\n"
    "v_mul_f32 v38, v19, v38\n"
    "v_mfma_f32_16x16x32_bf16 v[116:119], v[228:231], v[156:159], v[116:119]\n"
    "v_mul_f32 v39, v19, v39\n"
    "v_mul_f32 v36, v36, v38\n"
    "v_mfma_f32_16x16x32_bf16 v[120:123], v[224:227], v[160:163], v[120:123]\n"
    "v_mul_f32 v37, v37, v39\n"
    "v_cvt_pk_bf16_f32 v33, v36, v37\n"
    "v_mfma_f32_16x16x32_bf16 v[120:123], v[228:231], v[164:167], v[120:123]\n"
    "v_max_f32 v40, 0, v40\n"
    "v_max_f32 v41, 0, v41\n"
    "v_mfma_f32_16x16x32_bf16 v[124:127], v[224:227], v[168:171], v[124:127]\n"
    "v_mul_f32 v42, v21, v42\n"
    "v_mul_f32 v43, v21, v43\n"
    "v_mfma_f32_16x16x32_bf16 v[124:127], v[228:231], v[172:175], v[124:127]\n"
    "ds_read_b128 v[224:227], v14 offset:6144\n"
    "v_mul_f32 v40, v40, v42\n"
    "v_mul_f32 v41, v41, v43\n"
    "s_waitcnt lgkmcnt(5)\n"
    "v_mfma_f32_16x16x32_bf16 v[48:51], v[0:3], v[176:179], v[48:51]\n"
    "ds_read_b128 v[228:231], v14 offset:7168\n"
    "v_cvt_pk_bf16_f32 v34, v40, v41\n"
    "v_max_f32 v44, 0, v44\n"
    "v_mfma_f32_16x16x32_bf16 v[48:51], v[4:7], v[180:183], v[48:51]\n"
    "v_max_f32 v45, 0, v45\n"
    "v_mul_f32 v46, v23, v46\n"
    "v_mfma_f32_16x16x32_bf16 v[52:55], v[0:3], v[184:187], v[52:55]\n"
    "v_mul_f32 v47, v23, v47\n"
    "v_mul_f32 v44, v44, v46\n"
    "v_mfma_f32_16x16x32_bf16 v[52:55], v[4:7], v[188:191], v[52:55]\n"
    "v_mul_f32 v45, v45, v47\n"
    "v_cvt_pk_bf16_f32 v35, v44, v45\n"
    "v_mfma_f32_16x16x32_bf16 v[56:59], v[0:3], v[192:195], v[56:59]\n"
    "buffer_store_dwordx4 v[32:35], v13, %[rs], %[ab] offen offset:0 sc1\n"
    "v_max_f32 v48, 0, v48\n"
    "v_max_f32 v49, 0, v49\n"
    "v_mfma_f32_16x16x32_bf16 v[56:59], v[4:7], v[196:199], v[56:59]\n"
    "v_mul_f32 v50, v17, v50\n"
    "v_mul_f32 v51, v17, v51\n"
    "v_mfma_f32_16x16x32_bf16 v[60:63], v[0:3], v[200:203], v[60:63]\n"
    "v_mul_f32 v48, v48, v50\n"
    "v_mul_f32 v49, v49, v51\n"
    "v_mfma_f32_16x16x32_bf16 v[60:63], v[4:7], v[204:207], v[60:63]\n"
    "ds_read_b128 v[0:3], v14 offset:8192\n"
    "v_cvt_pk_bf16_f32 v48, v48, v49\n"
    "v_max_f32 v52, 0, v52\n"
    "s_waitcnt lgkmcnt(5)\n"
    "v_mfma_f32_16x16x32_bf16 v[128:131], v[208:211], v[144:147], v[128:131]\n"
    "ds_read_b128 v[4:7], v14 offset:9216\n"
    "v_max_f32 v53, 0, v53\n"
    "v_mul_f32 v54, v19, v54\n"
    "v_mfma_f32_16x16x32_bf16 v[128:131], v[212:215], v[148:151], v[128:131]\n"
    "ds_read_b128 v[144:147], v14 offset:10240\n"
    "v_mul_f32 v55, v19, v55\n"
    "v_mul_f32 v52, v52, v54\n"
    "v_mfma_f32_16x16x32_bf16 v[132:135], v[208:211], v[152:155], v[132:135]\n"
    "ds_read_b128 v[148:151], v14 offset:11264\n"
    "v_mul_f32 v53, v53, v55\n"
    "v_cvt_pk_bf16_f32 v49, v52, v53\n"
    "v_mfma_f32_16x16x32_bf16 v[132:135], v[212:215], v[156:159], v[132:135]\n"
    "ds_read_b128 v[152:155], v14 offset:12288\n"
    "v_max_f32 v56, 0, v56\n"
    "v_max_f32 v57, 0, v57\n"
    "v_mfma_f32_16x16x32_bf16 v[136:139], v[208:211], v[160:163], v[136:139]\n"
    "ds_read_b128 v[156:159], v14 offset:13312\n"
    "v_mul_f32 v58, v21, v58\n"
    "v_mul_f32 v59, v21, v59\n"
    "v_mfma_f32_16x16x32_bf16 v[136:139], v[212:215], v[164:167], v[136:139]\n"
    "v_mul_f32 v56, v56, v58\n"
    "v_mul_f32 v57, v57, v59\n"
    "v_mfma_f32_16x16x32_bf16 v[140:143], v[208:211], v[168:171], v[140:143]\n"
    "v_cvt_pk_bf16_f32 v50, v56, v57\n"
    "v_max_f32 v60, 0, v60\n"
    "v_mfma_f32_16x16x32_bf16 v[140:143], v[212:215], v[172:175], v[140:143]\n"
    "v_max_f32 v61, 0, v61\n"
    "v_mul_f32 v62, v23, v62\n"
    "s_waitcnt lgkmcnt(8)\n"
    "v_mfma_f32_16x16x32_bf16 v[64:67], v[216:219], v[176:179], v[64:67]\n"
    "v_mul_f32 v63, v23, v63\n"
    "v_mul_f32 v60, v60, v62\n"
    "s_waitcnt lgkmcnt(0)\n"
    "s_barrier\n"
    "v_mfma_f32_16x16x32_bf16 v[64:67], v[220:223], v[180:183], v[64:67]\n"
    "v_mul_f32 v61, v61, v63\n"
    "v_cvt_pk_bf16_f32 v51, v60, v61\n"
    "v_mfma_f32_16x16x32_bf16 v[68:71], v[216:219], v[184:187], v[68:71]\n"
    "buffer_store_dwordx4 v[48:51], v13, %[rs], %[ab] offen offset:1024 sc1\n"
    "v_mfma_f32_16x16x32_bf16 v[68:71], v[220:223], v[188:191], v[68:71]\n"
    "v_mfma_f32_16x16x32_bf16 v[72:75], v[216:219], v[192:195], v[72:75]\n"
    "v_mfma_f32_16x16x32_bf16 v[72:75], v[220:223], v[196:199], v[72:75]\n"
    "v_mfma_f32_16x16x32_bf16 v[76:79], v[216:219], v[200:203], v[76:79]\n"
    "v_max_f32 v64, 0, v64\n"
    "v_max_f32 v65, 0, v65\n"
    "v_mfma_f32_16x16x32_bf16 v[76:79], v[220:223], v[204:207], v[76:79]\n"
    "v_mul_f32 v66, v17, v66\n"
    "v_mul_f32 v67, v17, v67\n"
    "v_mfma_f32_16x16x32_bf16 v[80:83], v[224:227], v[176:179], v[80:83]\n"
    "v_mul_f32 v64, v64, v66\n"
    "v_mul_f32 v65, v65, v67\n"
    "v_mfma_f32_16x16x32_bf16 v[80:83], v[228:231], v[180:183], v[80:83]\n"
    "v_cvt_pk_bf16_f32 v64, v64, v65\n"
    "v_max_f32 v68, 0, v68\n"
    "v_mfma_f32_16x16x32_bf16 v[84:87], v[224:227], v[184:187], v[84:87]\n"
    "v_max_f32 v69, 0, v69\n"
    "v_mul_f32 v70, v19, v70\n"
    "v_mfma_f32_16x16x32_bf16 v[84:87], v[228:231], v[188:191], v[84:87]\n"
    "v_mul_f32 v71, v19, v71\n"
    "v_mul_f32 v68, v68, v70\n"
    "v_mfma_f32_16x16x32_bf16 v[88:91], v[224:227], v[192:195], v[88:91]\n"
    "v_mul_f32 v69, v69, v71\n"
    "v_cvt_pk_bf16_f32 v65, v68, v69\n"
    "v_mfma_f32_16x16x32_bf16 v[88:91], v[228:231], v[196:199], v[88:91]\n"
    "v_max_f32 v72, 0, v72\n"
    "v_max_f32 v73, 0, v73\n"
    "v_mfma_f32_16x16x32_bf16 v[92:95], v[224:227], v[200:203], v[92:95]\n"
    "v_mul_f32 v74, v21, v74\n"
    "v_mul_f32 v75, v21, v75\n"
    "v_mfma_f32_16x16x32_bf16 v[92:95], v[228:231], v[204:207], v[92:95]\n"
    "v_mul_f32 v72, v72, v74\n"
    "v_mul_f32 v73, v73, v75\n"
    "v_mfma_f32_16x16x32_bf16 v[96:99], v[0:3], v[176:179], v[96:99]\n"
    "v_cvt_pk_bf16_f32 v66, v72, v73\n"
    "v_max_f32 v76, 0, v76\n"
    "v_mfma_f32_16x16x32_bf16 v[96:99], v[4:7], v[180:183], v[96:99]\n"
    "v_max_f32 v77, 0, v77\n"
    "v_mul_f32 v78, v23, v78\n"
    "v_mfma_f32_16x16x32_bf16 v[100:103], v[0:3], v[184:187], v[100:103]\n"
    "v_mul_f32 v79, v23, v79\n"
    "v_mul_f32 v76, v76, v78\n"
    "v_mfma_f32_16x16x32_bf16 v[100:103], v[4:7], v[188:191], v[100:103]\n"
    "v_mul_f32 v77, v77, v79\n"
    "v_cvt_pk_bf16_f32 v67, v76, v77\n"
    "v_mfma_f32_16x16x32_bf16 v[104:107], v[0:3], v[192:195], v[104:107]\n"
    "buffer_store_dwordx4 v[64:67], v13, %[rs], %[ab] offen offset:2048 sc1\n"
    "v_max_f32 v80, 0, v80\n"
    "v_max_f32 v81, 0, v81\n"
    "v_mfma_f32_16x16x32_bf16 v[104:107], v[4:7], v[196:199], v[104:107]\n"
    "v_mul_f32 v82, v17, v82\n"
    "v_mul_f32 v83, v17, v83\n"
    "v_mfma_f32_16x16x32_bf16 v[108:111], v[0:3], v[200:203], v[108:111]\n"
    "v_mul_f32 v80, v80, v82\n"
    "v_mul_f32 v81, v81, v83\n"
    "v_mfma_f32_16x16x32_bf16 v[108:111], v[4:7], v[204:207], v[108:111]\n"
    "v_cvt_pk_bf16_f32 v80, v80, v81\n"
    "v_max_f32 v84, 0, v84\n"
    "v_mfma_f32_16x16x32_bf16 v[112:115], v[144:147], v[176:179], v[112:115]\n"
    "v_max_f32 v85, 0, v85\n"
    "v_mul_f32 v86, v19, v86\n"
    "v_mfma_f32_16x16x32_bf16 v[112:115], v[148:151], v[180:183], v[112:115]\n"
    "v_mul_f32 v87, v19, v87\n"
    "v_mul_f32 v84, v84, v86\n"
    "v_mfma_f32_16x16x32_bf16 v[116:119], v[144:147], v[184:187], v[116:119]\n"
    "v_mul_f32 v85, v85, v87\n"
    "v_cvt_pk_bf16_f32 v81, v84, v85\n"
    "v_mfma_f32_16x16x32_bf16 v[116:119], v[148:151], v[188:191], v[116:119]\n"
    "v_max_f32 v88, 0, v88\n"
    "v_max_f32 v89, 0, v89\n"
    "v_mfma_f32_16x16x32_bf16 v[120:123], v[144:147], v[192:195], v[120:123]\n"
    "v_mul_f32 v90, v21, v90\n"
    "v_mul_f32 v91, v21, v91\n"
    "v_mfma_f32_16x16x32_bf16 v[120:123], v[148:151], v[196:199], v[120:123]\n"
    "v_mul_f32 v88, v88, v90\n"
    "v_mul_f32 v89, v89, v91\n"
    "v_mfma_f32_16x16x32_bf16 v[124:127], v[144:147], v[200:203], v[124:127]\n"
    "v_cvt_pk_bf16_f32 v82, v88, v89\n"
    "v_max_f32 v92, 0, v92\n"
    "v_mfma_f32_16x16x32_bf16 v[124:127], v[148:151], v[204:207], v[124:127]\n"
    "v_max_f32 v93, 0, v93\n"
    "v_mul_f32 v94, v23, v94\n"
    "v_mfma_f32_16x16x32_bf16 v[128:131], v[152:155], v[176:179], v[128:131]\n"
    "v_mul_f32 v95, v23, v95\n"
    "v_mul_f32 v92, v92, v94\n"
    "v_mfma_f32_16x16x32_bf16 v[128:131], v[156:159], v[180:183], v[128:131]\n"
    "v_mul_f32 v93, v93, v95\n"
    "v_cvt_pk_bf16_f32 v83, v92, v93\n"
    "v_mfma_f32_16x16x32_bf16 v[132:135], v[152:155], v[184:187], v[132:135]\n"
    "buffer_store_dwordx4 v[80:83], v13, %[rs], %[ab] offen offset:3072 sc1\n"
    "s_add_u32 %[ab], %[ab], 4096\n"
    "v_max_f32 v96, 0, v96\n"
    "v_max_f32 v97, 0, v97\n"
    "v_mfma_f32_16x16x32_bf16 v[132:135], v[156:159], v[188:191], v[132:135]\n"
    "v_mul_f32 v98, v17, v98\n"
    "v_mul_f32 v99, v17, v99\n"
    "v_mfma_f32_16x16x32_bf16 v[136:139], v[152:155], v[192:195], v[136:139]\n"
    "v_mul_f32 v96, v96, v98\n"
    "v_mul_f32 v97, v97, v99\n"
    "v_mfma_f32_16x16x32_bf16 v[136:139], v[156:159], v[196:199], v[136:139]\n"
    "v_cvt_pk_bf16_f32 v96, v96, v97\n"
    "v_max_f32 v100, 0, v100\n"
    "v_mfma_f32_16x16x32_bf16 v[140:143], v[152:155], v[200:203], v[140:143]\n"
    "v_max_f32 v101, 0, v101\n"
    "v_mul_f32 v102, v19, v102\n"
    "v_mfma_f32_16x16x32_bf16 v[140:143], v[156:159], v[204:207], v[140:143]\n"
    "v_mul_f32 v103, v19, v103\n"
    "v_mul_f32 v100, v100, v102\n"
    "v_mul_f32 v101, v101, v103\n"
    "v_cvt_pk_bf16_f32 v97, v100, v101\n"
    "v_max_f32 v104, 0, v104\n"
    "v_max_f32 v105, 0, v105\n"
    "v_mul_f32 v106, v21, v106\n"
    "v_mul_f32 v107, v21, v107\n"
    "v_mul_f32 v104, v104, v106\n"
    "v_mul_f32 v105, v105, v107\n"
    "v_cvt_pk_bf16_f32 v98, v104, v105\n"
    "v_max_f32 v108, 0, v108\n"
    "v_max_f32 v109, 0, v109\n"
    "v_mul_f32 v110, v23, v110\n"
    "v_mul_f32 v111, v23, v111\n"
    "v_mul_f32 v108, v108, v110\n"
    "v_mul_f32 v109, v109, v111\n"
    "v_cvt_pk_bf16_f32 v99, v108, v109\n"
    "buffer_store_dwordx4 v[96:99], v13, %[rs], %[ab] offen offset:0 sc1\n"
    "v_max_f32 v112, 0, v112\n"
    "v_max_f32 v113, 0, v113\n"
    "v_mul_f32 v114, v17, v114\n"
    "v_mul_f32 v115, v17, v115\n"
    "v_mul_f32 v112, v112, v114\n"
    "v_mul_f32 v113, v113, v115\n"
    "v_cvt_pk_bf16_f32 v112, v112, v113\n"
    "v_max_f32 v116, 0, v116\n"
    "v_max_f32 v117, 0, v117\n"
    "v_mul_f32 v118, v19, v118\n"
    "v_mul_f32 v119, v19, v119\n"
    "v_mul_f32 v116, v116, v118\n"
    "v_mul_f32 v117, v117, v119\n"
    "v_cvt_pk_bf16_f32 v113, v116, v117\n"
    "v_max_f32 v120, 0, v120\n"
    "v_max_f32 v121, 0, v121\n"
    "v_mul_f32 v122, v21, v122\n"
    "v_mul_f32 v123, v21, v123\n"
    "v_mul_f32 v120, v120, v122\n"
    "v_mul_f32 v121, v121, v123\n"
    "v_cvt_pk_bf16_f32 v114, v120, v121\n"
    "v_max_f32 v124, 0, v124\n"
    "v_max_f32 v125, 0, v125\n"
    "v_mul_f32 v126, v23, v126\n"
    "v_mul_f32 v127, v23, v127\n"
    "v_mul_f32 v124, v124, v126\n"
    "v_mul_f32 v125, v125, v127\n"
    "v_cvt_pk_bf16_f32 v115, v124, v125\n"
    "buffer_store_dwordx4 v[112:115], v13, %[rs], %[ab] offen offset:1024 sc1\n"
    "v_max_f32 v128, 0, v128\n"
    "v_max_f32 v129, 0, v129\n"
    "v_mul_f32 v130, v17, v130\n"
    "v_mul_f32 v131, v17, v131\n"
    "v_mul_f32 v128, v128, v130\n"
    "v_mul_f32 v129, v129, v131\n"
    "v_cvt_pk_bf16_f32 v128, v128, v129\n"
    "v_max_f32 v132, 0, v132\n"
    "v_max_f32 v133, 0, v133\n"
    "v_mul_f32 v134, v19, v134\n"
    "v_mul_f32 v135, v19, v135\n"
    "v_mul_f32 v132, v132, v134\n"
    "v_mul_f32 v133, v133, v135\n"
    "v_cvt_pk_bf16_f32 v129, v132, v133\n"
    "v_max_f32 v136, 0, v136\n"
    "v_max_f32 v137, 0, v137\n"
    "v_mul_f32 v138, v21, v138\n"
    "v_mul_f32 v139, v21, v139\n"
    "v_mul_f32 v136, v136, v138\n"
    "v_mul_f32 v137, v137, v139\n"
    "v_cvt_pk_bf16_f32 v130, v136, v137\n"
    "v_max_f32 v140, 0, v140\n"
    "v_max_f32 v141, 0, v141\n"
    "v_mul_f32 v142, v23, v142\n"
    "v_mul_f32 v143, v23, v143\n"
    "v_mul_f32 v140, v140, v142\n"
    "v_mul_f32 v141, v141, v143\n"
    "v_cvt_pk_bf16_f32 v131, v140, v141\n"
    "s_cmp_eq_u64 %[em], 1\n"
    "s_cbranch_scc1 3f\n"
    "buffer_store_dwordx4 v[128:131], v13, %[rs], %[ab] offen offset:2048 sc1\n"
    "3:\n"
    "v_mov_b32 v16, 1\n"
    "s_mov_b64 exec, 3\n"
    "s_waitcnt vmcnt(0)\n"
    "global_atomic_add v15, v16, %[fl] sc1\n"
    "s_mov_b64 exec, -1\n"
    : "+{v[8:23]}"(v), [ab] "+s"(ab), [cnt] "=&s"(cnt), [dh] "=&s"(dh), [dw] "=&s"(dw)
    : [rs] "s"(rawrsrc(ws, WS_END)), [fl] "s"(ws + WS_FL), [em] "s"(nt == 13 && wn == 0 ? 1ull : ~0ull)
    : "scc", "memory", "v0", "v1", "v2", "v3", "v4", "v5", "v6", "v7", "v24", "v25", "v26", "v27", "v32", "v33", "v34", "v35", "v36", "v37", "v38", "v39", "v40", "v41", "v42", "v43", "v44", "v45", "v46", "v47", "v48", "v49", "v50", "v51", "v52", "v53", "v54", "v55", "v56", "v57", "v58", "v59", "v60", "v61", "v62", "v63", "v64", "v65", "v66", "v67", "v68", "v69", "v70", "v71", "v72", "v73", "v74", "v75", "v76", "v77", "v78", "v79", "v80", "v81", "v82", "v83", "v84", "v85", "v86", "v87", "v88", "v89", "v90", "v91", "v92", "v93", "v94", "v95", "v96", "v97", "v98", "v99", "v100", "v101", "v102", "v103", "v104", "v105", "v106", "v107", "v108", "v109", "v110", "v111", "v112", "v113", "v114", "v115", "v116", "v117", "v118", "v119", "v120", "v121", "v122", "v123", "v124", "v125", "v126", "v127", "v128", "v129", "v130", "v131", "v132", "v133", "v134", "v135", "v136", "v137", "v138", "v139", "v140", "v141", "v142", "v143", "v144", "v145", "v146", "v147", "v148", "v149", "v150", "v151", "v152", "v153", "v154", "v155", "v156", "v157", "v158", "v159", "v160", "v161", "v162", "v163", "v164", "v165", "v166", "v167", "v168", "v169", "v170", "v171", "v172", "v173", "v174", "v175", "v176", "v177", "v178", "v179", "v180", "v181", "v182", "v183", "v184", "v185", "v186", "v187", "v188", "v189", "v190", "v191", "v192", "v193", "v194", "v195", "v196", "v197", "v198", "v199", "v200", "v201", "v202", "v203", "v204", "v205", "v206", "v207", "v208", "v209", "v210", "v211", "v212", "v213", "v214", "v215", "v216", "v217", "v218", "v219", "v220", "v221", "v222", "v223", "v224", "v225", "v226", "v227", "v228", "v229", "v230", "v231", "v232", "v233", "v234", "v235", "v236", "v237", "v238", "v239", "v240", "v241", "v242", "v243", "v244", "v245", "v246", "v247", "v248", "v249", "v250", "v251", "v252", "v253", "v254", "v255");
}

struct P3In { u32x8 v; u32x4 t, c; };
DEV unsigned wg_part(int fb, int w) { return (w < 3 ? w * 7 : ((fb & 4) ? 19 : 21)) * 1024; }
DEV void p3_setup(P3In& p, int rb, int cb, int fb, int hw, int lane) {
  const int r = lane & 15, g = lane >> 4, nt = (fb & 4) ? 13 : 14, t = min(hw * 4 + g, nt - 1);
  const unsigned v16 = lane * 16;
  const unsigned xr = (rb * 64 + hw * 16 + r) * 1280 + cb * 160;
  p.v = u32x8{v16, v16 + 4096, L_FLG, (C_F2 + (cb & 1) * 128 + rb * 4 + hw) * 256, l_box(hw) + v16, l_box(1 ^ hw) + v16, l_box(2 ^ hw) + 5120 + v16, l_box(3 ^ hw) + 10240 + v16};
  p.t = u32x4{xr + g * 16, xr + (g & 1) * 16 + (g >> 1) * 8, (lane == 0 ? C_WO + cb : lane == 1 ? C_WD + cb : C_WG + fb) * 256, lane == 0 ? 5u : lane == 1 ? 27u : nt * 4u};
  p.c = u32x4{WS_CS + (fb_t0(fb) + t) * 256 + r * 4, L_MISC + 1024 + t * 64 + r * 4, wd_base(cb, hw) + (rb & 3) * 128 + lane * 512, 0};
  asm volatile("" : "+{v[24:31]}"(p.v), "+{v[20:23]}"(p.t), "+{v[4:7]}"(p.c));
}
DEV void p3_compute(const P3In& p, const char* x, char* ws, int rb, int cb, int fb, int hw) {
  const char* fl = ws + WS_FL;
  const u32x4 rs = rawrsrc(ws, WS_END);
  u32x8 o;
  f32x4 rc;
  int n;
  unsigned wo = WS_WO + (cb * 20 + hw * 5) * 5120, wg = WS_WGU + fb_t0(fb) * 20480 + wg_part(fb, hw);
  const unsigned ob = WS_O + rb * 4 * 20480 + hw * 5120;
  asm volatile(
    "s_mov_b32 %[n], 200000\n"
    "s_nop 3\n"
    "s_cmp_eq_u32 %[hw], 3\n"
    "s_cbranch_scc0 33f\n"
    "s_mov_b64 exec, 1\n"
    "s_branch 35f\n"
    "30:\n"
    "s_sleep 16\n"
    "35:\n"
    "global_atomic_add v252, v22, v7, %[fl] sc0 sc1\n"
    "s_waitcnt vmcnt(0)\n"
    "v_cmp_lt_u32 vcc, v252, v23\n"
    "s_cbranch_vccz 31f\n"
    "s_sub_u32 %[n], %[n], 1\n"
    "s_cbranch_scc0 30b\n"
    "31:\n"
    "v_mov_b32 v252, 1\n"
    "ds_write_b32 v26, v252 offset:4\n"
    "s_mov_b64 exec, -1\n"
    "s_branch 34f\n"
    "32:\n"
    "s_sleep 16\n"
    "33:\n"
    "ds_read_b32 v252, v26 offset:4\n"
    "s_waitcnt lgkmcnt(0)\n"
    "v_cmp_ge_u32 vcc, 0, v252\n"
    "s_cbranch_vccnz 32b\n"
    "34:\n"
    "global_load_dwordx4 v[8:11], v20, %[x]\n"
    "global_load_dwordx4 v[12:15], v20, %[x] offset:64\n"
    "global_load_dwordx2 v[16:17], v21, %[x] offset:128\n"
    "buffer_load_dwordx4 v[32:35], v24, %[rs], %[wo] offen offset:0 sc1\n"
    "buffer_load_dwordx4 v[36:39], v24, %[rs], %[wo] offen offset:1024 sc1\n"
    "buffer_load_dwordx4 v[40:43], v24, %[rs], %[wo] offen offset:2048 sc1\n"
    "buffer_load_dwordx4 v[44:47], v24, %[rs], %[wo] offen offset:3072 sc1\n"
    "buffer_load_dwordx4 v[48:51], v25, %[rs], %[wo] offen offset:0 sc1\n"
    "buffer_load_dwordx4 v[52:55], v25, %[rs], %[wo] offen offset:1024 sc1\n"
    "buffer_load_dwordx4 v[56:59], v25, %[rs], %[wo] offen offset:2048 sc1\n"
    "buffer_load_dwordx4 v[60:63], v25, %[rs], %[wo] offen offset:3072 sc1\n"
    "s_add_u32 %[wo], %[wo], 8192\n"
    "buffer_load_dwordx4 v[64:67], v24, %[rs], %[wo] offen offset:0 sc1\n"
    "buffer_load_dwordx4 v[68:71], v24, %[rs], %[wo] offen offset:1024 sc1\n"
    "buffer_load_dwordx4 v[72:75], v24, %[rs], %[wo] offen offset:2048 sc1\n"
    "buffer_load_dwordx4 v[76:79], v24, %[rs], %[wo] offen offset:3072 sc1\n"
    "buffer_load_dwordx4 v[80:83], v25, %[rs], %[wo] offen offset:0 sc1\n"
    "buffer_load_dwordx4 v[84:87], v25, %[rs], %[wo] offen offset:1024 sc1\n"
    "buffer_load_dwordx4 v[88:91], v25, %[rs], %[wo] offen offset:2048 sc1\n"
    "buffer_load_dwordx4 v[92:95], v25, %[rs], %[wo] offen offset:3072 sc1\n"
    "s_add_u32 %[wo], %[wo], 8192\n"
    "buffer_load_dwordx4 v[96:99], v24, %[rs], %[wo] offen offset:0 sc1\n"
    "buffer_load_dwordx4 v[100:103], v24, %[rs], %[wo] offen offset:1024 sc1\n"
    "buffer_load_dwordx4 v[104:107], v24, %[rs], %[wo] offen offset:2048 sc1\n"
    "buffer_load_dwordx4 v[108:111], v24, %[rs], %[wo] offen offset:3072 sc1\n"
    "buffer_load_dwordx4 v[112:115], v25, %[rs], %[wo] offen offset:0 sc1\n"
    "buffer_load_dwordx4 v[116:119], v25, %[rs], %[wo] offen offset:1024 sc1\n"
    "buffer_load_dwordx4 v[120:123], v25, %[rs], %[wo] offen offset:2048 sc1\n"
    "buffer_load_dwordx4 v[124:127], v25, %[rs], %[wo] offen offset:3072 sc1\n"
    "s_add_u32 %[wo], %[wo], 8192\n"
    "buffer_load_dwordx4 v[128:131], v24, %[rs], %[wo] offen offset:0 sc1\n"
    "s_cmp_eq_u32 %[hw], 3\n"
    "s_cbranch_scc0 39f\n"
    "s_mov_b64 exec, 2\n"
    "s_branch 41f\n"
    "36:\n"
    "s_sleep 16\n"
    "41:\n"
    "global_atomic_add v252, v22, v7, %[fl] sc0 sc1\n"
    "s_waitcnt vmcnt(0)\n"
    "v_cmp_lt_u32 vcc, v252, v23\n"
    "s_cbranch_vccz 37f\n"
    "s_sub_u32 %[n], %[n], 1\n"
    "s_cbranch_scc0 36b\n"
    "37:\n"
    "v_mov_b32 v252, 2\n"
    "ds_write_b32 v26, v252 offset:4\n"
    "s_mov_b64 exec, -1\n"
    "s_branch 40f\n"
    "38:\n"
    "s_sleep 16\n"
    "39:\n"
    "ds_read_b32 v252, v26 offset:4\n"
    "s_waitcnt lgkmcnt(0)\n"
    "v_cmp_ge_u32 vcc, 1, v252\n"
    "s_cbranch_vccnz 38b\n"
    "40:\n"
    "s_mov_b32 %[wo], 0\n"
    "s_mov_b32 exec_lo, 0x3fffffff\n"
    "s_mov_b32 exec_hi, 0x0\n"
    "buffer_load_dword v220, v6, %[rs], %[wo] offen sc1\n"
    "s_mov_b64 exec, -1\n"
    "s_mov_b32 %[wo], 32768\n"
    "s_mov_b32 exec_lo, 0xfc000000\n"
    "s_mov_b32 exec_hi, 0xffffffff\n"
    "buffer_load_dword v221, v6, %[rs], %[wo] offen sc1\n"
    "s_mov_b64 exec, -1\n"
    "s_mov_b32 %[wo], 65536\n"
    "s_mov_b32 exec_lo, 0x3\n"
    "s_mov_b32 exec_hi, 0x0\n"
    "buffer_load_dword v222, v6, %[rs], %[wo] offen sc1\n"
    "s_mov_b64 exec, -1\n"
    "s_cmp_eq_u32 %[hw], 3\n"
    "s_cbranch_scc0 45f\n"
    "s_mov_b64 exec, 4\n"
    "s_branch 47f\n"
    "42:\n"
    "s_sleep 16\n"
    "47:\n"
    "global_atomic_add v252, v22, v7, %[fl] sc0 sc1\n"
    "s_waitcnt vmcnt(0)\n"
    "v_cmp_lt_u32 vcc, v252, v23\n"
    "s_cbranch_vccz 43f\n"
    "s_sub_u32 %[n], %[n], 1\n"
    "s_cbranch_scc0 42b\n"
    "43:\n"
    "v_mov_b32 v252, 3\n"
    "ds_write_b32 v26, v252 offset:4\n"
    "s_mov_b64 exec, -1\n"
    "s_branch 46f\n"
    "44:\n"
    "s_sleep 16\n"
    "45:\n"
    "ds_read_b32 v252, v26 offset:4\n"
    "s_waitcnt lgkmcnt(0)\n"
    "v_cmp_ge_u32 vcc, 2, v252\n"
    "s_cbranch_vccnz 44b\n"
    "46:\n"
    "buffer_load_dword v232, v4, %[rs], 0 offen offset:0 sc1\n"
    "buffer_load_dword v233, v4, %[rs], 0 offen offset:64 sc1\n"
    "buffer_load_dword v234, v4, %[rs], 0 offen offset:128 sc1\n"
    "buffer_load_dword v235, v4, %[rs], 0 offen offset:192 sc1\n"
    "s_branch 11f\n"
    "10:\n"
    "s_sleep 16\n"
    "11:\n"
    "ds_read_b128 v[252:255], v26 offset:16\n"
    "s_waitcnt lgkmcnt(0)\n"
    "v_min3_u32 v252, v252, v253, v254\n"
    "v_min_u32 v252, v252, v255\n"
    "v_cmp_gt_u32 vcc, 4, v252\n"
    "s_cbranch_vccnz 10b\n"
    "s_add_u32 m0, %[wl], 0\n"
    "s_nop 0\n"
    "buffer_load_dwordx4 v24, %[rs], %[wg] offen offset:0 sc1 lds\n"
    "buffer_load_dwordx4 v24, %[rs], %[wg] offen offset:1024 sc1 lds\n"
    "buffer_load_dwordx4 v24, %[rs], %[wg] offen offset:2048 sc1 lds\n"
    "buffer_load_dwordx4 v24, %[rs], %[wg] offen offset:3072 sc1 lds\n"
    "s_add_u32 m0, %[wl], 4096\n"
    "s_nop 0\n"
    "buffer_load_dwordx4 v25, %[rs], %[wg] offen offset:0 sc1 lds\n"
    "buffer_load_dwordx4 v25, %[rs], %[wg] offen offset:1024 sc1 lds\n"
    "buffer_load_dwordx4 v25, %[rs], %[wg] offen offset:2048 sc1 lds\n"
    "s_add_u32 %[wg], %[wg], %[wst]\n"
    "s_waitcnt vmcnt(7)\n"
    "v_add_f32 v232, v232, v233\n"
    "v_add_f32 v234, v234, v235\n"
    "v_add_f32 v232, v232, v234\n"
    "ds_write_b32 v5, v232\n"
    "s_branch 13f\n"
    "12:\n"
    "s_sleep 16\n"
    "13:\n"
    "ds_read_b128 v[252:255], v26 offset:16\n"
    "s_waitcnt lgkmcnt(0)\n"
    "v_min3_u32 v252, v252, v253, v254\n"
    "v_min_u32 v252, v252, v255\n"
    "v_cmp_gt_u32 vcc, 5, v252\n"
    "s_cbranch_vccnz 12b\n"
    "s_add_u32 m0, %[wl], 28672\n"
    "s_nop 0\n"
    "buffer_load_dwordx4 v24, %[rs], %[wg] offen offset:0 sc1 lds\n"
    "buffer_load_dwordx4 v24, %[rs], %[wg] offen offset:1024 sc1 lds\n"
    "buffer_load_dwordx4 v24, %[rs], %[wg] offen offset:2048 sc1 lds\n"
    "buffer_load_dwordx4 v24, %[rs], %[wg] offen offset:3072 sc1 lds\n"
    "s_add_u32 m0, %[wl], 32768\n"
    "s_nop 0\n"
    "buffer_load_dwordx4 v25, %[rs], %[wg] offen offset:0 sc1 lds\n"
    "buffer_load_dwordx4 v25, %[rs], %[wg] offen offset:1024 sc1 lds\n"
    "buffer_load_dwordx4 v25, %[rs], %[wg] offen offset:2048 sc1 lds\n"
    "s_add_u32 %[wg], %[wg], %[wst]\n"
    "s_branch 15f\n"
    "14:\n"
    "s_sleep 16\n"
    "15:\n"
    "ds_read_b128 v[252:255], v26 offset:16\n"
    "s_waitcnt lgkmcnt(0)\n"
    "v_min3_u32 v252, v252, v253, v254\n"
    "v_min_u32 v252, v252, v255\n"
    "v_cmp_gt_u32 vcc, 6, v252\n"
    "s_cbranch_vccnz 14b\n"
    "s_add_u32 m0, %[wl], 57344\n"
    "s_nop 0\n"
    "buffer_load_dwordx4 v24, %[rs], %[wg] offen offset:0 sc1 lds\n"
    "buffer_load_dwordx4 v24, %[rs], %[wg] offen offset:1024 sc1 lds\n"
    "buffer_load_dwordx4 v24, %[rs], %[wg] offen offset:2048 sc1 lds\n"
    "buffer_load_dwordx4 v24, %[rs], %[wg] offen offset:3072 sc1 lds\n"
    "s_add_u32 m0, %[wl], 61440\n"
    "s_nop 0\n"
    "buffer_load_dwordx4 v25, %[rs], %[wg] offen offset:0 sc1 lds\n"
    "buffer_load_dwordx4 v25, %[rs], %[wg] offen offset:1024 sc1 lds\n"
    "buffer_load_dwordx4 v25, %[rs], %[wg] offen offset:2048 sc1 lds\n"
    "s_add_u32 %[wg], %[wg], %[wst]\n"
    "s_barrier\n"
    "v_mov_b32 v255, 0\n"
    "s_mov_b32 %[wo], 200000\n"
    "s_mov_b64 exec, 1\n"
    "global_atomic_add v252, v27, v255, %[fl] sc0 sc1\n"
    "s_sleep 4\n"
    "1:\n"
    "global_atomic_add v253, v27, v255, %[fl] sc0 sc1\n"
    "s_sleep 4\n"
    "s_waitcnt vmcnt(1)\n"
    "v_cmp_gt_u32 vcc, 8, v252\n"
    "s_cbranch_vccz 2f\n"
    "global_atomic_add v252, v27, v255, %[fl] sc0 sc1\n"
    "s_sleep 4\n"
    "s_waitcnt vmcnt(1)\n"
    "v_cmp_gt_u32 vcc, 8, v253\n"
    "s_cbranch_vccz 2f\n"
    "s_sub_u32 %[wo], %[wo], 1\n"
    "s_cbranch_scc0 1b\n"
    "2:\n"
    "s_mov_b64 exec, -1\n"
    "buffer_load_dwordx4 v[132:135], v24, %[rs], %[o0] offen offset:0 sc1\n"
    "buffer_load_dwordx4 v[136:139], v24, %[rs], %[o0] offen offset:1024 sc1\n"
    "buffer_load_dwordx4 v[140:143], v24, %[rs], %[o0] offen offset:2048 sc1\n"
    "buffer_load_dwordx4 v[144:147], v24, %[rs], %[o0] offen offset:3072 sc1\n"
    "buffer_load_dwordx4 v[148:151], v25, %[rs], %[o0] offen offset:0 sc1\n"
    "buffer_load_dwordx4 v[152:155], v24, %[rs], %[o1] offen offset:0 sc1\n"
    "buffer_load_dwordx4 v[156:159], v24, %[rs], %[o1] offen offset:1024 sc1\n"
    "buffer_load_dwordx4 v[160:163], v24, %[rs], %[o1] offen offset:2048 sc1\n"
    "buffer_load_dwordx4 v[164:167], v24, %[rs], %[o1] offen offset:3072 sc1\n"
    "buffer_load_dwordx4 v[168:171], v25, %[rs], %[o1] offen offset:0 sc1\n"
    "buffer_load_dwordx4 v[172:175], v24, %[rs], %[o2] offen offset:0 sc1\n"
    "buffer_load_dwordx4 v[176:179], v24, %[rs], %[o2] offen offset:1024 sc1\n"
    "buffer_load_dwordx4 v[180:183], v24, %[rs], %[o2] offen offset:2048 sc1\n"
    "buffer_load_dwordx4 v[184:187], v24, %[rs], %[o2] offen offset:3072 sc1\n"
    "buffer_load_dwordx4 v[188:191], v25, %[rs], %[o2] offen offset:0 sc1\n"
    "buffer_load_dwordx4 v[192:195], v24, %[rs], %[o3] offen offset:0 sc1\n"
    "buffer_load_dwordx4 v[196:199], v24, %[rs], %[o3] offen offset:1024 sc1\n"
    "buffer_load_dwordx4 v[200:203], v24, %[rs], %[o3] offen offset:2048 sc1\n"
    "buffer_load_dwordx4 v[204:207], v24, %[rs], %[o3] offen offset:3072 sc1\n"
    "buffer_load_dwordx4 v[208:211], v25, %[rs], %[o3] offen offset:0 sc1\n"
    "s_waitcnt vmcnt(19)\n"
    "v_mfma_f32_16x16x32_bf16 v[212:215], v[32:35], v[132:135], 0\n"
    "v_mfma_f32_16x16x32_bf16 v[216:219], v[36:39], v[132:135], 0\n"
    "v_mfma_f32_16x16x32_bf16 v[220:223], v[40:43], v[132:135], 0\n"
    "v_mfma_f32_16x16x32_bf16 v[224:227], v[44:47], v[132:135], 0\n"
    "v_mfma_f32_16x16x32_bf16 v[228:231], v[48:51], v[132:135], 0\n"
    "s_waitcnt vmcnt(18)\n"
    "v_mfma_f32_16x16x32_bf16 v[212:215], v[52:55], v[136:139], v[212:215]\n"
    "v_mfma_f32_16x16x32_bf16 v[216:219], v[56:59], v[136:139], v[216:219]\n"
    "v_mfma_f32_16x16x32_bf16 v[220:223], v[60:63], v[136:139], v[220:223]\n"
    "v_mfma_f32_16x16x32_bf16 v[224:227], v[64:67], v[136:139], v[224:227]\n"
    "v_mfma_f32_16x16x32_bf16 v[228:231], v[68:71], v[136:139], v[228:231]\n"
    "s_waitcnt vmcnt(17)\n"
    "v_mfma_f32_16x16x32_bf16 v[212:215], v[72:75], v[140:143], v[212:215]\n"
    "v_mfma_f32_16x16x32_bf16 v[216:219], v[76:79], v[140:143], v[216:219]\n"
    "v_mfma_f32_16x16x32_bf16 v[220:223], v[80:83], v[140:143], v[220:223]\n"
    "v_mfma_f32_16x16x32_bf16 v[224:227], v[84:87], v[140:143], v[224:227]\n"
    "v_mfma_f32_16x16x32_bf16 v[228:231], v[88:91], v[140:143], v[228:231]\n"
    "s_waitcnt vmcnt(16)\n"
    "v_mfma_f32_16x16x32_bf16 v[212:215], v[92:95], v[144:147], v[212:215]\n"
    "v_mfma_f32_16x16x32_bf16 v[216:219], v[96:99], v[144:147], v[216:219]\n"
    "v_mfma_f32_16x16x32_bf16 v[220:223], v[100:103], v[144:147], v[220:223]\n"
    "v_mfma_f32_16x16x32_bf16 v[224:227], v[104:107], v[144:147], v[224:227]\n"
    "v_mfma_f32_16x16x32_bf16 v[228:231], v[108:111], v[144:147], v[228:231]\n"
    "s_waitcnt vmcnt(15)\n"
    "v_mfma_f32_16x16x32_bf16 v[212:215], v[112:115], v[148:151], v[212:215]\n"
    "v_mfma_f32_16x16x32_bf16 v[216:219], v[116:119], v[148:151], v[216:219]\n"
    "v_mfma_f32_16x16x32_bf16 v[220:223], v[120:123], v[148:151], v[220:223]\n"
    "v_mfma_f32_16x16x32_bf16 v[224:227], v[124:127], v[148:151], v[224:227]\n"
    "v_mfma_f32_16x16x32_bf16 v[228:231], v[128:131], v[148:151], v[228:231]\n"
    "s_waitcnt vmcnt(14)\n"
    "v_mfma_f32_16x16x32_bf16 v[232:235], v[32:35], v[152:155], 0\n"
    "v_mfma_f32_16x16x32_bf16 v[236:239], v[36:39], v[152:155], 0\n"
    "v_mfma_f32_16x16x32_bf16 v[240:243], v[40:43], v[152:155], 0\n"
    "v_mfma_f32_16x16x32_bf16 v[244:247], v[44:47], v[152:155], 0\n"
    "v_mfma_f32_16x16x32_bf16 v[248:251], v[48:51], v[152:155], 0\n"
    "ds_write_b128 v29, v[212:215] offset:0\n"
    "s_waitcnt vmcnt(13)\n"
    "v_mfma_f32_16x16x32_bf16 v[232:235], v[52:55], v[156:159], v[232:235]\n"
    "v_mfma_f32_16x16x32_bf16 v[236:239], v[56:59], v[156:159], v[236:239]\n"
    "ds_write_b128 v29, v[216:219] offset:1024\n"
    "v_mfma_f32_16x16x32_bf16 v[240:243], v[60:63], v[156:159], v[240:243]\n"
    "v_mfma_f32_16x16x32_bf16 v[244:247], v[64:67], v[156:159], v[244:247]\n"
    "ds_write_b128 v29, v[220:223] offset:2048\n"
    "v_mfma_f32_16x16x32_bf16 v[248:251], v[68:71], v[156:159], v[248:251]\n"
    "s_waitcnt vmcnt(12)\n"
    "v_mfma_f32_16x16x32_bf16 v[232:235], v[72:75], v[160:163], v[232:235]\n"
    "ds_write_b128 v29, v[224:227] offset:3072\n"
    "v_mfma_f32_16x16x32_bf16 v[236:239], v[76:79], v[160:163], v[236:239]\n"
    "v_mfma_f32_16x16x32_bf16 v[240:243], v[80:83], v[160:163], v[240:243]\n"
    "ds_write_b128 v29, v[228:231] offset:4096\n"
    "v_mfma_f32_16x16x32_bf16 v[244:247], v[84:87], v[160:163], v[244:247]\n"
    "v_lshlrev_b32 v132, 16, v8\n"
    "v_mfma_f32_16x16x32_bf16 v[248:251], v[88:91], v[160:163], v[248:251]\n"
    "v_and_b32 v133, 0xffff0000, v8\n"
    "s_waitcnt vmcnt(11)\n"
    "v_mfma_f32_16x16x32_bf16 v[232:235], v[92:95], v[164:167], v[232:235]\n"
    "v_lshlrev_b32 v134, 16, v9\n"
    "v_mfma_f32_16x16x32_bf16 v[236:239], v[96:99], v[164:167], v[236:239]\n"
    "v_and_b32 v135, 0xffff0000, v9\n"
    "v_mfma_f32_16x16x32_bf16 v[240:243], v[100:103], v[164:167], v[240:243]\n"
    "v_lshlrev_b32 v136, 16, v10\n"
    "v_mfma_f32_16x16x32_bf16 v[244:247], v[104:107], v[164:167], v[244:247]\n"
    "v_and_b32 v137, 0xffff0000, v10\n"
    "v_mfma_f32_16x16x32_bf16 v[248:251], v[108:111], v[164:167], v[248:251]\n"
    "v_lshlrev_b32 v138, 16, v11\n"
    "s_waitcnt vmcnt(10)\n"
    "v_mfma_f32_16x16x32_bf16 v[232:235], v[112:115], v[168:171], v[232:235]\n"
    "v_and_b32 v139, 0xffff0000, v11\n"
    "v_mfma_f32_16x16x32_bf16 v[236:239], v[116:119], v[168:171], v[236:239]\n"
    "v_lshlrev_b32 v140, 16, v12\n"
    "v_mfma_f32_16x16x32_bf16 v[240:243], v[120:123], v[168:171], v[240:243]\n"
    "v_and_b32 v141, 0xffff0000, v12\n"
    "v_mfma_f32_16x16x32_bf16 v[244:247], v[124:127], v[168:171], v[244:247]\n"
    "v_mfma_f32_16x16x32_bf16 v[248:251], v[128:131], v[168:171], v[248:251]\n"
    "s_waitcnt vmcnt(9)\n"
    "v_mfma_f32_16x16x32_bf16 v[212:215], v[32:35], v[172:175], 0\n"
    "v_mfma_f32_16x16x32_bf16 v[216:219], v[36:39], v[172:175], 0\n"
    "v_mfma_f32_16x16x32_bf16 v[220:223], v[40:43], v[172:175], 0\n"
    "v_mfma_f32_16x16x32_bf16 v[224:227], v[44:47], v[172:175], 0\n"
    "v_mfma_f32_16x16x32_bf16 v[228:231], v[48:51], v[172:175], 0\n"
    "ds_write_b128 v30, v[232:235] offset:0\n"
    "s_waitcnt vmcnt(8)\n"
    "v_mfma_f32_16x16x32_bf16 v[212:215], v[52:55], v[176:179], v[212:215]\n"
    "v_mfma_f32_16x16x32_bf16 v[216:219], v[56:59], v[176:179], v[216:219]\n"
    "ds_write_b128 v30, v[236:239] offset:1024\n"
    "v_mfma_f32_16x16x32_bf16 v[220:223], v[60:63], v[176:179], v[220:223]\n"
    "v_mfma_f32_16x16x32_bf16 v[224:227], v[64:67], v[176:179], v[224:227]\n"
    "ds_write_b128 v30, v[240:243] offset:2048\n"
    "v_mfma_f32_16x16x32_bf16 v[228:231], v[68:71], v[176:179], v[228:231]\n"
    "s_waitcnt vmcnt(7)\n"
    "v_mfma_f32_16x16x32_bf16 v[212:215], v[72:75], v[180:183], v[212:215]\n"
    "ds_write_b128 v30, v[244:247] offset:3072\n"
    "v_mfma_f32_16x16x32_bf16 v[216:219], v[76:79], v[180:183], v[216:219]\n"
    "v_mfma_f32_16x16x32_bf16 v[220:223], v[80:83], v[180:183], v[220:223]\n"
    "ds_write_b128 v30, v[248:251] offset:4096\n"
    "v_mfma_f32_16x16x32_bf16 v[224:227], v[84:87], v[180:183], v[224:227]\n"
    "v_lshlrev_b32 v142, 16, v13\n"
    "v_mfma_f32_16x16x32_bf16 v[228:231], v[88:91], v[180:183], v[228:231]\n"
    "v_and_b32 v143, 0xffff0000, v13\n"
    "s_waitcnt vmcnt(6)\n"
    "v_mfma_f32_16x16x32_bf16 v[212:215], v[92:95], v[184:187], v[212:215]\n"
    "v_lshlrev_b32 v144, 16, v14\n"
    "v_mfma_f32_16x16x32_bf16 v[216:219], v[96:99], v[184:187], v[216:219]\n"
    "v_and_b32 v145, 0xffff0000, v14\n"
    "v_mfma_f32_16x16x32_bf16 v[220:223], v[100:103], v[184:187], v[220:223]\n"
    "v_lshlrev_b32 v146, 16, v15\n"
    "v_mfma_f32_16x16x32_bf16 v[224:227], v[104:107], v[184:187], v[224:227]\n"
    "v_and_b32 v147, 0xffff0000, v15\n"
    "v_mfma_f32_16x16x32_bf16 v[228:231], v[108:111], v[184:187], v[228:231]\n"
    "v_lshlrev_b32 v148, 16, v16\n"
    "s_waitcnt vmcnt(5)\n"
    "v_mfma_f32_16x16x32_bf16 v[212:215], v[112:115], v[188:191], v[212:215]\n"
    "v_and_b32 v149, 0xffff0000, v16\n"
    "v_mfma_f32_16x16x32_bf16 v[216:219], v[116:119], v[188:191], v[216:219]\n"
    "v_lshlrev_b32 v150, 16, v17\n"
    "v_mfma_f32_16x16x32_bf16 v[220:223], v[120:123], v[188:191], v[220:223]\n"
    "v_and_b32 v151, 0xffff0000, v17\n"
    "v_mfma_f32_16x16x32_bf16 v[224:227], v[124:127], v[188:191], v[224:227]\n"
    "v_mfma_f32_16x16x32_bf16 v[228:231], v[128:131], v[188:191], v[228:231]\n"
    "s_waitcnt vmcnt(4)\n"
    "v_mfma_f32_16x16x32_bf16 v[232:235], v[32:35], v[192:195], v[132:135]\n"
    "v_mfma_f32_16x16x32_bf16 v[236:239], v[36:39], v[192:195], v[136:139]\n"
    "v_mfma_f32_16x16x32_bf16 v[240:243], v[40:43], v[192:195], v[140:143]\n"
    "v_mfma_f32_16x16x32_bf16 v[244:247], v[44:47], v[192:195], v[144:147]\n"
    "v_mfma_f32_16x16x32_bf16 v[248:251], v[48:51], v[192:195], v[148:151]\n"
    "ds_write_b128 v31, v[212:215] offset:0\n"
    "s_waitcnt vmcnt(3)\n"
    "v_mfma_f32_16x16x32_bf16 v[232:235], v[52:55], v[196:199], v[232:235]\n"
    "v_mfma_f32_16x16x32_bf16 v[236:239], v[56:59], v[196:199], v[236:239]\n"
    "ds_write_b128 v31, v[216:219] offset:1024\n"
    "v_mfma_f32_16x16x32_bf16 v[240:243], v[60:63], v[196:199], v[240:243]\n"
    "v_mfma_f32_16x16x32_bf16 v[244:247], v[64:67], v[196:199], v[244:247]\n"
    "ds_write_b128 v31, v[220:223] offset:2048\n"
    "v_mfma_f32_16x16x32_bf16 v[248:251], v[68:71], v[196:199], v[248:251]\n"
    "s_waitcnt vmcnt(2)\n"
    "v_mfma_f32_16x16x32_bf16 v[232:235], v[72:75], v[200:203], v[232:235]\n"
    "ds_write_b128 v31, v[224:227] offset:3072\n"
    "v_mfma_f32_16x16x32_bf16 v[236:239], v[76:79], v[200:203], v[236:239]\n"
    "v_mfma_f32_16x16x32_bf16 v[240:243], v[80:83], v[200:203], v[240:243]\n"
    "ds_write_b128 v31, v[228:231] offset:4096\n"
    "v_mfma_f32_16x16x32_bf16 v[244:247], v[84:87], v[200:203], v[244:247]\n"
    "v_mfma_f32_16x16x32_bf16 v[248:251], v[88:91], v[200:203], v[248:251]\n"
    "s_waitcnt vmcnt(1)\n"
    "v_mfma_f32_16x16x32_bf16 v[232:235], v[92:95], v[204:207], v[232:235]\n"
    "v_mfma_f32_16x16x32_bf16 v[236:239], v[96:99], v[204:207], v[236:239]\n"
    "v_mfma_f32_16x16x32_bf16 v[240:243], v[100:103], v[204:207], v[240:243]\n"
    "v_mfma_f32_16x16x32_bf16 v[244:247], v[104:107], v[204:207], v[244:247]\n"
    "v_mfma_f32_16x16x32_bf16 v[248:251], v[108:111], v[204:207], v[248:251]\n"
    "s_waitcnt lgkmcnt(0)\n"
    "s_barrier\n"
    "s_waitcnt vmcnt(0)\n"
    "v_mfma_f32_16x16x32_bf16 v[232:235], v[112:115], v[208:211], v[232:235]\n"
    "ds_read_b128 v[132:135], v28 offset:0\n"
    "ds_read_b128 v[152:155], v28 offset:5120\n"
    "ds_read_b128 v[172:175], v28 offset:10240\n"
    "v_mfma_f32_16x16x32_bf16 v[236:239], v[116:119], v[208:211], v[236:239]\n"
    "ds_read_b128 v[136:139], v28 offset:1024\n"
    "ds_read_b128 v[156:159], v28 offset:6144\n"
    "ds_read_b128 v[176:179], v28 offset:11264\n"
    "v_mfma_f32_16x16x32_bf16 v[240:243], v[120:123], v[208:211], v[240:243]\n"
    "ds_read_b128 v[140:143], v28 offset:2048\n"
    "ds_read_b128 v[160:163], v28 offset:7168\n"
    "ds_read_b128 v[180:183], v28 offset:12288\n"
    "v_mfma_f32_16x16x32_bf16 v[244:247], v[124:127], v[208:211], v[244:247]\n"
    "ds_read_b128 v[144:147], v28 offset:3072\n"
    "ds_read_b128 v[164:167], v28 offset:8192\n"
    "ds_read_b128 v[184:187], v28 offset:13312\n"
    "v_mfma_f32_16x16x32_bf16 v[248:251], v[128:131], v[208:211], v[248:251]\n"
    "ds_read_b128 v[148:151], v28 offset:4096\n"
    "ds_read_b128 v[168:171], v28 offset:9216\n"
    "ds_read_b128 v[188:191], v28 offset:14336\n"
    "s_nop 4\n"
    "s_waitcnt lgkmcnt(12)\n"
    "v_pk_add_f32 v[132:133], v[132:133], v[152:153]\n"
    "v_pk_add_f32 v[232:233], v[232:233], v[172:173]\n"
    "v_pk_add_f32 v[134:135], v[134:135], v[154:155]\n"
    "v_pk_add_f32 v[234:235], v[234:235], v[174:175]\n"
    "v_pk_add_f32 v[232:233], v[232:233], v[132:133]\n"
    "v_pk_add_f32 v[234:235], v[234:235], v[134:135]\n"
    "v_cvt_pk_bf16_f32 v8, v232, v233\n"
    "v_cvt_pk_bf16_f32 v9, v234, v235\n"
    "s_waitcnt lgkmcnt(9)\n"
    "v_pk_add_f32 v[136:137], v[136:137], v[156:157]\n"
    "v_pk_add_f32 v[236:237], v[236:237], v[176:177]\n"
    "v_pk_add_f32 v[138:139], v[138:139], v[158:159]\n"
    "v_pk_add_f32 v[238:239], v[238:239], v[178:179]\n"
    "v_pk_add_f32 v[236:237], v[236:237], v[136:137]\n"
    "v_pk_add_f32 v[238:239], v[238:239], v[138:139]\n"
    "v_cvt_pk_bf16_f32 v10, v236, v237\n"
    "v_cvt_pk_bf16_f32 v11, v238, v239\n"
    "buffer_store_dwordx4 v[8:11], v24, %[rs], %[hb] offen offset:0 sc1\n"
    "s_waitcnt lgkmcnt(6)\n"
    "v_pk_add_f32 v[140:141], v[140:141], v[160:161]\n"
    "v_pk_add_f32 v[240:241], v[240:241], v[180:181]\n"
    "v_pk_add_f32 v[142:143], v[142:143], v[162:163]\n"
    "v_pk_add_f32 v[242:243], v[242:243], v[182:183]\n"
    "v_pk_add_f32 v[240:241], v[240:241], v[140:141]\n"
    "v_pk_add_f32 v[242:243], v[242:243], v[142:143]\n"
    "v_cvt_pk_bf16_f32 v12, v240, v241\n"
    "v_cvt_pk_bf16_f32 v13, v242, v243\n"
    "s_waitcnt lgkmcnt(3)\n"
    "v_pk_add_f32 v[144:145], v[144:145], v[164:165]\n"
    "v_pk_add_f32 v[244:245], v[244:245], v[184:185]\n"
    "v_pk_add_f32 v[146:147], v[146:147], v[166:167]\n"
    "v_pk_add_f32 v[246:247], v[246:247], v[186:187]\n"
    "v_pk_add_f32 v[244:245], v[244:245], v[144:145]\n"
    "v_pk_add_f32 v[246:247], v[246:247], v[146:147]\n"
    "v_cvt_pk_bf16_f32 v14, v244, v245\n"
    "v_cvt_pk_bf16_f32 v15, v246, v247\n"
    "s_waitcnt lgkmcnt(0)\n"
    "v_pk_add_f32 v[148:149], v[148:149], v[168:169]\n"
    "v_pk_add_f32 v[248:249], v[248:249], v[188:189]\n"
    "v_pk_add_f32 v[150:151], v[150:151], v[170:171]\n"
    "v_pk_add_f32 v[250:251], v[250:251], v[190:191]\n"
    "v_pk_add_f32 v[248:249], v[248:249], v[148:149]\n"
    "v_pk_add_f32 v[250:251], v[250:251], v[150:151]\n"
    "v_cvt_pk_bf16_f32 v16, v248, v249\n"
    "v_cvt_pk_bf16_f32 v17, v250, v251\n"
    "buffer_store_dwordx4 v[12:15], v24, %[rs], %[hb] offen offset:1024 sc1\n"
    "v_permlane32_swap_b32 v16, v18\n"
    "s_nop 0\n"
    "v_permlane32_swap_b32 v17, v19\n"
    "s_mov_b32 exec_hi, 0\n"
    "buffer_store_dwordx4 v[16:19], v24, %[rs], %[hb] offen offset:2048 sc1\n"
    "s_mov_b32 exec_hi, -1\n"
    : "={v[8:15]}"(o), "={v[248:251]}"(rc), [wo] "+s"(wo), [wg] "+s"(wg), [n] "=&s"(n)
    : [rs] "s"(rs), [fl] "s"(fl), [wl] "s"(L_WR + wg_part(fb, hw)), [wst] "s"(((fb & 4) ? 13 : 14) * 2048), [o0] "s"(ob + (1 ^ hw) * 20480), [o1] "s"(ob + (2 ^ hw) * 20480), [o2] "s"(ob + (3 ^ hw) * 20480),
      [o3] "s"(ob + hw * 20480), [hb] "s"(WS_H + (rb * 4 + hw) * 20480 + cb * 2560), "{v[24:31]}"(p.v), "{v[20:23]}"(p.t), "{v[4:7]}"(p.c), [x] "s"(x), [hw] "s"(hw)
    : "vcc", "scc", "memory", "v16", "v17", "v18", "v19", "v32", "v33", "v34", "v35", "v36", "v37", "v38", "v39", "v40", "v41", "v42", "v43", "v44", "v45", "v46", "v47", "v48", "v49", "v50", "v51", "v52", "v53", "v54", "v55", "v56", "v57", "v58", "v59", "v60", "v61", "v62", "v63", "v64", "v65", "v66", "v67", "v68", "v69", "v70", "v71", "v72", "v73", "v74", "v75", "v76", "v77", "v78", "v79", "v80", "v81", "v82", "v83", "v84", "v85", "v86", "v87", "v88", "v89", "v90", "v91", "v92", "v93", "v94", "v95", "v96", "v97", "v98", "v99", "v100", "v101", "v102", "v103", "v104", "v105", "v106", "v107", "v108", "v109", "v110", "v111", "v112", "v113", "v114", "v115", "v116", "v117", "v118", "v119", "v120", "v121", "v122", "v123", "v124", "v125", "v126", "v127", "v128", "v129", "v130", "v131", "v132", "v133", "v134", "v135", "v136", "v137", "v138", "v139", "v140", "v141", "v142", "v143", "v144", "v145", "v146", "v147", "v148", "v149", "v150", "v151", "v152", "v153", "v154", "v155", "v156", "v157", "v158", "v159", "v160", "v161", "v162", "v163", "v164", "v165", "v166", "v167", "v168", "v169", "v170", "v171", "v172", "v173", "v174", "v175", "v176", "v177", "v178", "v179", "v180", "v181", "v182", "v183", "v184", "v185", "v186", "v187", "v188", "v189", "v190", "v191", "v192", "v193", "v194", "v195", "v196", "v197", "v198", "v199", "v200", "v201", "v202", "v203", "v204", "v205", "v206", "v207", "v208", "v209", "v210", "v211", "v212", "v213", "v214", "v215", "v216", "v217", "v218", "v219", "v220", "v221", "v222", "v223", "v224", "v225", "v226", "v227", "v228", "v229", "v230", "v231", "v232", "v233", "v234", "v235", "v236", "v237", "v238", "v239", "v240", "v241", "v242", "v243", "v244", "v245", "v246", "v247", "v252", "v253", "v254", "v255");
  const int lane = lane_id(), g3 = rb * 2 + (hw >> 1);
  const unsigned o8 = pack2(rc[0], rc[1]), o9 = pack2(rc[2], rc[3]);
  rc = f32x4{bflo(o8), bfhi(o8), bflo(o9), bfhi(o9)};
  float sm = (rc[0] + rc[1]) + (rc[2] + rc[3]), sq = fmaf(rc[0], rc[0], fmaf(rc[1], rc[1], fmaf(rc[2], rc[2], rc[3] * rc[3])));
#pragma unroll
  for (int i = 0; i < 8; ++i) { const float a = bflo(o[i]), c = bfhi(o[i]); sm += a + c; sq = fmaf(a, a, fmaf(c, c, sq)); }
  XSUM(16, sm, sq); XSUM(32, sm, sq);
  asm volatile("s_mov_b64 exec, 0xffff\n\tbuffer_store_dwordx2 %0, %1, %2, 0 offen sc1\n\ts_mov_b64 exec, 15\n\ts_waitcnt vmcnt(1)\n\tglobal_atomic_add %3, %4, %5 sc1\n\t"
               "s_mov_b64 exec, 16\n\ts_waitcnt vmcnt(1)\n\tglobal_atomic_add %3, %4, %5 sc1\n\ts_mov_b64 exec, -1"
               :: "v"(f32x2{sm, sq}), "v"(WS_ST + (rb * 4 + hw) * 1024 + cb * 128 + (lane & 15) * 8), "s"(rs),
                  "v"((lane < 4 ? C_F3 + lane * 64 + g3 : C_FQ + (g3 & 7) * 8 + (g3 >> 3)) * 256), "v"(1), "s"(fl) : "memory");
}
DEV u32x16 p5_setup(int rb, int cb, int tb, int fb, int wv) {
  const int g4 = tb * 4 + wv;
  const unsigned wst = ((fb & 4) ? 13 : 14) * 2048;
  u32x16 s = {WS_WGU + fb_t0(fb) * 20480 + wg_part(fb, wv) + 3 * wst, L_WR + wg_part(fb, wv), wst, l_box(wv), WS_H + (tb * 8 + wv * 2) * 20480, wd_base(cb, wv),
              WS_A + (rb * 216 + (27 * (wv >> 1) + 14 * (wv & 1)) * 4) * 1024, WS_H + (rb * 4 + wv) * 20480 + cb * 2560, (C_F3 + (fb & 3) * 64 + g4) * 256,
              (C_FQ + (g4 & 7) * 8 + (g4 >> 3)) * 256, (C_F4 + (cb & 1) * 128 + rb * 4 + wv) * 256, (unsigned)wv, 30720u + wv * 3072, wv * 7168u, 6 * L_OCT + wv * 8192, (wv & 1) - 1u};
  asm volatile("" : "+{s[36:51]}"(s));
  return s;
}
DEV void p5_compute(char* ws, char* out, int head, int qb, int rb, int cb, int tb, u32x16 s, int wv, int lane, const Stash& st) {
  const int r = lane & 15, g = lane >> 4;
  const unsigned v16 = lane * 16;
  const u32x8 v = {v16, v16 + 4096, v16 + 20480 - 2048, WS_ST + (tb * 8 + wv * 2 + (g & 1)) * 1024 + (g >> 1) * 512 + r * 8, L_MISC + (wv * 32 + (lane & 31)) * 8,
                   (lane & 31) * 16u + (lane >> 5) * 8, (rb * 64 + wv * 16 + r) * 1280u + cb * 160 + g * 16, (C_F2 + lane * 128 + (qb * 4 + wv) * 4 + (head >> 3)) * 256};
  int cnt;
  asm volatile(
    "s_barrier\n"
    "v_mov_b32 v252, 1\n"
    "s_mov_b64 exec, 3\n"
    "s_waitcnt vmcnt(0)\n"
    "global_atomic_add v15, v252, %[fl] sc1\n"
    "s_mov_b64 exec, -1\n"
    "buffer_load_dwordx4 v[72:75], v8, %[rs], s41 offen offset:0 sc1\n"
    "buffer_load_dwordx4 v[76:79], v8, %[rs], s41 offen offset:1024 sc1\n"
    "buffer_load_dwordx4 v[80:83], v8, %[rs], s41 offen offset:2048 sc1\n"
    "buffer_load_dwordx4 v[84:87], v8, %[rs], s41 offen offset:3072 sc1\n"
    "buffer_load_dwordx4 v[88:91], v9, %[rs], s41 offen offset:0 sc1\n"
    "buffer_load_dwordx4 v[92:95], v9, %[rs], s41 offen offset:1024 sc1\n"
    "buffer_load_dwordx4 v[96:99], v9, %[rs], s41 offen offset:2048 sc1\n"
    "buffer_load_dwordx4 v[100:103], v9, %[rs], s41 offen offset:3072 sc1\n"
    "s_add_u32 s41, s41, 8192\n"
    "buffer_load_dwordx4 v[104:107], v8, %[rs], s41 offen offset:0 sc1\n"
    "buffer_load_dwordx4 v[108:111], v8, %[rs], s41 offen offset:1024 sc1\n"
    "buffer_load_dwordx4 v[112:115], v8, %[rs], s41 offen offset:2048 sc1\n"
    "buffer_load_dwordx4 v[116:119], v8, %[rs], s41 offen offset:3072 sc1\n"
    "buffer_load_dwordx4 v[120:123], v9, %[rs], s41 offen offset:0 sc1\n"
    "buffer_load_dwordx4 v[124:127], v9, %[rs], s41 offen offset:1024 sc1\n"
    "buffer_load_dwordx4 v[128:131], v9, %[rs], s41 offen offset:2048 sc1\n"
    "s_add_u32 s41, s41, 37888\n"
    "buffer_load_dwordx4 v[24:27], v8, %[rs], s41 offen offset:0 sc1\n"
    "buffer_load_dwordx4 v[28:31], v8, %[rs], s41 offen offset:1024 sc1\n"
    "buffer_load_dwordx4 v[32:35], v8, %[rs], s41 offen offset:2048 sc1\n"
    "s_add_u32 s41, s41, 12288\n"
    "s_add_u32 m0, s48, 0\n"
    "s_nop 0\n"
    "buffer_load_dwordx4 v8, %[rs], s41 offen offset:0 sc1 lds\n"
    "buffer_load_dwordx4 v8, %[rs], s41 offen offset:1024 sc1 lds\n"
    "buffer_load_dwordx4 v8, %[rs], s41 offen offset:2048 sc1 lds\n"
    "v_mov_b32 v15, s44\n"
    "s_barrier\n"
    "s_add_u32 s41, s41, -9216\n"
    "buffer_load_dwordx4 v[36:39], v8, %[rs], s41 offen offset:0 sc1\n"
    "buffer_load_dwordx4 v[40:43], v8, %[rs], s41 offen offset:1024 sc1\n"
    "buffer_load_dwordx4 v[44:47], v8, %[rs], s41 offen offset:2048 sc1\n"
    "buffer_load_dwordx4 v[48:51], v8, %[rs], s41 offen offset:3072 sc1\n"
    "buffer_load_dwordx4 v[52:55], v9, %[rs], s41 offen offset:0 sc1\n"
    "buffer_load_dwordx4 v[56:59], v9, %[rs], s41 offen offset:1024 sc1\n"
    "buffer_load_dwordx4 v[60:63], v9, %[rs], s41 offen offset:2048 sc1\n"
    "buffer_load_dwordx4 v[64:67], v9, %[rs], s41 offen offset:3072 sc1\n"
    "v_mov_b32 v255, 0\n"
    "s_mov_b32 %[cnt], 200000\n"
    "s_mov_b64 exec, 1\n"
    "global_atomic_add v252, v15, v255, %[fl] sc0 sc1\n"
    "s_sleep 4\n"
    "1:\n"
    "global_atomic_add v253, v15, v255, %[fl] sc0 sc1\n"
    "s_sleep 4\n"
    "s_waitcnt vmcnt(1)\n"
    "v_cmp_gt_u32 vcc, 16, v252\n"
    "s_cbranch_vccz 2f\n"
    "global_atomic_add v252, v15, v255, %[fl] sc0 sc1\n"
    "s_sleep 4\n"
    "s_waitcnt vmcnt(1)\n"
    "v_cmp_gt_u32 vcc, 16, v253\n"
    "s_cbranch_vccz 2f\n"
    "s_sub_u32 %[cnt], %[cnt], 1\n"
    "s_cbranch_scc0 1b\n"
    "2:\n"
    "s_mov_b64 exec, -1\n"
    "s_add_u32 m0, s39, 0\n"
    "s_nop 0\n"
    "buffer_load_dwordx4 v8, %[rs], s40 offen offset:0 sc1 lds\n"
    "buffer_load_dwordx4 v8, %[rs], s40 offen offset:1024 sc1 lds\n"
    "buffer_load_dwordx4 v10, %[rs], s40 offen offset:2048 sc1 lds\n"
    "buffer_load_dwordx4 v10, %[rs], s40 offen offset:3072 sc1 lds\n"
    "s_add_u32 s40, s40, 2048\n"
    "s_add_u32 m0, s39, 4096\n"
    "s_nop 0\n"
    "buffer_load_dwordx4 v8, %[rs], s40 offen offset:0 sc1 lds\n"
    "buffer_load_dwordx4 v8, %[rs], s40 offen offset:1024 sc1 lds\n"
    "buffer_load_dwordx4 v10, %[rs], s40 offen offset:2048 sc1 lds\n"
    "buffer_load_dwordx4 v10, %[rs], s40 offen offset:3072 sc1 lds\n"
    "s_add_u32 s40, s40, 2048\n"
    "s_add_u32 m0, s39, 8192\n"
    "s_nop 0\n"
    "buffer_load_dwordx4 v8, %[rs], s40 offen offset:0 sc1 lds\n"
    "buffer_load_dwordx4 v8, %[rs], s40 offen offset:1024 sc1 lds\n"
    "buffer_load_dwordx4 v10, %[rs], s40 offen offset:2048 sc1 lds\n"
    "buffer_load_dwordx4 v10, %[rs], s40 offen offset:3072 sc1 lds\n"
    "s_add_u32 s40, s40, 2048\n"
    "s_add_u32 s41, s41, 8192\n"
    "buffer_load_dwordx4 v[68:71], v8, %[rs], s41 offen offset:0 sc1\n"
    "s_add_u32 s41, s41, 8192\n"
    "buffer_load_dwordx4 v[16:19], v8, %[rs], s41 offen offset:0 sc1\n"
    "v_mov_b32 v15, s45\n"
    "s_waitcnt vmcnt(10)\n"
    "s_barrier\n"
    "s_waitcnt vmcnt(6)\n"
    "s_barrier\n"
    "s_add_u32 m0, s39, 0\n"
    "s_nop 0\n"
    "buffer_load_dwordx4 v8, %[rs], s40 offen offset:0 sc1 lds\n"
    "buffer_load_dwordx4 v8, %[rs], s40 offen offset:1024 sc1 lds\n"
    "buffer_load_dwordx4 v10, %[rs], s40 offen offset:2048 sc1 lds\n"
    "buffer_load_dwordx4 v10, %[rs], s40 offen offset:3072 sc1 lds\n"
    "s_add_u32 s40, s40, 2048\n"
    "s_add_u32 m0, s37, 0\n"
    "s_nop 0\n"
    "buffer_load_dwordx4 v8, %[rs], s36 offen offset:0 sc1 lds\n"
    "buffer_load_dwordx4 v8, %[rs], s36 offen offset:1024 sc1 lds\n"
    "buffer_load_dwordx4 v8, %[rs], s36 offen offset:2048 sc1 lds\n"
    "buffer_load_dwordx4 v8, %[rs], s36 offen offset:3072 sc1 lds\n"
    "s_add_u32 m0, s37, 4096\n"
    "s_nop 0\n"
    "buffer_load_dwordx4 v9, %[rs], s36 offen offset:0 sc1 lds\n"
    "buffer_load_dwordx4 v9, %[rs], s36 offen offset:1024 sc1 lds\n"
    "buffer_load_dwordx4 v9, %[rs], s36 offen offset:2048 sc1 lds\n"
    "s_add_u32 s36, s36, s38\n"
    "s_waitcnt vmcnt(13)\n"
    "s_barrier\n"
    "s_mov_b64 exec, 1\n"
    "global_atomic_add v252, v15, v255, %[fl] sc0 sc1\n"
    "s_mov_b64 exec, -1\n"
    "s_add_u32 m0, s39, 4096\n"
    "s_nop 0\n"
    "buffer_load_dwordx4 v8, %[rs], s40 offen offset:0 sc1 lds\n"
    "buffer_load_dwordx4 v8, %[rs], s40 offen offset:1024 sc1 lds\n"
    "buffer_load_dwordx4 v10, %[rs], s40 offen offset:2048 sc1 lds\n"
    "buffer_load_dwordx4 v10, %[rs], s40 offen offset:3072 sc1 lds\n"
    "s_add_u32 s40, s40, 2048\n"
    "s_add_u32 m0, s37, 28672\n"
    "s_nop 0\n"
    "buffer_load_dwordx4 v8, %[rs], s36 offen offset:0 sc1 lds\n"
    "buffer_load_dwordx4 v8, %[rs], s36 offen offset:1024 sc1 lds\n"
    "buffer_load_dwordx4 v8, %[rs], s36 offen offset:2048 sc1 lds\n"
    "buffer_load_dwordx4 v8, %[rs], s36 offen offset:3072 sc1 lds\n"
    "s_add_u32 m0, s37, 32768\n"
    "s_nop 0\n"
    "buffer_load_dwordx4 v9, %[rs], s36 offen offset:0 sc1 lds\n"
    "buffer_load_dwordx4 v9, %[rs], s36 offen offset:1024 sc1 lds\n"
    "buffer_load_dwordx4 v9, %[rs], s36 offen offset:2048 sc1 lds\n"
    "s_add_u32 s36, s36, s38\n"
    "s_waitcnt vmcnt(12)\n"
    "s_barrier\n"
    "s_add_u32 m0, s39, 8192\n"
    "s_nop 0\n"
    "buffer_load_dwordx4 v8, %[rs], s40 offen offset:0 sc1 lds\n"
    "buffer_load_dwordx4 v8, %[rs], s40 offen offset:1024 sc1 lds\n"
    "buffer_load_dwordx4 v10, %[rs], s40 offen offset:2048 sc1 lds\n"
    "buffer_load_dwordx4 v10, %[rs], s40 offen offset:3072 sc1 lds\n"
    "s_add_u32 s40, s40, 2048\n"
    "s_add_u32 m0, s37, 57344\n"
    "s_nop 0\n"
    "buffer_load_dwordx4 v8, %[rs], s36 offen offset:0 sc1 lds\n"
    "buffer_load_dwordx4 v8, %[rs], s36 offen offset:1024 sc1 lds\n"
    "buffer_load_dwordx4 v8, %[rs], s36 offen offset:2048 sc1 lds\n"
    "buffer_load_dwordx4 v8, %[rs], s36 offen offset:3072 sc1 lds\n"
    "s_add_u32 m0, s37, 61440\n"
    "s_nop 0\n"
    "buffer_load_dwordx4 v9, %[rs], s36 offen offset:0 sc1 lds\n"
    "buffer_load_dwordx4 v9, %[rs], s36 offen offset:1024 sc1 lds\n"
    "buffer_load_dwordx4 v9, %[rs], s36 offen offset:2048 sc1 lds\n"
    "s_add_u32 s36, s36, s38\n"
    "s_waitcnt vmcnt(11)\n"
    "s_barrier\n"
    "s_add_u32 m0, s39, 0\n"
    "s_nop 0\n"
    "buffer_load_dwordx4 v8, %[rs], s40 offen offset:0 sc1 lds\n"
    "buffer_load_dwordx4 v8, %[rs], s40 offen offset:1024 sc1 lds\n"
    "buffer_load_dwordx4 v10, %[rs], s40 offen offset:2048 sc1 lds\n"
    "buffer_load_dwordx4 v10, %[rs], s40 offen offset:3072 sc1 lds\n"
    "s_add_u32 s40, s40, 2048\n"
    "s_add_u32 m0, s37, 0\n"
    "s_nop 0\n"
    "buffer_load_dwordx4 v8, %[rs], s36 offen offset:0 sc1 lds\n"
    "buffer_load_dwordx4 v8, %[rs], s36 offen offset:1024 sc1 lds\n"
    "buffer_load_dwordx4 v8, %[rs], s36 offen offset:2048 sc1 lds\n"
    "buffer_load_dwordx4 v8, %[rs], s36 offen offset:3072 sc1 lds\n"
    "s_add_u32 m0, s37, 4096\n"
    "s_nop 0\n"
    "buffer_load_dwordx4 v9, %[rs], s36 offen offset:0 sc1 lds\n"
    "buffer_load_dwordx4 v9, %[rs], s36 offen offset:1024 sc1 lds\n"
    "buffer_load_dwordx4 v9, %[rs], s36 offen offset:2048 sc1 lds\n"
    "s_add_u32 s36, s36, s38\n"
    "s_waitcnt vmcnt(11)\n"
    "s_barrier\n"
    "s_add_u32 m0, s39, 4096\n"
    "s_nop 0\n"
    "buffer_load_dwordx4 v8, %[rs], s40 offen offset:0 sc1 lds\n"
    "buffer_load_dwordx4 v8, %[rs], s40 offen offset:1024 sc1 lds\n"
    "buffer_load_dwordx4 v10, %[rs], s40 offen offset:2048 sc1 lds\n"
    "buffer_load_dwordx4 v10, %[rs], s40 offen offset:3072 sc1 lds\n"
    "s_add_u32 s40, s40, 2048\n"
    "s_add_u32 m0, s37, 28672\n"
    "s_nop 0\n"
    "buffer_load_dwordx4 v8, %[rs], s36 offen offset:0 sc1 lds\n"
    "buffer_load_dwordx4 v8, %[rs], s36 offen offset:1024 sc1 lds\n"
    "buffer_load_dwordx4 v8, %[rs], s36 offen offset:2048 sc1 lds\n"
    "buffer_load_dwordx4 v8, %[rs], s36 offen offset:3072 sc1 lds\n"
    "s_add_u32 m0, s37, 32768\n"
    "s_nop 0\n"
    "buffer_load_dwordx4 v9, %[rs], s36 offen offset:0 sc1 lds\n"
    "buffer_load_dwordx4 v9, %[rs], s36 offen offset:1024 sc1 lds\n"
    "buffer_load_dwordx4 v9, %[rs], s36 offen offset:2048 sc1 lds\n"
    "s_add_u32 s36, s36, s38\n"
    "s_mov_b64 exec, 1\n"
    "3:\n"
    "v_cmp_gt_u32 vcc, 24, v252\n"
    "s_cbranch_vccz 4f\n"
    "global_atomic_add v252, v15, v255, %[fl] sc0 sc1\n"
    "s_waitcnt vmcnt(0)\n"
    "s_sub_u32 %[cnt], %[cnt], 1\n"
    "s_cbranch_scc0 3b\n"
    "4:\n"
    "s_mov_b64 exec, -1\n"
    "buffer_load_dwordx2 v[0:1], v11, %[rs], 0 offen offset:0 sc1\n"
    "buffer_load_dwordx2 v[2:3], v11, %[rs], 0 offen offset:128 sc1\n"
    "buffer_load_dwordx2 v[4:5], v11, %[rs], 0 offen offset:256 sc1\n"
    "buffer_load_dwordx2 v[6:7], v11, %[rs], 0 offen offset:384 sc1\n"
    "s_waitcnt vmcnt(15)\n"
    "s_barrier\n"
    "s_add_u32 m0, s39, 8192\n"
    "s_nop 0\n"
    "buffer_load_dwordx4 v8, %[rs], s40 offen offset:0 sc1 lds\n"
    "buffer_load_dwordx4 v8, %[rs], s40 offen offset:1024 sc1 lds\n"
    "buffer_load_dwordx4 v10, %[rs], s40 offen offset:2048 sc1 lds\n"
    "buffer_load_dwordx4 v10, %[rs], s40 offen offset:3072 sc1 lds\n"
    "s_add_u32 s40, s40, 2048\n"
    "s_add_u32 m0, s37, 57344\n"
    "s_nop 0\n"
    "buffer_load_dwordx4 v8, %[rs], s36 offen offset:0 sc1 lds\n"
    "buffer_load_dwordx4 v8, %[rs], s36 offen offset:1024 sc1 lds\n"
    "buffer_load_dwordx4 v8, %[rs], s36 offen offset:2048 sc1 lds\n"
    "buffer_load_dwordx4 v8, %[rs], s36 offen offset:3072 sc1 lds\n"
    "s_add_u32 m0, s37, 61440\n"
    "s_nop 0\n"
    "buffer_load_dwordx4 v9, %[rs], s36 offen offset:0 sc1 lds\n"
    "buffer_load_dwordx4 v9, %[rs], s36 offen offset:1024 sc1 lds\n"
    "buffer_load_dwordx4 v9, %[rs], s36 offen offset:2048 sc1 lds\n"
    "s_add_u32 s36, s36, s38\n"
    "s_waitcnt vmcnt(11)\n"
    "s_barrier\n"
    "s_add_u32 m0, s39, 0\n"
    "s_nop 0\n"
    "buffer_load_dwordx4 v8, %[rs], s40 offen offset:0 sc1 lds\n"
    "buffer_load_dwordx4 v8, %[rs], s40 offen offset:1024 sc1 lds\n"
    "buffer_load_dwordx4 v10, %[rs], s40 offen offset:2048 sc1 lds\n"
    "buffer_load_dwordx4 v10, %[rs], s40 offen offset:3072 sc1 lds\n"
    "s_add_u32 s40, s40, 2048\n"
    "s_add_u32 m0, s37, 0\n"
    "s_nop 0\n"
    "buffer_load_dwordx4 v8, %[rs], s36 offen offset:0 sc1 lds\n"
    "buffer_load_dwordx4 v8, %[rs], s36 offen offset:1024 sc1 lds\n"
    "buffer_load_dwordx4 v8, %[rs], s36 offen offset:2048 sc1 lds\n"
    "buffer_load_dwordx4 v8, %[rs], s36 offen offset:3072 sc1 lds\n"
    "s_add_u32 m0, s37, 4096\n"
    "s_nop 0\n"
    "buffer_load_dwordx4 v9, %[rs], s36 offen offset:0 sc1 lds\n"
    "buffer_load_dwordx4 v9, %[rs], s36 offen offset:1024 sc1 lds\n"
    "buffer_load_dwordx4 v9, %[rs], s36 offen offset:2048 sc1 lds\n"
    "s_add_u32 s36, s36, s38\n"
    "v_add_f32 v0, v0, v2\n"
    "v_add_f32 v1, v1, v3\n"
    "v_add_f32 v4, v4, v6\n"
    "v_add_f32 v5, v5, v7\n"
    "v_add_f32 v0, v0, v4\n"
    "v_add_f32 v1, v1, v5\n"
    "v_mov_b32 v2, v0\n"
    "v_mov_b32 v3, v1\n"
    "s_nop 0\n"
    "v_permlane32_swap_b32 v0, v2\n"
    "v_permlane32_swap_b32 v1, v3\n"
    "v_add_f32 v0, v0, v2\n"
    "v_add_f32 v1, v1, v3\n"
    "v_mul_f32 v0, 0x3acccccd, v0\n"
    "v_mul_f32 v1, 0x3acccccd, v1\n"
    "v_fma_f32 v1, -v0, v0, v1\n"
    "v_max_f32 v1, 0, v1\n"
    "v_cvt_pk_bf16_f32 v1, v1, v1\n"
    "v_lshlrev_b32 v1, 16, v1\n"
    "v_add_f32 v1, 0x3727c5ac, v1\n"
    "v_cvt_pk_bf16_f32 v1, v1, v1\n"
    "v_lshlrev_b32 v1, 16, v1\n"
    "v_sqrt_f32 v1, v1\n"
    "v_cvt_pk_bf16_f32 v0, v0, v0\n"
    "v_lshlrev_b32 v0, 16, v0\n"
    "v_cvt_pk_bf16_f32 v1, v1, v1\n"
    "v_lshlrev_b32 v1, 16, v1\n"
    "v_rcp_f32 v1, v1\n"
    "v_xor_b32 v0, 0x80000000, v0\n"
    "v_mul_f32 v1, v1, v1\n"
    "ds_write_b64 v12, v[0:1]\n"
    "s_waitcnt lgkmcnt(0)\n"
    "s_waitcnt vmcnt(11)\n"
    "s_barrier\n"
    "global_load_dword v252, v14, %[out] offset:0\n"
    "global_load_dword v253, v14, %[out] offset:64\n"
    "global_load_dword v254, v14, %[out] offset:96\n"
    "v_add_u32 v0, s48, v8\n"
    "v_add_u32 v1, s49, v8\n"
    "v_add_u32 v6, s50, v8\n"
    "v_add_u32 v2, 43008, v1\n"
    "v_mov_b32 v15, s46\n"
    "s_xor_b32 %[cnt], s47, 1\n"
    "s_mul_i32 %[cnt], %[cnt], 7168\n"
    "s_add_u32 %[cnt], %[cnt], 43008\n"
    "v_add_u32 v3, %[cnt], v8\n"
    "s_xor_b32 %[cnt], s47, 2\n"
    "s_mul_i32 %[cnt], %[cnt], 7168\n"
    "s_add_u32 %[cnt], %[cnt], 43008\n"
    "v_add_u32 v4, %[cnt], v8\n"
    "s_xor_b32 %[cnt], s47, 3\n"
    "s_mul_i32 %[cnt], %[cnt], 7168\n"
    "s_add_u32 %[cnt], %[cnt], 43008\n"
    "v_add_u32 v5, %[cnt], v8\n"
    "s_waitcnt vmcnt(3)\n"
    "s_barrier\n"
    "s_barrier\n"
    "s_add_u32 s41, s41, -4096\n"
    "s_add_u32 m0, s49, 3072\n"
    "s_nop 0\n"
    "buffer_load_dwordx4 v8, %[rs], s41 offen offset:0 sc1 lds\n"
    "buffer_load_dwordx4 v8, %[rs], s41 offen offset:1024 sc1 lds\n"
    "buffer_load_dwordx4 v8, %[rs], s41 offen offset:2048 sc1 lds\n"
    "buffer_load_dwordx4 v8, %[rs], s41 offen offset:3072 sc1 lds\n"
    "s_waitcnt vmcnt(4)\n"
    "ds_write_b128 v2, v[192:195] offset:33792\n"
    "ds_write_b128 v2, v[196:199] offset:34816\n"
    "ds_write_b128 v2, v[200:203] offset:57344\n"
    "ds_write_b128 v2, v[204:207] offset:58368\n"
    "ds_write_b128 v2, v[208:211] offset:59392\n"
    "ds_write_b128 v2, v[212:215] offset:60416\n"
    "ds_write_b128 v2, v[216:219] offset:61440\n"
    "ds_write_b128 v2, v[220:223] offset:62464\n"
    "s_waitcnt lgkmcnt(5)\n"
    "buffer_load_dwordx4 v[192:195], v8, %[rs], s43 offen offset:0 sc1\n"
    "buffer_load_dwordx4 v[196:199], v8, %[rs], s43 offen offset:1024 sc1\n"
    "buffer_load_dwordx2 v[200:201], v13, %[rs], s43 offen offset:2048 sc1\n"
    "ds_write_b128 v2, v[172:175] offset:28672\n"
    "ds_write_b128 v2, v[176:179] offset:29696\n"
    "ds_write_b128 v2, v[180:183] offset:30720\n"
    "ds_write_b128 v2, v[184:187] offset:31744\n"
    "ds_write_b128 v2, v[188:191] offset:32768\n"
    "ds_write_b128 v2, v[224:227] offset:63488\n"
    "ds_write_b128 v2, v[228:231] offset:0\n"
    "ds_write_b128 v2, v[232:235] offset:1024\n"
    "ds_write_b128 v2, v[236:239] offset:2048\n"
    "ds_write_b128 v2, v[240:243] offset:3072\n"
    "ds_write_b128 v2, v[244:247] offset:4096\n"
    "ds_write_b128 v2, v[248:251] offset:5120\n"
    "ds_write_b128 v2, v[24:27] offset:6144\n"
    "ds_write_b128 v6, v[28:31] offset:0\n"
    "ds_write_b128 v6, v[32:35] offset:1024\n"
    "ds_write_b128 v6, v[36:39] offset:2048\n"
    "ds_write_b128 v6, v[40:43] offset:3072\n"
    "ds_write_b128 v6, v[44:47] offset:4096\n"
    "ds_write_b128 v6, v[48:51] offset:5120\n"
    "ds_write_b128 v6, v[52:55] offset:6144\n"
    "ds_write_b128 v6, v[56:59] offset:7168\n"
    "ds_write_b128 v1, v[60:63] offset:0\n"
    "ds_write_b128 v1, v[64:67] offset:1024\n"
    "ds_write_b128 v1, v[68:71] offset:2048\n"
    "s_waitcnt vmcnt(0) lgkmcnt(15)\n"
    "v_lshlrev_b32 v172, 16, v192\n"
    "v_and_b32 v173, 0xffff0000, v192\n"
    "v_lshlrev_b32 v174, 16, v193\n"
    "v_and_b32 v175, 0xffff0000, v193\n"
    "v_lshlrev_b32 v176, 16, v194\n"
    "v_and_b32 v177, 0xffff0000, v194\n"
    "v_lshlrev_b32 v178, 16, v195\n"
    "v_and_b32 v179, 0xffff0000, v195\n"
    "v_lshlrev_b32 v180, 16, v196\n"
    "v_and_b32 v181, 0xffff0000, v196\n"
    "v_lshlrev_b32 v182, 16, v197\n"
    "v_and_b32 v183, 0xffff0000, v197\n"
    "v_lshlrev_b32 v184, 16, v198\n"
    "v_and_b32 v185, 0xffff0000, v198\n"
    "v_lshlrev_b32 v186, 16, v199\n"
    "v_and_b32 v187, 0xffff0000, v199\n"
    "v_lshlrev_b32 v188, 16, v200\n"
    "v_and_b32 v189, 0xffff0000, v200\n"
    "v_lshlrev_b32 v190, 16, v201\n"
    "v_and_b32 v191, 0xffff0000, v201\n"
    "s_add_u32 s41, s41, 5120\n"
    "s_waitcnt lgkmcnt(0)\n"
    "v_mov_b32 v255, 0\n"
    "s_mov_b32 %[cnt], 200000\n"
    "s_mov_b64 exec, 1\n"
    "global_atomic_add v252, v15, v255, %[fl] sc0 sc1\n"
    "s_sleep 4\n"
    "5:\n"
    "global_atomic_add v253, v15, v255, %[fl] sc0 sc1\n"
    "s_sleep 4\n"
    "s_waitcnt vmcnt(1)\n"
    "v_cmp_gt_u32 vcc, 8, v252\n"
    "s_cbranch_vccz 6f\n"
    "global_atomic_add v252, v15, v255, %[fl] sc0 sc1\n"
    "s_sleep 4\n"
    "s_waitcnt vmcnt(1)\n"
    "v_cmp_gt_u32 vcc, 8, v253\n"
    "s_cbranch_vccz 6f\n"
    "s_sub_u32 %[cnt], %[cnt], 1\n"
    "s_cbranch_scc0 5b\n"
    "6:\n"
    "s_mov_b64 exec, -1\n"
    "buffer_load_dwordx4 v[24:27], v8, %[rs], s42 offen offset:0 sc1\n"
    "buffer_load_dwordx4 v[28:31], v8, %[rs], s42 offen offset:1024 sc1\n"
    "buffer_load_dwordx4 v[32:35], v8, %[rs], s42 offen offset:2048 sc1\n"
    "buffer_load_dwordx4 v[36:39], v8, %[rs], s42 offen offset:3072 sc1\n"
    "s_add_u32 s42, s42, 4096\n"
    "buffer_load_dwordx4 v[40:43], v8, %[rs], s42 offen offset:0 sc1\n"
    "buffer_load_dwordx4 v[44:47], v8, %[rs], s42 offen offset:1024 sc1\n"
    "buffer_load_dwordx4 v[48:51], v8, %[rs], s42 offen offset:2048 sc1\n"
    "buffer_load_dwordx4 v[52:55], v8, %[rs], s42 offen offset:3072 sc1\n"
    "s_add_u32 s42, s42, 4096\n"
    "buffer_load_dwordx4 v[56:59], v8, %[rs], s42 offen offset:0 sc1\n"
    "buffer_load_dwordx4 v[60:63], v8, %[rs], s42 offen offset:1024 sc1\n"
    "buffer_load_dwordx4 v[64:67], v8, %[rs], s42 offen offset:2048 sc1\n"
    "buffer_load_dwordx4 v[68:71], v8, %[rs], s42 offen offset:3072 sc1\n"
    "s_add_u32 s42, s42, 4096\n"
    "s_waitcnt vmcnt(8)\n"
    "v_swap_b32 v25, v28\n"
    "v_swap_b32 v26, v32\n"
    "v_swap_b32 v27, v36\n"
    "v_swap_b32 v30, v33\n"
    "v_swap_b32 v31, v37\n"
    "v_swap_b32 v35, v38\n"
    "s_nop 0\n"
    "v_mfma_f32_16x16x32_bf16 v[172:175], v[72:75], v[24:27], v[172:175]\n"
    "v_mfma_f32_16x16x32_bf16 v[176:179], v[76:79], v[24:27], v[176:179]\n"
    "v_mfma_f32_16x16x32_bf16 v[180:183], v[80:83], v[24:27], v[180:183]\n"
    "v_mfma_f32_16x16x32_bf16 v[184:187], v[84:87], v[24:27], v[184:187]\n"
    "v_mfma_f32_16x16x32_bf16 v[188:191], v[88:91], v[24:27], v[188:191]\n"
    "buffer_load_dwordx4 v[24:27], v8, %[rs], s42 offen offset:0 sc1\n"
    "v_mfma_f32_16x16x32_bf16 v[208:211], v[88:91], v[28:31], 0\n"
    "v_mfma_f32_16x16x32_bf16 v[204:207], v[84:87], v[28:31], 0\n"
    "v_mfma_f32_16x16x32_bf16 v[200:203], v[80:83], v[28:31], 0\n"
    "v_mfma_f32_16x16x32_bf16 v[196:199], v[76:79], v[28:31], 0\n"
    "v_mfma_f32_16x16x32_bf16 v[192:195], v[72:75], v[28:31], 0\n"
    "buffer_load_dwordx4 v[28:31], v8, %[rs], s42 offen offset:1024 sc1\n"
    "v_mfma_f32_16x16x32_bf16 v[212:215], v[72:75], v[32:35], 0\n"
    "v_mfma_f32_16x16x32_bf16 v[216:219], v[76:79], v[32:35], 0\n"
    "s_waitcnt vmcnt(6)\n"
    "v_swap_b32 v41, v44\n"
    "v_mfma_f32_16x16x32_bf16 v[220:223], v[80:83], v[32:35], 0\n"
    "v_swap_b32 v42, v48\n"
    "v_mfma_f32_16x16x32_bf16 v[224:227], v[84:87], v[32:35], 0\n"
    "v_swap_b32 v43, v52\n"
    "v_mfma_f32_16x16x32_bf16 v[228:231], v[88:91], v[32:35], 0\n"
    "buffer_load_dwordx4 v[32:35], v8, %[rs], s42 offen offset:2048 sc1\n"
    "v_swap_b32 v46, v49\n"
    "v_mfma_f32_16x16x32_bf16 v[248:251], v[88:91], v[36:39], 0\n"
    "v_swap_b32 v47, v53\n"
    "v_mfma_f32_16x16x32_bf16 v[244:247], v[84:87], v[36:39], 0\n"
    "v_swap_b32 v51, v54\n"
    "v_mfma_f32_16x16x32_bf16 v[240:243], v[80:83], v[36:39], 0\n"
    "v_mfma_f32_16x16x32_bf16 v[236:239], v[76:79], v[36:39], 0\n"
    "v_mfma_f32_16x16x32_bf16 v[232:235], v[72:75], v[36:39], 0\n"
    "buffer_load_dwordx4 v[36:39], v8, %[rs], s42 offen offset:3072 sc1\n"
    "s_add_u32 s42, s42, 4096\n"
    "v_mfma_f32_16x16x32_bf16 v[172:175], v[92:95], v[40:43], v[172:175]\n"
    "v_mfma_f32_16x16x32_bf16 v[176:179], v[96:99], v[40:43], v[176:179]\n"
    "v_mfma_f32_16x16x32_bf16 v[180:183], v[100:103], v[40:43], v[180:183]\n"
    "v_mfma_f32_16x16x32_bf16 v[184:187], v[104:107], v[40:43], v[184:187]\n"
    "v_mfma_f32_16x16x32_bf16 v[188:191], v[108:111], v[40:43], v[188:191]\n"
    "buffer_load_dwordx4 v[40:43], v8, %[rs], s42 offen offset:0 sc1\n"
    "v_mfma_f32_16x16x32_bf16 v[208:211], v[108:111], v[44:47], v[208:211]\n"
    "v_mfma_f32_16x16x32_bf16 v[204:207], v[104:107], v[44:47], v[204:207]\n"
    "v_mfma_f32_16x16x32_bf16 v[200:203], v[100:103], v[44:47], v[200:203]\n"
    "v_mfma_f32_16x16x32_bf16 v[196:199], v[96:99], v[44:47], v[196:199]\n"
    "v_mfma_f32_16x16x32_bf16 v[192:195], v[92:95], v[44:47], v[192:195]\n"
    "buffer_load_dwordx4 v[44:47], v8, %[rs], s42 offen offset:1024 sc1\n"
    "v_mfma_f32_16x16x32_bf16 v[212:215], v[92:95], v[48:51], v[212:215]\n"
    "v_mfma_f32_16x16x32_bf16 v[216:219], v[96:99], v[48:51], v[216:219]\n"
    "s_waitcnt vmcnt(6)\n"
    "v_swap_b32 v57, v60\n"
    "v_mfma_f32_16x16x32_bf16 v[220:223], v[100:103], v[48:51], v[220:223]\n"
    "v_swap_b32 v58, v64\n"
    "v_mfma_f32_16x16x32_bf16 v[224:227], v[104:107], v[48:51], v[224:227]\n"
    "v_swap_b32 v59, v68\n"
    "v_mfma_f32_16x16x32_bf16 v[228:231], v[108:111], v[48:51], v[228:231]\n"
    "buffer_load_dwordx4 v[48:51], v8, %[rs], s42 offen offset:2048 sc1\n"
    "v_swap_b32 v62, v65\n"
    "v_mfma_f32_16x16x32_bf16 v[248:251], v[108:111], v[52:55], v[248:251]\n"
    "v_swap_b32 v63, v69\n"
    "v_mfma_f32_16x16x32_bf16 v[244:247], v[104:107], v[52:55], v[244:247]\n"
    "v_swap_b32 v67, v70\n"
    "v_mfma_f32_16x16x32_bf16 v[240:243], v[100:103], v[52:55], v[240:243]\n"
    "v_mfma_f32_16x16x32_bf16 v[236:239], v[96:99], v[52:55], v[236:239]\n"
    "v_mfma_f32_16x16x32_bf16 v[232:235], v[92:95], v[52:55], v[232:235]\n"
    "buffer_load_dwordx4 v[52:55], v8, %[rs], s42 offen offset:3072 sc1\n"
    "s_add_u32 s42, s42, 4096\n"
    "v_mfma_f32_16x16x32_bf16 v[172:175], v[112:115], v[56:59], v[172:175]\n"
    "v_mfma_f32_16x16x32_bf16 v[176:179], v[116:119], v[56:59], v[176:179]\n"
    "v_mfma_f32_16x16x32_bf16 v[180:183], v[120:123], v[56:59], v[180:183]\n"
    "v_mfma_f32_16x16x32_bf16 v[184:187], v[124:127], v[56:59], v[184:187]\n"
    "v_mfma_f32_16x16x32_bf16 v[188:191], v[128:131], v[56:59], v[188:191]\n"
    "buffer_load_dwordx4 v[56:59], v8, %[rs], s42 offen offset:0 sc1\n"
    "v_mfma_f32_16x16x32_bf16 v[208:211], v[128:131], v[60:63], v[208:211]\n"
    "v_mfma_f32_16x16x32_bf16 v[204:207], v[124:127], v[60:63], v[204:207]\n"
    "v_mfma_f32_16x16x32_bf16 v[200:203], v[120:123], v[60:63], v[200:203]\n"
    "v_mfma_f32_16x16x32_bf16 v[196:199], v[116:119], v[60:63], v[196:199]\n"
    "v_mfma_f32_16x16x32_bf16 v[192:195], v[112:115], v[60:63], v[192:195]\n"
    "buffer_load_dwordx4 v[60:63], v8, %[rs], s42 offen offset:1024 sc1\n"
    "v_mfma_f32_16x16x32_bf16 v[212:215], v[112:115], v[64:67], v[212:215]\n"
    "v_mfma_f32_16x16x32_bf16 v[216:219], v[116:119], v[64:67], v[216:219]\n"
    "s_waitcnt vmcnt(6)\n"
    "v_swap_b32 v25, v28\n"
    "v_mfma_f32_16x16x32_bf16 v[220:223], v[120:123], v[64:67], v[220:223]\n"
    "v_swap_b32 v26, v32\n"
    "v_mfma_f32_16x16x32_bf16 v[224:227], v[124:127], v[64:67], v[224:227]\n"
    "v_swap_b32 v27, v36\n"
    "v_mfma_f32_16x16x32_bf16 v[228:231], v[128:131], v[64:67], v[228:231]\n"
    "buffer_load_dwordx4 v[64:67], v8, %[rs], s42 offen offset:2048 sc1\n"
    "v_swap_b32 v30, v33\n"
    "v_mfma_f32_16x16x32_bf16 v[248:251], v[128:131], v[68:71], v[248:251]\n"
    "v_swap_b32 v31, v37\n"
    "v_mfma_f32_16x16x32_bf16 v[244:247], v[124:127], v[68:71], v[244:247]\n"
    "v_swap_b32 v35, v38\n"
    "v_mfma_f32_16x16x32_bf16 v[240:243], v[120:123], v[68:71], v[240:243]\n"
    "v_mfma_f32_16x16x32_bf16 v[236:239], v[116:119], v[68:71], v[236:239]\n"
    "v_mfma_f32_16x16x32_bf16 v[232:235], v[112:115], v[68:71], v[232:235]\n"
    "buffer_load_dwordx4 v[68:71], v8, %[rs], s42 offen offset:3072 sc1\n"
    "s_add_u32 s42, s42, 4096\n"
    "v_mfma_f32_16x16x32_bf16 v[172:175], v[132:135], v[24:27], v[172:175]\n"
    "v_mfma_f32_16x16x32_bf16 v[176:179], v[136:139], v[24:27], v[176:179]\n"
    "v_mfma_f32_16x16x32_bf16 v[180:183], v[140:143], v[24:27], v[180:183]\n"
    "v_mfma_f32_16x16x32_bf16 v[184:187], v[144:147], v[24:27], v[184:187]\n"
    "v_mfma_f32_16x16x32_bf16 v[188:191], v[148:151], v[24:27], v[188:191]\n"
    "buffer_load_dwordx4 v[24:27], v8, %[rs], s42 offen offset:0 sc1\n"
    "v_mfma_f32_16x16x32_bf16 v[208:211], v[148:151], v[28:31], v[208:211]\n"
    "v_mfma_f32_16x16x32_bf16 v[204:207], v[144:147], v[28:31], v[204:207]\n"
    "v_mfma_f32_16x16x32_bf16 v[200:203], v[140:143], v[28:31], v[200:203]\n"
    "v_mfma_f32_16x16x32_bf16 v[196:199], v[136:139], v[28:31], v[196:199]\n"
    "v_mfma_f32_16x16x32_bf16 v[192:195], v[132:135], v[28:31], v[192:195]\n"
    "buffer_load_dwordx4 v[28:31], v8, %[rs], s42 offen offset:1024 sc1\n"
    "v_mfma_f32_16x16x32_bf16 v[212:215], v[132:135], v[32:35], v[212:215]\n"
    "v_mfma_f32_16x16x32_bf16 v[216:219], v[136:139], v[32:35], v[216:219]\n"
    "s_waitcnt vmcnt(6)\n"
    "v_swap_b32 v41, v44\n"
    "v_mfma_f32_16x16x32_bf16 v[220:223], v[140:143], v[32:35], v[220:223]\n"
    "v_swap_b32 v42, v48\n"
    "v_mfma_f32_16x16x32_bf16 v[224:227], v[144:147], v[32:35], v[224:227]\n"
    "v_swap_b32 v43, v52\n"
    "v_mfma_f32_16x16x32_bf16 v[228:231], v[148:151], v[32:35], v[228:231]\n"
    "buffer_load_dwordx4 v[32:35], v8, %[rs], s42 offen offset:2048 sc1\n"
    "v_swap_b32 v46, v49\n"
    "v_mfma_f32_16x16x32_bf16 v[248:251], v[148:151], v[36:39], v[248:251]\n"
    "v_swap_b32 v47, v53\n"
    "v_mfma_f32_16x16x32_bf16 v[244:247], v[144:147], v[36:39], v[244:247]\n"
    "v_swap_b32 v51, v54\n"
    "v_mfma_f32_16x16x32_bf16 v[240:243], v[140:143], v[36:39], v[240:243]\n"
    "v_mfma_f32_16x16x32_bf16 v[236:239], v[136:139], v[36:39], v[236:239]\n"
    "v_mfma_f32_16x16x32_bf16 v[232:235], v[132:135], v[36:39], v[232:235]\n"
    "buffer_load_dwordx4 v[36:39], v8, %[rs], s42 offen offset:3072 sc1\n"
    "s_add_u32 s42, s42, 4096\n"
    "v_mfma_f32_16x16x32_bf16 v[172:175], v[152:155], v[40:43], v[172:175]\n"
    "ds_read_b128 v[72:75], v2 offset:28672\n"
    "v_mfma_f32_16x16x32_bf16 v[176:179], v[156:159], v[40:43], v[176:179]\n"
    "v_mfma_f32_16x16x32_bf16 v[180:183], v[160:163], v[40:43], v[180:183]\n"
    "ds_read_b128 v[76:79], v2 offset:29696\n"
    "v_mfma_f32_16x16x32_bf16 v[184:187], v[164:167], v[40:43], v[184:187]\n"
    "v_mfma_f32_16x16x32_bf16 v[188:191], v[168:171], v[40:43], v[188:191]\n"
    "buffer_load_dwordx4 v[40:43], v8, %[rs], s42 offen offset:0 sc1\n"
    "ds_read_b128 v[80:83], v2 offset:30720\n"
    "v_mfma_f32_16x16x32_bf16 v[208:211], v[168:171], v[44:47], v[208:211]\n"
    "v_mfma_f32_16x16x32_bf16 v[204:207], v[164:167], v[44:47], v[204:207]\n"
    "ds_read_b128 v[84:87], v2 offset:31744\n"
    "s_mov_b32 exec_lo, s51\n"
    "s_mov_b32 exec_hi, s51\n"
    "buffer_load_dwordx4 v[112:115], v8, %[rs], s41 offen offset:0 sc1\n"
    "s_mov_b64 exec, -1\n"
    "v_mfma_f32_16x16x32_bf16 v[200:203], v[160:163], v[44:47], v[200:203]\n"
    "v_mfma_f32_16x16x32_bf16 v[196:199], v[156:159], v[44:47], v[196:199]\n"
    "ds_read_b128 v[88:91], v2 offset:32768\n"
    "v_mfma_f32_16x16x32_bf16 v[192:195], v[152:155], v[44:47], v[192:195]\n"
    "buffer_load_dwordx4 v[44:47], v8, %[rs], s42 offen offset:1024 sc1\n"
    "v_mfma_f32_16x16x32_bf16 v[212:215], v[152:155], v[48:51], v[212:215]\n"
    "v_mfma_f32_16x16x32_bf16 v[216:219], v[156:159], v[48:51], v[216:219]\n"
    "s_waitcnt vmcnt(7)\n"
    "v_swap_b32 v57, v60\n"
    "v_mfma_f32_16x16x32_bf16 v[220:223], v[160:163], v[48:51], v[220:223]\n"
    "v_swap_b32 v58, v64\n"
    "v_mfma_f32_16x16x32_bf16 v[224:227], v[164:167], v[48:51], v[224:227]\n"
    "v_swap_b32 v59, v68\n"
    "v_mfma_f32_16x16x32_bf16 v[228:231], v[168:171], v[48:51], v[228:231]\n"
    "buffer_load_dwordx4 v[48:51], v8, %[rs], s42 offen offset:2048 sc1\n"
    "v_swap_b32 v62, v65\n"
    "v_mfma_f32_16x16x32_bf16 v[248:251], v[168:171], v[52:55], v[248:251]\n"
    "v_swap_b32 v63, v69\n"
    "v_mfma_f32_16x16x32_bf16 v[244:247], v[164:167], v[52:55], v[244:247]\n"
    "v_swap_b32 v67, v70\n"
    "v_mfma_f32_16x16x32_bf16 v[240:243], v[160:163], v[52:55], v[240:243]\n"
    "v_mfma_f32_16x16x32_bf16 v[236:239], v[156:159], v[52:55], v[236:239]\n"
    "v_mfma_f32_16x16x32_bf16 v[232:235], v[152:155], v[52:55], v[232:235]\n"
    "buffer_load_dwordx4 v[52:55], v8, %[rs], s42 offen offset:3072 sc1\n"
    "s_add_u32 s42, s42, 4096\n"
    "s_waitcnt lgkmcnt(0)\n"
    "v_mfma_f32_16x16x32_bf16 v[172:175], v[72:75], v[56:59], v[172:175]\n"
    "ds_read_b128 v[92:95], v2 offset:33792\n"
    "v_mfma_f32_16x16x32_bf16 v[176:179], v[76:79], v[56:59], v[176:179]\n"
    "v_mfma_f32_16x16x32_bf16 v[180:183], v[80:83], v[56:59], v[180:183]\n"
    "ds_read_b128 v[96:99], v2 offset:34816\n"
    "v_mfma_f32_16x16x32_bf16 v[184:187], v[84:87], v[56:59], v[184:187]\n"
    "v_mfma_f32_16x16x32_bf16 v[188:191], v[88:91], v[56:59], v[188:191]\n"
    "buffer_load_dwordx4 v[56:59], v8, %[rs], s42 offen offset:0 sc1\n"
    "ds_read_b128 v[100:103], v2 offset:57344\n"
    "v_mfma_f32_16x16x32_bf16 v[208:211], v[88:91], v[60:63], v[208:211]\n"
    "v_mfma_f32_16x16x32_bf16 v[204:207], v[84:87], v[60:63], v[204:207]\n"
    "ds_read_b128 v[104:107], v2 offset:58368\n"
    "s_mov_b32 exec_lo, s51\n"
    "s_mov_b32 exec_hi, s51\n"
    "buffer_load_dwordx4 v[116:119], v8, %[rs], s41 offen offset:1024 sc1\n"
    "s_mov_b64 exec, -1\n"
    "v_mfma_f32_16x16x32_bf16 v[200:203], v[80:83], v[60:63], v[200:203]\n"
    "v_mfma_f32_16x16x32_bf16 v[196:199], v[76:79], v[60:63], v[196:199]\n"
    "ds_read_b128 v[108:111], v2 offset:59392\n"
    "v_mfma_f32_16x16x32_bf16 v[192:195], v[72:75], v[60:63], v[192:195]\n"
    "buffer_load_dwordx4 v[60:63], v8, %[rs], s42 offen offset:1024 sc1\n"
    "v_mfma_f32_16x16x32_bf16 v[212:215], v[72:75], v[64:67], v[212:215]\n"
    "v_mfma_f32_16x16x32_bf16 v[216:219], v[76:79], v[64:67], v[216:219]\n"
    "s_waitcnt vmcnt(8)\n"
    "v_swap_b32 v25, v28\n"
    "v_mfma_f32_16x16x32_bf16 v[220:223], v[80:83], v[64:67], v[220:223]\n"
    "v_swap_b32 v26, v32\n"
    "v_mfma_f32_16x16x32_bf16 v[224:227], v[84:87], v[64:67], v[224:227]\n"
    "v_swap_b32 v27, v36\n"
    "v_mfma_f32_16x16x32_bf16 v[228:231], v[88:91], v[64:67], v[228:231]\n"
    "buffer_load_dwordx4 v[64:67], v8, %[rs], s42 offen offset:2048 sc1\n"
    "v_swap_b32 v30, v33\n"
    "v_mfma_f32_16x16x32_bf16 v[248:251], v[88:91], v[68:71], v[248:251]\n"
    "v_swap_b32 v31, v37\n"
    "v_mfma_f32_16x16x32_bf16 v[244:247], v[84:87], v[68:71], v[244:247]\n"
    "v_swap_b32 v35, v38\n"
    "v_mfma_f32_16x16x32_bf16 v[240:243], v[80:83], v[68:71], v[240:243]\n"
    "v_mfma_f32_16x16x32_bf16 v[236:239], v[76:79], v[68:71], v[236:239]\n"
    "v_mfma_f32_16x16x32_bf16 v[232:235], v[72:75], v[68:71], v[232:235]\n"
    "buffer_load_dwordx4 v[68:71], v8, %[rs], s42 offen offset:3072 sc1\n"
    "s_add_u32 s42, s42, 4096\n"
    "s_waitcnt lgkmcnt(0)\n"
    "v_mfma_f32_16x16x32_bf16 v[172:175], v[92:95], v[24:27], v[172:175]\n"
    "ds_read_b128 v[72:75], v2 offset:60416\n"
    "v_mfma_f32_16x16x32_bf16 v[176:179], v[96:99], v[24:27], v[176:179]\n"
    "v_mfma_f32_16x16x32_bf16 v[180:183], v[100:103], v[24:27], v[180:183]\n"
    "ds_read_b128 v[76:79], v2 offset:61440\n"
    "v_mfma_f32_16x16x32_bf16 v[184:187], v[104:107], v[24:27], v[184:187]\n"
    "v_mfma_f32_16x16x32_bf16 v[188:191], v[108:111], v[24:27], v[188:191]\n"
    "buffer_load_dwordx4 v[24:27], v8, %[rs], s42 offen offset:0 sc1\n"
    "ds_read_b128 v[80:83], v2 offset:62464\n"
    "v_mfma_f32_16x16x32_bf16 v[208:211], v[108:111], v[28:31], v[208:211]\n"
    "v_mfma_f32_16x16x32_bf16 v[204:207], v[104:107], v[28:31], v[204:207]\n"
    "ds_read_b128 v[84:87], v2 offset:63488\n"
    "s_mov_b32 exec_lo, s51\n"
    "s_mov_b32 exec_hi, s51\n"
    "buffer_load_dwordx4 v[120:123], v8, %[rs], s41 offen offset:2048 sc1\n"
    "s_mov_b64 exec, -1\n"
    "v_mfma_f32_16x16x32_bf16 v[200:203], v[100:103], v[28:31], v[200:203]\n"
    "v_mfma_f32_16x16x32_bf16 v[196:199], v[96:99], v[28:31], v[196:199]\n"
    "ds_read_b128 v[88:91], v2 offset:0\n"
    "v_mfma_f32_16x16x32_bf16 v[192:195], v[92:95], v[28:31], v[192:195]\n"
    "buffer_load_dwordx4 v[28:31], v8, %[rs], s42 offen offset:1024 sc1\n"
    "v_mfma_f32_16x16x32_bf16 v[212:215], v[92:95], v[32:35], v[212:215]\n"
    "v_mfma_f32_16x16x32_bf16 v[216:219], v[96:99], v[32:35], v[216:219]\n"
    "s_waitcnt vmcnt(8)\n"
    "v_swap_b32 v41, v44\n"
    "v_mfma_f32_16x16x32_bf16 v[220:223], v[100:103], v[32:35], v[220:223]\n"
    "v_swap_b32 v42, v48\n"
    "v_mfma_f32_16x16x32_bf16 v[224:227], v[104:107], v[32:35], v[224:227]\n"
    "v_swap_b32 v43, v52\n"
    "v_mfma_f32_16x16x32_bf16 v[228:231], v[108:111], v[32:35], v[228:231]\n"
    "buffer_load_dwordx4 v[32:35], v8, %[rs], s42 offen offset:2048 sc1\n"
    "v_swap_b32 v46, v49\n"
    "v_mfma_f32_16x16x32_bf16 v[248:251], v[108:111], v[36:39], v[248:251]\n"
    "v_swap_b32 v47, v53\n"
    "v_mfma_f32_16x16x32_bf16 v[244:247], v[104:107], v[36:39], v[244:247]\n"
    "v_swap_b32 v51, v54\n"
    "v_mfma_f32_16x16x32_bf16 v[240:243], v[100:103], v[36:39], v[240:243]\n"
    "v_mfma_f32_16x16x32_bf16 v[236:239], v[96:99], v[36:39], v[236:239]\n"
    "v_mfma_f32_16x16x32_bf16 v[232:235], v[92:95], v[36:39], v[232:235]\n"
    "buffer_load_dwordx4 v[36:39], v8, %[rs], s42 offen offset:3072 sc1\n"
    "s_add_u32 s42, s42, 4096\n"
    "s_waitcnt lgkmcnt(0)\n"
    "v_mfma_f32_16x16x32_bf16 v[172:175], v[72:75], v[40:43], v[172:175]\n"
    "ds_read_b128 v[92:95], v2 offset:1024\n"
    "v_mfma_f32_16x16x32_bf16 v[176:179], v[76:79], v[40:43], v[176:179]\n"
    "v_mfma_f32_16x16x32_bf16 v[180:183], v[80:83], v[40:43], v[180:183]\n"
    "ds_read_b128 v[96:99], v2 offset:2048\n"
    "v_mfma_f32_16x16x32_bf16 v[184:187], v[84:87], v[40:43], v[184:187]\n"
    "v_mfma_f32_16x16x32_bf16 v[188:191], v[88:91], v[40:43], v[188:191]\n"
    "buffer_load_dwordx4 v[40:43], v8, %[rs], s42 offen offset:0 sc1\n"
    "ds_read_b128 v[100:103], v2 offset:3072\n"
    "v_mfma_f32_16x16x32_bf16 v[208:211], v[88:91], v[44:47], v[208:211]\n"
    "v_mfma_f32_16x16x32_bf16 v[204:207], v[84:87], v[44:47], v[204:207]\n"
    "ds_read_b128 v[104:107], v2 offset:4096\n"
    "s_mov_b32 exec_lo, s51\n"
    "s_mov_b32 exec_hi, s51\n"
    "buffer_load_dwordx4 v[124:127], v8, %[rs], s41 offen offset:3072 sc1\n"
    "s_mov_b64 exec, -1\n"
    "v_mfma_f32_16x16x32_bf16 v[200:203], v[80:83], v[44:47], v[200:203]\n"
    "v_mfma_f32_16x16x32_bf16 v[196:199], v[76:79], v[44:47], v[196:199]\n"
    "ds_read_b128 v[108:111], v2 offset:5120\n"
    "v_mfma_f32_16x16x32_bf16 v[192:195], v[72:75], v[44:47], v[192:195]\n"
    "buffer_load_dwordx4 v[44:47], v8, %[rs], s42 offen offset:1024 sc1\n"
    "v_mfma_f32_16x16x32_bf16 v[212:215], v[72:75], v[48:51], v[212:215]\n"
    "v_mfma_f32_16x16x32_bf16 v[216:219], v[76:79], v[48:51], v[216:219]\n"
    "s_waitcnt vmcnt(8)\n"
    "v_swap_b32 v57, v60\n"
    "v_mfma_f32_16x16x32_bf16 v[220:223], v[80:83], v[48:51], v[220:223]\n"
    "v_swap_b32 v58, v64\n"
    "v_mfma_f32_16x16x32_bf16 v[224:227], v[84:87], v[48:51], v[224:227]\n"
    "v_swap_b32 v59, v68\n"
    "v_mfma_f32_16x16x32_bf16 v[228:231], v[88:91], v[48:51], v[228:231]\n"
    "buffer_load_dwordx4 v[48:51], v8, %[rs], s42 offen offset:2048 sc1\n"
    "v_swap_b32 v62, v65\n"
    "v_mfma_f32_16x16x32_bf16 v[248:251], v[88:91], v[52:55], v[248:251]\n"
    "v_swap_b32 v63, v69\n"
    "v_mfma_f32_16x16x32_bf16 v[244:247], v[84:87], v[52:55], v[244:247]\n"
    "v_swap_b32 v67, v70\n"
    "v_mfma_f32_16x16x32_bf16 v[240:243], v[80:83], v[52:55], v[240:243]\n"
    "v_mfma_f32_16x16x32_bf16 v[236:239], v[76:79], v[52:55], v[236:239]\n"
    "v_mfma_f32_16x16x32_bf16 v[232:235], v[72:75], v[52:55], v[232:235]\n"
    "buffer_load_dwordx4 v[52:55], v8, %[rs], s42 offen offset:3072 sc1\n"
    "s_add_u32 s42, s42, 4096\n"
    "s_waitcnt lgkmcnt(0)\n"
    "v_mfma_f32_16x16x32_bf16 v[172:175], v[92:95], v[56:59], v[172:175]\n"
    "ds_read_b128 v[72:75], v2 offset:6144\n"
    "v_mfma_f32_16x16x32_bf16 v[176:179], v[96:99], v[56:59], v[176:179]\n"
    "v_mfma_f32_16x16x32_bf16 v[180:183], v[100:103], v[56:59], v[180:183]\n"
    "ds_read_b128 v[76:79], v6 offset:0\n"
    "v_mfma_f32_16x16x32_bf16 v[184:187], v[104:107], v[56:59], v[184:187]\n"
    "v_mfma_f32_16x16x32_bf16 v[188:191], v[108:111], v[56:59], v[188:191]\n"
    "buffer_load_dwordx4 v[56:59], v8, %[rs], s42 offen offset:0 sc1\n"
    "ds_read_b128 v[80:83], v6 offset:1024\n"
    "v_mfma_f32_16x16x32_bf16 v[208:211], v[108:111], v[60:63], v[208:211]\n"
    "v_mfma_f32_16x16x32_bf16 v[204:207], v[104:107], v[60:63], v[204:207]\n"
    "ds_read_b128 v[84:87], v6 offset:2048\n"
    "s_mov_b32 exec_lo, s51\n"
    "s_mov_b32 exec_hi, s51\n"
    "buffer_load_dwordx4 v[128:131], v9, %[rs], s41 offen offset:0 sc1\n"
    "s_mov_b64 exec, -1\n"
    "v_mfma_f32_16x16x32_bf16 v[200:203], v[100:103], v[60:63], v[200:203]\n"
    "v_mfma_f32_16x16x32_bf16 v[196:199], v[96:99], v[60:63], v[196:199]\n"
    "ds_read_b128 v[88:91], v6 offset:3072\n"
    "v_mfma_f32_16x16x32_bf16 v[192:195], v[92:95], v[60:63], v[192:195]\n"
    "buffer_load_dwordx4 v[60:63], v8, %[rs], s42 offen offset:1024 sc1\n"
    "v_mfma_f32_16x16x32_bf16 v[212:215], v[92:95], v[64:67], v[212:215]\n"
    "v_mfma_f32_16x16x32_bf16 v[216:219], v[96:99], v[64:67], v[216:219]\n"
    "s_waitcnt vmcnt(8)\n"
    "v_swap_b32 v25, v28\n"
    "v_mfma_f32_16x16x32_bf16 v[220:223], v[100:103], v[64:67], v[220:223]\n"
    "v_swap_b32 v26, v32\n"
    "v_mfma_f32_16x16x32_bf16 v[224:227], v[104:107], v[64:67], v[224:227]\n"
    "v_swap_b32 v27, v36\n"
    "v_mfma_f32_16x16x32_bf16 v[228:231], v[108:111], v[64:67], v[228:231]\n"
    "buffer_load_dwordx4 v[64:67], v8, %[rs], s42 offen offset:2048 sc1\n"
    "v_swap_b32 v30, v33\n"
    "v_mfma_f32_16x16x32_bf16 v[248:251], v[108:111], v[68:71], v[248:251]\n"
    "v_swap_b32 v31, v37\n"
    "v_mfma_f32_16x16x32_bf16 v[244:247], v[104:107], v[68:71], v[244:247]\n"
    "v_swap_b32 v35, v38\n"
    "v_mfma_f32_16x16x32_bf16 v[240:243], v[100:103], v[68:71], v[240:243]\n"
    "v_mfma_f32_16x16x32_bf16 v[236:239], v[96:99], v[68:71], v[236:239]\n"
    "v_mfma_f32_16x16x32_bf16 v[232:235], v[92:95], v[68:71], v[232:235]\n"
    "buffer_load_dwordx4 v[68:71], v8, %[rs], s42 offen offset:3072 sc1\n"
    "s_add_u32 s42, s42, 8192\n"
    "s_waitcnt lgkmcnt(0)\n"
    "v_mfma_f32_16x16x32_bf16 v[172:175], v[72:75], v[24:27], v[172:175]\n"
    "ds_read_b128 v[92:95], v6 offset:4096\n"
    "v_mfma_f32_16x16x32_bf16 v[176:179], v[76:79], v[24:27], v[176:179]\n"
    "v_mfma_f32_16x16x32_bf16 v[180:183], v[80:83], v[24:27], v[180:183]\n"
    "ds_read_b128 v[96:99], v6 offset:5120\n"
    "v_mfma_f32_16x16x32_bf16 v[184:187], v[84:87], v[24:27], v[184:187]\n"
    "v_mfma_f32_16x16x32_bf16 v[188:191], v[88:91], v[24:27], v[188:191]\n"
    "s_mov_b32 exec_lo, s51\n"
    "s_mov_b32 exec_hi, s51\n"
    "buffer_load_dwordx4 v[24:27], v8, %[rs], s42 offen offset:0 sc1\n"
    "s_mov_b64 exec, -1\n"
    "ds_read_b128 v[100:103], v6 offset:6144\n"
    "v_mfma_f32_16x16x32_bf16 v[208:211], v[88:91], v[28:31], v[208:211]\n"
    "v_mfma_f32_16x16x32_bf16 v[204:207], v[84:87], v[28:31], v[204:207]\n"
    "ds_read_b128 v[104:107], v6 offset:7168\n"
    "v_mfma_f32_16x16x32_bf16 v[200:203], v[80:83], v[28:31], v[200:203]\n"
    "v_mfma_f32_16x16x32_bf16 v[196:199], v[76:79], v[28:31], v[196:199]\n"
    "ds_read_b128 v[108:111], v1 offset:0\n"
    "v_mfma_f32_16x16x32_bf16 v[192:195], v[72:75], v[28:31], v[192:195]\n"
    "s_mov_b32 exec_lo, s51\n"
    "s_mov_b32 exec_hi, s51\n"
    "buffer_load_dwordx4 v[28:31], v8, %[rs], s42 offen offset:1024 sc1\n"
    "s_mov_b64 exec, -1\n"
    "v_mfma_f32_16x16x32_bf16 v[212:215], v[72:75], v[32:35], v[212:215]\n"
    "v_mfma_f32_16x16x32_bf16 v[216:219], v[76:79], v[32:35], v[216:219]\n"
    "s_waitcnt vmcnt(7)\n"
    "v_swap_b32 v41, v44\n"
    "v_mfma_f32_16x16x32_bf16 v[220:223], v[80:83], v[32:35], v[220:223]\n"
    "v_swap_b32 v42, v48\n"
    "v_mfma_f32_16x16x32_bf16 v[224:227], v[84:87], v[32:35], v[224:227]\n"
    "v_swap_b32 v43, v52\n"
    "v_mfma_f32_16x16x32_bf16 v[228:231], v[88:91], v[32:35], v[228:231]\n"
    "s_mov_b32 exec_lo, s51\n"
    "s_mov_b32 exec_hi, s51\n"
    "buffer_load_dwordx4 v[32:35], v8, %[rs], s42 offen offset:2048 sc1\n"
    "s_mov_b64 exec, -1\n"
    "v_swap_b32 v46, v49\n"
    "v_mfma_f32_16x16x32_bf16 v[248:251], v[88:91], v[36:39], v[248:251]\n"
    "v_swap_b32 v47, v53\n"
    "v_mfma_f32_16x16x32_bf16 v[244:247], v[84:87], v[36:39], v[244:247]\n"
    "v_swap_b32 v51, v54\n"
    "v_mfma_f32_16x16x32_bf16 v[240:243], v[80:83], v[36:39], v[240:243]\n"
    "v_mfma_f32_16x16x32_bf16 v[236:239], v[76:79], v[36:39], v[236:239]\n"
    "v_mfma_f32_16x16x32_bf16 v[232:235], v[72:75], v[36:39], v[232:235]\n"
    "s_mov_b32 exec_lo, s51\n"
    "s_mov_b32 exec_hi, s51\n"
    "buffer_load_dwordx4 v[36:39], v8, %[rs], s42 offen offset:3072 sc1\n"
    "s_mov_b64 exec, -1\n"
    "s_add_u32 s42, s42, -4096\n"
    "s_waitcnt lgkmcnt(0)\n"
    "v_mfma_f32_16x16x32_bf16 v[172:175], v[92:95], v[40:43], v[172:175]\n"
    "ds_read_b128 v[72:75], v1 offset:1024\n"
    "v_mfma_f32_16x16x32_bf16 v[176:179], v[96:99], v[40:43], v[176:179]\n"
    "v_mfma_f32_16x16x32_bf16 v[180:183], v[100:103], v[40:43], v[180:183]\n"
    "ds_read_b128 v[76:79], v1 offset:2048\n"
    "v_mfma_f32_16x16x32_bf16 v[184:187], v[104:107], v[40:43], v[184:187]\n"
    "v_mfma_f32_16x16x32_bf16 v[188:191], v[108:111], v[40:43], v[188:191]\n"
    "buffer_load_dwordx4 v[40:43], v8, %[rs], s42 offen offset:0 sc1\n"
    "ds_read_b128 v[80:83], v0 offset:0\n"
    "v_mfma_f32_16x16x32_bf16 v[208:211], v[108:111], v[44:47], v[208:211]\n"
    "v_mfma_f32_16x16x32_bf16 v[204:207], v[104:107], v[44:47], v[204:207]\n"
    "ds_read_b128 v[84:87], v0 offset:1024\n"
    "v_mfma_f32_16x16x32_bf16 v[200:203], v[100:103], v[44:47], v[200:203]\n"
    "v_mfma_f32_16x16x32_bf16 v[196:199], v[96:99], v[44:47], v[196:199]\n"
    "ds_read_b128 v[88:91], v0 offset:2048\n"
    "v_mfma_f32_16x16x32_bf16 v[192:195], v[92:95], v[44:47], v[192:195]\n"
    "buffer_load_dwordx4 v[44:47], v8, %[rs], s42 offen offset:1024 sc1\n"
    "v_mfma_f32_16x16x32_bf16 v[212:215], v[92:95], v[48:51], v[212:215]\n"
    "v_mfma_f32_16x16x32_bf16 v[216:219], v[96:99], v[48:51], v[216:219]\n"
    "s_waitcnt vmcnt(6)\n"
    "v_swap_b32 v57, v60\n"
    "v_mfma_f32_16x16x32_bf16 v[220:223], v[100:103], v[48:51], v[220:223]\n"
    "v_swap_b32 v58, v64\n"
    "v_mfma_f32_16x16x32_bf16 v[224:227], v[104:107], v[48:51], v[224:227]\n"
    "v_swap_b32 v59, v68\n"
    "v_mfma_f32_16x16x32_bf16 v[228:231], v[108:111], v[48:51], v[228:231]\n"
    "buffer_load_dwordx4 v[48:51], v8, %[rs], s42 offen offset:2048 sc1\n"
    "v_swap_b32 v62, v65\n"
    "v_mfma_f32_16x16x32_bf16 v[248:251], v[108:111], v[52:55], v[248:251]\n"
    "v_swap_b32 v63, v69\n"
    "v_mfma_f32_16x16x32_bf16 v[244:247], v[104:107], v[52:55], v[244:247]\n"
    "v_swap_b32 v67, v70\n"
    "v_mfma_f32_16x16x32_bf16 v[240:243], v[100:103], v[52:55], v[240:243]\n"
    "v_mfma_f32_16x16x32_bf16 v[236:239], v[96:99], v[52:55], v[236:239]\n"
    "v_mfma_f32_16x16x32_bf16 v[232:235], v[92:95], v[52:55], v[232:235]\n"
    "buffer_load_dwordx4 v[52:55], v8, %[rs], s42 offen offset:3072 sc1\n"
    "s_waitcnt lgkmcnt(0)\n"
    "v_mfma_f32_16x16x32_bf16 v[172:175], v[72:75], v[56:59], v[172:175]\n"
    "ds_read_b128 v[92:95], v1 offset:3072\n"
    "v_mfma_f32_16x16x32_bf16 v[176:179], v[76:79], v[56:59], v[176:179]\n"
    "v_mfma_f32_16x16x32_bf16 v[180:183], v[80:83], v[56:59], v[180:183]\n"
    "ds_read_b128 v[96:99], v1 offset:4096\n"
    "v_mfma_f32_16x16x32_bf16 v[184:187], v[84:87], v[56:59], v[184:187]\n"
    "v_mfma_f32_16x16x32_bf16 v[188:191], v[88:91], v[56:59], v[188:191]\n"
    "ds_read_b128 v[100:103], v1 offset:5120\n"
    "v_mfma_f32_16x16x32_bf16 v[208:211], v[88:91], v[60:63], v[208:211]\n"
    "v_mfma_f32_16x16x32_bf16 v[204:207], v[84:87], v[60:63], v[204:207]\n"
    "ds_read_b128 v[104:107], v1 offset:6144\n"
    "v_mfma_f32_16x16x32_bf16 v[200:203], v[80:83], v[60:63], v[200:203]\n"
    "v_mfma_f32_16x16x32_bf16 v[196:199], v[76:79], v[60:63], v[196:199]\n"
    "v_mfma_f32_16x16x32_bf16 v[192:195], v[72:75], v[60:63], v[192:195]\n"
    "v_mfma_f32_16x16x32_bf16 v[212:215], v[72:75], v[64:67], v[212:215]\n"
    "v_mfma_f32_16x16x32_bf16 v[216:219], v[76:79], v[64:67], v[216:219]\n"
    "s_waitcnt vmcnt(4)\n"
    "v_swap_b32 v25, v28\n"
    "v_mfma_f32_16x16x32_bf16 v[220:223], v[80:83], v[64:67], v[220:223]\n"
    "v_swap_b32 v26, v32\n"
    "v_mfma_f32_16x16x32_bf16 v[224:227], v[84:87], v[64:67], v[224:227]\n"
    "v_swap_b32 v27, v36\n"
    "v_mfma_f32_16x16x32_bf16 v[228:231], v[88:91], v[64:67], v[228:231]\n"
    "v_swap_b32 v30, v33\n"
    "v_mfma_f32_16x16x32_bf16 v[248:251], v[88:91], v[68:71], v[248:251]\n"
    "v_swap_b32 v31, v37\n"
    "v_mfma_f32_16x16x32_bf16 v[244:247], v[84:87], v[68:71], v[244:247]\n"
    "v_swap_b32 v35, v38\n"
    "v_mfma_f32_16x16x32_bf16 v[240:243], v[80:83], v[68:71], v[240:243]\n"
    "v_mfma_f32_16x16x32_bf16 v[236:239], v[76:79], v[68:71], v[236:239]\n"
    "s_cmp_eq_u32 s51, 0\n"
    "v_mfma_f32_16x16x32_bf16 v[232:235], v[72:75], v[68:71], v[232:235]\n"
    "s_cbranch_scc0 7f\n"
    "s_waitcnt vmcnt(0)\n"
    "v_swap_b32 v41, v44\n"
    "v_swap_b32 v42, v48\n"
    "v_swap_b32 v43, v52\n"
    "v_swap_b32 v46, v49\n"
    "v_swap_b32 v47, v53\n"
    "v_swap_b32 v51, v54\n"
    "s_nop 0\n"
    "s_branch 8f\n"
    "7:\n"
    "s_waitcnt vmcnt(11)\n"
    "v_mfma_f32_16x16x32_bf16 v[172:175], v[112:115], v[24:27], v[172:175]\n"
    "v_mfma_f32_16x16x32_bf16 v[176:179], v[116:119], v[24:27], v[176:179]\n"
    "v_mfma_f32_16x16x32_bf16 v[180:183], v[120:123], v[24:27], v[180:183]\n"
    "v_mfma_f32_16x16x32_bf16 v[184:187], v[124:127], v[24:27], v[184:187]\n"
    "v_mfma_f32_16x16x32_bf16 v[188:191], v[128:131], v[24:27], v[188:191]\n"
    "v_mfma_f32_16x16x32_bf16 v[208:211], v[128:131], v[28:31], v[208:211]\n"
    "v_mfma_f32_16x16x32_bf16 v[204:207], v[124:127], v[28:31], v[204:207]\n"
    "v_mfma_f32_16x16x32_bf16 v[200:203], v[120:123], v[28:31], v[200:203]\n"
    "v_mfma_f32_16x16x32_bf16 v[196:199], v[116:119], v[28:31], v[196:199]\n"
    "v_mfma_f32_16x16x32_bf16 v[192:195], v[112:115], v[28:31], v[192:195]\n"
    "v_mfma_f32_16x16x32_bf16 v[212:215], v[112:115], v[32:35], v[212:215]\n"
    "v_mfma_f32_16x16x32_bf16 v[216:219], v[116:119], v[32:35], v[216:219]\n"
    "s_waitcnt vmcnt(0)\n"
    "v_swap_b32 v41, v44\n"
    "v_mfma_f32_16x16x32_bf16 v[220:223], v[120:123], v[32:35], v[220:223]\n"
    "v_swap_b32 v42, v48\n"
    "v_mfma_f32_16x16x32_bf16 v[224:227], v[124:127], v[32:35], v[224:227]\n"
    "v_swap_b32 v43, v52\n"
    "v_mfma_f32_16x16x32_bf16 v[228:231], v[128:131], v[32:35], v[228:231]\n"
    "v_swap_b32 v46, v49\n"
    "v_mfma_f32_16x16x32_bf16 v[248:251], v[128:131], v[36:39], v[248:251]\n"
    "v_swap_b32 v47, v53\n"
    "v_mfma_f32_16x16x32_bf16 v[244:247], v[124:127], v[36:39], v[244:247]\n"
    "v_swap_b32 v51, v54\n"
    "v_mfma_f32_16x16x32_bf16 v[240:243], v[120:123], v[36:39], v[240:243]\n"
    "v_mfma_f32_16x16x32_bf16 v[236:239], v[116:119], v[36:39], v[236:239]\n"
    "v_mfma_f32_16x16x32_bf16 v[232:235], v[112:115], v[36:39], v[232:235]\n"
    "8:\n"
    "s_waitcnt lgkmcnt(0)\n"
    "v_mfma_f32_16x16x32_bf16 v[192:195], v[92:95], v[44:47], v[192:195]\n"
    "v_mfma_f32_16x16x32_bf16 v[196:199], v[96:99], v[44:47], v[196:199]\n"
    "v_mfma_f32_16x16x32_bf16 v[200:203], v[100:103], v[44:47], v[200:203]\n"
    "v_mfma_f32_16x16x32_bf16 v[204:207], v[104:107], v[44:47], v[204:207]\n"
    "v_mfma_f32_16x16x32_bf16 v[208:211], v[16:19], v[44:47], v[208:211]\n"
    "v_mfma_f32_16x16x32_bf16 v[212:215], v[92:95], v[48:51], v[212:215]\n"
    "v_mfma_f32_16x16x32_bf16 v[216:219], v[96:99], v[48:51], v[216:219]\n"
    "v_mfma_f32_16x16x32_bf16 v[220:223], v[100:103], v[48:51], v[220:223]\n"
    "v_mfma_f32_16x16x32_bf16 v[224:227], v[104:107], v[48:51], v[224:227]\n"
    "v_mfma_f32_16x16x32_bf16 v[228:231], v[16:19], v[48:51], v[228:231]\n"
    "ds_write_b128 v2, v[192:195] offset:0\n"
    "v_mfma_f32_16x16x32_bf16 v[232:235], v[92:95], v[52:55], v[232:235]\n"
    "ds_write_b128 v2, v[196:199] offset:1024\n"
    "v_mfma_f32_16x16x32_bf16 v[236:239], v[96:99], v[52:55], v[236:239]\n"
    "ds_write_b128 v2, v[200:203] offset:2048\n"
    "v_mfma_f32_16x16x32_bf16 v[240:243], v[100:103], v[52:55], v[240:243]\n"
    "ds_write_b128 v2, v[204:207] offset:3072\n"
    "v_mfma_f32_16x16x32_bf16 v[244:247], v[104:107], v[52:55], v[244:247]\n"
    "ds_write_b128 v2, v[208:211] offset:4096\n"
    "v_mfma_f32_16x16x32_bf16 v[248:251], v[16:19], v[52:55], v[248:251]\n"
    "ds_write_b128 v2, v[212:215] offset:28672\n"
    "v_mfma_f32_16x16x32_bf16 v[172:175], v[92:95], v[40:43], v[172:175]\n"
    "ds_write_b128 v2, v[216:219] offset:29696\n"
    "v_mfma_f32_16x16x32_bf16 v[176:179], v[96:99], v[40:43], v[176:179]\n"
    "ds_write_b128 v2, v[220:223] offset:30720\n"
    "v_mfma_f32_16x16x32_bf16 v[180:183], v[100:103], v[40:43], v[180:183]\n"
    "ds_write_b128 v2, v[224:227] offset:31744\n"
    "v_mfma_f32_16x16x32_bf16 v[184:187], v[104:107], v[40:43], v[184:187]\n"
    "ds_write_b128 v2, v[228:231] offset:32768\n"
    "v_mfma_f32_16x16x32_bf16 v[188:191], v[16:19], v[40:43], v[188:191]\n"
    "ds_write_b128 v2, v[232:235] offset:57344\n"
    "ds_write_b128 v2, v[236:239] offset:58368\n"
    "ds_write_b128 v2, v[240:243] offset:59392\n"
    "ds_write_b128 v2, v[244:247] offset:60416\n"
    "ds_write_b128 v2, v[248:251] offset:61440\n"
    "s_waitcnt lgkmcnt(0)\n"
    "s_barrier\n"
    "ds_read_b128 v[24:27], v3 offset:0\n"
    "ds_read_b128 v[44:47], v4 offset:28672\n"
    "ds_read_b128 v[64:67], v5 offset:57344\n"
    "ds_read_b128 v[28:31], v3 offset:1024\n"
    "ds_read_b128 v[48:51], v4 offset:29696\n"
    "ds_read_b128 v[68:71], v5 offset:58368\n"
    "ds_read_b128 v[32:35], v3 offset:2048\n"
    "ds_read_b128 v[52:55], v4 offset:30720\n"
    "ds_read_b128 v[72:75], v5 offset:59392\n"
    "ds_read_b128 v[36:39], v3 offset:3072\n"
    "ds_read_b128 v[56:59], v4 offset:31744\n"
    "ds_read_b128 v[76:79], v5 offset:60416\n"
    "ds_read_b128 v[40:43], v3 offset:4096\n"
    "ds_read_b128 v[60:63], v4 offset:32768\n"
    "ds_read_b128 v[80:83], v5 offset:61440\n"
    "s_waitcnt lgkmcnt(12)\n"
    "v_pk_add_f32 v[24:25], v[24:25], v[44:45]\n"
    "v_pk_add_f32 v[172:173], v[172:173], v[64:65]\n"
    "v_pk_add_f32 v[26:27], v[26:27], v[46:47]\n"
    "v_pk_add_f32 v[174:175], v[174:175], v[66:67]\n"
    "v_pk_add_f32 v[172:173], v[172:173], v[24:25]\n"
    "v_pk_add_f32 v[174:175], v[174:175], v[26:27]\n"
    "v_cvt_pk_bf16_f32 v192, v172, v173\n"
    "v_cvt_pk_bf16_f32 v193, v174, v175\n"
    "s_waitcnt lgkmcnt(9)\n"
    "v_pk_add_f32 v[28:29], v[28:29], v[48:49]\n"
    "v_pk_add_f32 v[176:177], v[176:177], v[68:69]\n"
    "v_pk_add_f32 v[30:31], v[30:31], v[50:51]\n"
    "v_pk_add_f32 v[178:179], v[178:179], v[70:71]\n"
    "v_pk_add_f32 v[176:177], v[176:177], v[28:29]\n"
    "v_pk_add_f32 v[178:179], v[178:179], v[30:31]\n"
    "v_cvt_pk_bf16_f32 v194, v176, v177\n"
    "v_cvt_pk_bf16_f32 v195, v178, v179\n"
    "global_store_dwordx4 v14, v[192:195], %[out] offset:0 sc1\n"
    "s_waitcnt lgkmcnt(6)\n"
    "v_pk_add_f32 v[32:33], v[32:33], v[52:53]\n"
    "v_pk_add_f32 v[180:181], v[180:181], v[72:73]\n"
    "v_pk_add_f32 v[34:35], v[34:35], v[54:55]\n"
    "v_pk_add_f32 v[182:183], v[182:183], v[74:75]\n"
    "v_pk_add_f32 v[180:181], v[180:181], v[32:33]\n"
    "v_pk_add_f32 v[182:183], v[182:183], v[34:35]\n"
    "v_cvt_pk_bf16_f32 v196, v180, v181\n"
    "v_cvt_pk_bf16_f32 v197, v182, v183\n"
    "s_waitcnt lgkmcnt(3)\n"
    "v_pk_add_f32 v[36:37], v[36:37], v[56:57]\n"
    "v_pk_add_f32 v[184:185], v[184:185], v[76:77]\n"
    "v_pk_add_f32 v[38:39], v[38:39], v[58:59]\n"
    "v_pk_add_f32 v[186:187], v[186:187], v[78:79]\n"
    "v_pk_add_f32 v[184:185], v[184:185], v[36:37]\n"
    "v_pk_add_f32 v[186:187], v[186:187], v[38:39]\n"
    "v_cvt_pk_bf16_f32 v198, v184, v185\n"
    "v_cvt_pk_bf16_f32 v199, v186, v187\n"
    "s_waitcnt lgkmcnt(0)\n"
    "v_pk_add_f32 v[40:41], v[40:41], v[60:61]\n"
    "v_pk_add_f32 v[188:189], v[188:189], v[80:81]\n"
    "v_pk_add_f32 v[42:43], v[42:43], v[62:63]\n"
    "v_pk_add_f32 v[190:191], v[190:191], v[82:83]\n"
    "v_pk_add_f32 v[188:189], v[188:189], v[40:41]\n"
    "v_pk_add_f32 v[190:191], v[190:191], v[42:43]\n"
    "v_cvt_pk_bf16_f32 v200, v188, v189\n"
    "v_cvt_pk_bf16_f32 v201, v190, v191\n"
    "global_store_dwordx4 v14, v[196:199], %[out] offset:64 sc1\n"
    "v_permlane32_swap_b32 v200, v202\n"
    "s_nop 0\n"
    "v_permlane32_swap_b32 v201, v203\n"
    "s_mov_b32 exec_hi, 0\n"
    "global_store_dwordx4 v14, v[200:203], %[out] offset:128 sc1\n"
    "s_mov_b32 exec_hi, -1\n"
    : [cnt] "=&s"(cnt)
    : [rs] "s"(rawrsrc(ws, WS_END)), [fl] "s"(ws + WS_FL), [out] "s"(out), "{v[8:15]}"(v), "{s[36:51]}"(s),
      "{v[132:163]}"(st.a), "{v[164:195]}"(st.b), "{v[196:227]}"(st.c), "{v[228:243]}"(st.d), "{v[244:251]}"(st.e)
    : "vcc", "scc", "memory", "v0", "v1", "v2", "v3", "v4", "v5", "v6", "v7", "v16", "v17", "v18", "v19", "v24", "v25", "v26", "v27", "v28", "v29", "v30", "v31", "v32", "v33", "v34", "v35", "v36", "v37", "v38", "v39", "v40", "v41", "v42", "v43", "v44", "v45", "v46", "v47", "v48", "v49", "v50", "v51", "v52", "v53", "v54", "v55", "v56", "v57", "v58", "v59", "v60", "v61", "v62", "v63", "v64", "v65", "v66", "v67", "v68", "v69", "v70", "v71", "v72", "v73", "v74", "v75", "v76", "v77", "v78", "v79", "v80", "v81", "v82", "v83", "v84", "v85", "v86", "v87", "v88", "v89", "v90", "v91", "v92", "v93", "v94", "v95", "v96", "v97", "v98", "v99", "v100", "v101", "v102", "v103", "v104", "v105", "v106", "v107", "v108", "v109", "v110", "v111", "v112", "v113", "v114", "v115", "v116", "v117", "v118", "v119", "v120", "v121", "v122", "v123", "v124", "v125", "v126", "v127", "v128", "v129", "v130", "v131", "v252", "v253", "v254", "v255");
}

DEV void code_touch(int wv, int lane) {
  unsigned t;
  asm volatile("s_getpc_b64 s[72:73]\n1:\n\ts_sub_u32 s72, s72, 1b-k_block\n\ts_subb_u32 s73, s73, 0\n\ts_mov_b32 s74, .Lkb_end0-k_block-4\n\ts_max_u32 s74, s74, .Lkb_end1-k_block-4\n\t"
               "v_min_u32 %0, s74, %1\n\tglobal_load_dword %0, %0, s[72:73]\n\ts_waitcnt vmcnt(0)"
               : "=v"(t) : "v"(wv * 8192 + lane * 128) : "s72", "s73", "s74", "scc", "memory");
}
extern "C" __global__ __launch_bounds__(512) void k_block(const char* __restrict__ x, const char* __restrict__ wqkv, const char* __restrict__ wo, const char* __restrict__ wg,
                                                          const char* __restrict__ wu, const char* __restrict__ wd, char* __restrict__ ws, char* __restrict__ out) {
  u32x4 rx, rw;
  u64 rs;
  asm volatile("s_load_dwordx2 s[84:85], %3, 0\n\ts_load_dwordx2 s[88:89], %3, 8\n\ts_load_dwordx2 s[92:93], %3, 48\n\ts_mov_b32 s86, %4\n\ts_mov_b32 s87, 0x20000\n\t"
               "s_mov_b32 s90, %5\n\ts_mov_b32 s91, 0x20000"
               : "={s[84:87]}"(rx), "={s[88:91]}"(rw), "={s[92:93]}"(rs) : "s"(__builtin_amdgcn_kernarg_segment_ptr()), "n"(S_ * D_ * 2), "n"(3 * D_ * D_ * 2));
#define ARGS_DONE() asm volatile("s_waitcnt lgkmcnt(0)" :: "{s[84:87]}"(rx), "{s[88:91]}"(rw), "{s[92:93]}"(rs) : "memory")
  const int wv = __builtin_amdgcn_readfirstlane(threadIdx.x >> 6);
  const int b = blockIdx.x, xc = b & 7, j = b >> 3;
  const int u1 = (xc & 3) * 30 + j, cb1 = (0xe49d83c271b6a05ull >> ((u1 >> 3) * 4)) & 15, rb1 = (u1 & 7) * 2 + (xc >> 2);
  const int head = xc * 4 + (j & 3), tb = (xc >> 1) * 4 + (j >> 3), fb = (xc & 1) * 8 + (j & 7);
  if (wv < 4) {
    const int lane = lane_id();
    if (wv == 0 && lane < 8) lds_set(L_FLG + lane * 4, 0);
    ARGS_DONE();
    P1Out p;
    if (j >= 30) {
      if (PFC && j == 30) code_touch(wv, lane);
      asm volatile("" : "={v[252:253]}"(p.s));
      p.sm = 0;
      BAR();
    }
    else if (cb1 < 10) p1_compute<true>(ws, rb1, cb1, wv, lane_id(), p);
    else p1_compute<false>(ws, rb1, cb1, wv, lane_id(), p);
    Stash st;
    const u32x16 s5 = p5_setup(xc * 4 + (j >> 3), j & 7, tb, fb, wv);
    p2_compute(ws, head, j >> 2, j & 7, wv, lane_id(), p, st);
    p5_compute(ws, out, head, j >> 2, xc * 4 + (j >> 3), j & 7, tb, s5, wv, lane_id(), st);
    asm volatile(".Lkb_end0:");
  } else {
    const int hw = wv - 4, lane = lane_id(), id = (b * 4 + hw) & 63;
    if (__builtin_expect(j < 30, 1)) p1_loader(rx, rw, rs, rb1, cb1, (5 * (((u1 >> 3) + u1) & 1) + (xc & 3) + (xc >> 1 & 1) + 2 * (xc >> 2)) % 10, hw, id, lane);
    else {
      if (PFC && j == 30) code_touch(wv, lane);
      ARGS_DONE();
      if (b == 255 && hw == 3) hb_lead(ws, lane); else hb_check(ws + WS_FL, id, lane);
    }
    BAR();
    __builtin_amdgcn_s_setprio(HP);
    P3In p;
    __builtin_amdgcn_s_sleep(SL0);
    p3_setup(p, xc * 4 + (j >> 3), j & 7, fb, hw, lane);
    if (hw == 0) p2_kvload(ws, head, j >> 2, p.v, lane);
    else if (hw == 3) {
      unsigned a = WS_O + ((j >> 2) * 16 + (lane >> 2)) * 20480 + (head >> 1) * 1280 + 512 + (lane & 3) * 64 + (head & 1) * 8;
      asm volatile("" : "+v"(a));
      WAIT_KV();
      if (CLM) st4(mkrsrc(ws, WS_END), a, 0, a);
    } else if (j >= 27) { if (hw == 1) retile_rows<4>(wo + (j - 27) * 128 * 1280 + xc * 160, ws, WS_WO + (xc * 20 + 4 * (j - 27)) * 5120, L_STG, C_WO + xc, lane); }
    else if (hw == 1) retile_gu(wg, wu, ws, xc * 27 + j, lane);
    else retile_rows<2>(wd + j * 64 * 1280 + xc * 160, ws, WS_WD + (xc * 54 + 2 * j) * 5120, L_STG + 20480, C_WD + xc, lane);
    p3_compute(p, x, ws, xc * 4 + (j >> 3), j & 7, fb, hw);
    p4_compute(ws, tb, fb, hw, lane_id());
    asm volatile(".Lkb_end1:");
  }
}

constexpr int WS_BASE = 0;
extern "C" void kernel_launch(void* const* d_in, const int* in_sizes, int n_in, void* d_out, int out_size, void* d_ws, size_t ws_size,
                              hipStream_t stream) {
  (void)hipFuncSetAttribute((const void*)k_block, hipFuncAttributeMaxDynamicSharedMemorySize, L_TOTAL);
  k_block<<<256, 512, L_TOTAL, stream>>>((const char*)d_in[0], (const char*)d_in[1], (const char*)d_in[2], (const char*)d_in[4], (const char*)d_in[5],
                                         (const char*)d_in[6], (char*)d_ws + WS_BASE, (char*)d_out);
}
```
